```python
import torch

def _softmax(x, axis=-1):
    return torch.softmax(x, dim=axis)
def _gelu(x):
    return torch.nn.functional.gelu(x, approximate="tanh")
def _silu(x):
    return torch.nn.functional.silu(x)
def _sigmoid(x):
    return torch.sigmoid(x)
def _layernorm(x, axis=-1, eps=1e-5):
    m = x.mean(dim=axis, keepdim=True)
    v = x.var(dim=axis, keepdim=True, unbiased=False)
    return (x - m) / torch.sqrt(v + eps)
def _scatter_add(tgt, idx, src, axis=0):
    return tgt.clone().index_add_(axis, idx, src)
def _segment_sum(src, seg, n):
    out = torch.zeros((n,) + src.shape[1:], dtype=src.dtype)
    return out.index_add_(0, seg, src)
def _pad(x, pw):
    flat = tuple(v for pair in reversed(pw) for v in pair)
    return torch.nn.functional.pad(x, flat)

kernel_name = 'recurrent_scan__sliding_block_cached__dense_activation_gelu__prompt'

def setup_inputs():

    _w = (lambda a: (lambda t: t.to(torch.bfloat16) if t.is_floating_point() else t)(torch.as_tensor(a)))
    def _A_setup():
        B, Sq, D = 1, 2048, 1024
        Hq, Hkv, Sk, Dh = 32, 4, 2048, 128
        assert Hq % Hkv == 0, (Hq, Hkv)
        return {
            "x": _w((lambda *s: torch.randn(*s, dtype=torch.bfloat16) * 0.05)(B, Sq, D)),
            "k_cache": _w((lambda *s: torch.randn(*s, dtype=torch.bfloat16) * 1.6)(B, Hkv, Sk, Dh)),
            "v_cache": _w((lambda *s: torch.randn(*s, dtype=torch.bfloat16) * 0.8)(B, Hkv, Sk, Dh)),
            "wq": _w((lambda *s: torch.randn(*s, dtype=torch.bfloat16) * 1.6)(D, Hq * Dh) * (D ** -0.5)),
            "wo": _w((lambda *s: torch.randn(*s, dtype=torch.bfloat16) * 0.8)(Hq * Dh, D) * ((Hq * Dh) ** -0.5)),
        }
    _A_in = _A_setup()

    def _B_setup():
        B, S, D, F = 1, 2048, 1024, 3584
        return {
            "x": _w((lambda *s: torch.randn(*s, dtype=torch.bfloat16) * 0.05)(B, S, D)),
            "w1": _w((lambda *s: torch.randn(*s, dtype=torch.bfloat16) * 1.6)(D, F) * (D ** -0.5)),
            "b1": _w((lambda *s: torch.randn(*s, dtype=torch.bfloat16) * 1.6)(F) * 0.01),
            "w2": _w((lambda *s: torch.randn(*s, dtype=torch.bfloat16) * 0.2)(F, D) * (F ** -0.5)),
            "b2": _w((lambda *s: torch.randn(*s, dtype=torch.bfloat16) * 0.2)(D) * 0.01),
        }
    _B_in = _B_setup()

    return {
        "A_x": _A_in["x"],
        "A_k_cache": _A_in["k_cache"],
        "A_v_cache": _A_in["v_cache"],
        "A_wq": _A_in["wq"],
        "A_wo": _A_in["wo"],
        "B_x": _B_in["x"],
        "B_w1": _B_in["w1"],
        "B_b1": _B_in["b1"],
        "B_w2": _B_in["w2"],
        "B_b2": _B_in["b2"],
    }

def reference(A_x, A_k_cache, A_v_cache, A_wq, A_wo, B_x, B_w1, B_b1, B_w2, B_b2):
    def _A_ref(x, k_cache, v_cache, wq, wo):
        B, Sq, D = x.shape
        Hq, Dh = 32, 128
        Hkv, Sk = k_cache.shape[1], k_cache.shape[2]
        W = min(4096, max(256, Sk // 4))
        rep = Hq // Hkv
        q = (lambda x, *d: x.permute(*d))((x @ wq).reshape(B, Sq, Hq, Dh), 0, 2, 1, 3)
        k = (lambda x, n, axis: torch.repeat_interleave(x, n, dim=axis))(k_cache[:, :, Sk - W:, :], rep, axis=1)
        v = (lambda x, n, axis: torch.repeat_interleave(x, n, dim=axis))(v_cache[:, :, Sk - W:, :], rep, axis=1)
        scores = torch.einsum("bhqd,bhkd->bhqk", q, k) * (Dh ** -0.5)
        p = _softmax(scores, axis=-1)
        o = torch.einsum("bhqk,bhkd->bhqd", p, v)
        return (lambda x, *d: x.permute(*d))(o, 0, 2, 1, 3).reshape(B, Sq, Hq * Dh) @ wo

    def _B_ref(x, w1, b1, w2, b2):
        h = torch.einsum("bsd,df->bsf", x, w1) + b1
        h = _gelu(h)
        return torch.einsum("bsf,fd->bsd", h, w2) + b2

    x = A_x
    xn = _layernorm(x, axis=-1)
    s = _A_ref(xn, A_k_cache, A_v_cache, A_wq, A_wo)
    g = _silu(_B_ref(xn, B_w1, B_b1, B_w2, B_b2))
    return x + s * g
```

```cpp
#include <hip/hip_runtime.h>
#include <hip/hip_bf16.h>
typedef __bf16 bf16;
typedef __bf16 bf16x8 __attribute__((ext_vector_type(8)));
typedef float f32x16 __attribute__((ext_vector_type(16)));
__device__ __forceinline__ float b2f(bf16 x) { return (float)x; }
__device__ __forceinline__ bf16 f2b(float x) { return (bf16)x; }

__global__ void k_ln(const bf16* x, bf16* xn) {
  int row = blockIdx.x; int t = threadIdx.x;
  float v[4]; float s = 0;
  for (int i = 0; i < 4; ++i) { v[i] = b2f(x[row * 1024 + t * 4 + i]); s += v[i]; }
  __shared__ float red[256];
  red[t] = s; __syncthreads();
  for (int o = 128; o > 0; o >>= 1) { if (t < o) red[t] += red[t + o]; __syncthreads(); }
  float mean = red[0] / 1024.f; __syncthreads();
  float q = 0; for (int i = 0; i < 4; ++i) { float d = v[i] - mean; q += d * d; }
  red[t] = q; __syncthreads();
  for (int o = 128; o > 0; o >>= 1) { if (t < o) red[t] += red[t + o]; __syncthreads(); }
  float var = red[0] / 1024.f;
  float rs = 1.0f / sqrtf(var + 1e-5f);
  for (int i = 0; i < 4; ++i) xn[row * 1024 + t * 4 + i] = f2b((v[i] - mean) * rs);
}
template<int EPI>
__global__ void k_gemm(const bf16* A, const bf16* B, const bf16* bias, bf16* C, int M, int N, int K) {
  int wave = threadIdx.x >> 6, lane = threadIdx.x & 63, r = lane & 31, h = lane >> 5;
  int m0 = blockIdx.y * 128 + wave * 32, n0 = blockIdx.x * 32;
  f32x16 acc = {};
  for (int k = 0; k < K; k += 16) {
    bf16x8 a = *(const bf16x8*)(A + (size_t)(m0 + r) * K + k + 8 * h);
    bf16x8 b;
    for (int j = 0; j < 8; ++j) b[j] = B[(size_t)(k + 8 * h + j) * N + n0 + r];
    acc = __builtin_amdgcn_mfma_f32_32x32x16_bf16(a, b, acc, 0, 0, 0);
  }
  for (int i = 0; i < 16; ++i) {
    int row = m0 + (i & 3) + 8 * (i >> 2) + 4 * h, col = n0 + r;
    float v = acc[i];
    if (EPI >= 1) v += b2f(bias[col]);
    if (EPI == 1) { float u = 0.7978845608f * (v + 0.044715f * v * v * v); v = 0.5f * v * (1.f + tanhf(u)); }
    C[(size_t)row * N + col] = f2b(v);
  }
}
__global__ void k_attn(const bf16* q, const bf16* kc, const bf16* vc, bf16* o) {
  int gw = blockIdx.x * 4 + (threadIdx.x >> 6), lane = threadIdx.x & 63;
  int row = gw >> 5, hd = gw & 31, kv = hd >> 3;
  const bf16* qp = q + (size_t)row * 4096 + hd * 128;
  const bf16* kp = kc + ((size_t)kv * 2048 + 1536) * 128;
  const bf16* vp = vc + ((size_t)kv * 2048 + 1536) * 128;
  float s[8]; float mx = -1e30f;
  for (int i = 0; i < 8; ++i) { int key = lane + 64 * i; float a = 0; for (int d = 0; d < 128; ++d) a += b2f(qp[d]) * b2f(kp[key * 128 + d]); s[i] = a * 0.08838834764f; mx = fmaxf(mx, s[i]); }
  for (int o2 = 32; o2 > 0; o2 >>= 1) mx = fmaxf(mx, __shfl_xor(mx, o2));
  float sum = 0; for (int i = 0; i < 8; ++i) { s[i] = __expf(s[i] - mx); sum += s[i]; }
  for (int o2 = 32; o2 > 0; o2 >>= 1) sum += __shfl_xor(sum, o2);
  float inv = 1.f / sum;
  __shared__ float p[4][512];
  int w = threadIdx.x >> 6;
  for (int i = 0; i < 8; ++i) p[w][lane + 64 * i] = s[i] * inv;
  __syncthreads();
  float o0 = 0, o1 = 0;
  for (int key = 0; key < 512; ++key) { float pp = p[w][key]; o0 += pp * b2f(vp[key * 128 + lane * 2]); o1 += pp * b2f(vp[key * 128 + lane * 2 + 1]); }
  o[(size_t)row * 4096 + hd * 128 + lane * 2] = f2b(o0); o[(size_t)row * 4096 + hd * 128 + lane * 2 + 1] = f2b(o1);
}
__global__ void k_fin(const bf16* x, const bf16* s, const bf16* g, bf16* out) {
  int i = blockIdx.x * 256 + threadIdx.x;
  float gg = b2f(g[i]); float sl = gg / (1.f + __expf(-gg));
  out[i] = f2b(b2f(x[i]) + b2f(s[i]) * sl);
}
extern "C" void kernel_launch(void* const* d_in, const int* in_sizes, int n_in, void* d_out, int out_size, void* d_ws, size_t ws_size, hipStream_t stream) {
  const bf16* x = (const bf16*)d_in[0]; const bf16* kc = (const bf16*)d_in[1]; const bf16* vc = (const bf16*)d_in[2];
  const bf16* wq = (const bf16*)d_in[3]; const bf16* wo = (const bf16*)d_in[4];
  const bf16* w1 = (const bf16*)d_in[6]; const bf16* b1 = (const bf16*)d_in[7]; const bf16* w2 = (const bf16*)d_in[8]; const bf16* b2 = (const bf16*)d_in[9];
  bf16* ws = (bf16*)d_ws;
  bf16* xn = ws; bf16* q = xn + 2048 * 1024; bf16* o = q + 2048 * 4096; bf16* h = o + 2048 * 4096; bf16* s = h + 2048 * 3584; bf16* g = s + 2048 * 1024;
  k_ln<<<2048, 256, 0, stream>>>(x, xn);
  k_gemm<0><<<dim3(4096 / 32, 16), 256, 0, stream>>>(xn, wq, nullptr, q, 2048, 4096, 1024);
  k_gemm<1><<<dim3(3584 / 32, 16), 256, 0, stream>>>(xn, w1, b1, h, 2048, 3584, 1024);
  k_attn<<<2048 * 32 / 4, 256, 0, stream>>>(q, kc, vc, o);
  k_gemm<0><<<dim3(1024 / 32, 16), 256, 0, stream>>>(o, wo, nullptr, s, 2048, 1024, 4096);
  k_gemm<2><<<dim3(1024 / 32, 16), 256, 0, stream>>>(h, w2, b2, g, 2048, 1024, 3584);
  k_fin<<<2048 * 1024 / 256, 256, 0, stream>>>(x, s, g, (bf16*)d_out);
}
```

```cpp
#include <hip/hip_runtime.h>
#include <hip/hip_bf16.h>
constexpr size_t F_EPOCH = 0u;
constexpr size_t F_STARTED = 1024u;
constexpr size_t F_XN = 4096u;
constexpr size_t F_KV = 6144u;
constexpr size_t WS_DBG = 65536u;
constexpr size_t WS_XN = 1048576u;
constexpr size_t WS_KP = 5242880u;
constexpr size_t WS_VP = 5767168u;
constexpr size_t WS_O = 6291456u;
constexpr size_t WS_H = 23068672u;
constexpr size_t WS_EX = 37748736u;
constexpr size_t WS_END = 54525952u;
constexpr int MAIN_LDS = 163840;
struct MainArgs { const void* x; const void* wq; const void* wo; const void* w1; const void* b1; const void* w2; const void* b2; void* out; void* ws; const void* kc; const void* vc; };
__global__ __launch_bounds__(256, 1) void main_kernel(MainArgs args) {
  asm volatile(
  "KSTART_%=:\n"
  "s_load_dwordx16 s[64:79], %0, 0x0\n"
  "s_load_dwordx2 s[80:81], %0, 0x40\n"
  "s_mov_b32 s28, %1\n"
  "v_mov_b32 v18, %2\n"
  "v_and_b32 v1, 63, v18\n"
  "v_lshrrev_b32 v19, 6, v18\n"
  "s_nop 0\n"
  "v_readfirstlane_b32 s25, v19\n"
  "s_and_b32 s26, s28, 7\n"
  "s_lshr_b32 s27, s28, 3\n"
  "s_getpc_b64 s[50:51]\n"
  "HERE_%=:\n"
  "s_add_u32 s52, s50, GEMM_FIRST_%=-HERE_%=\n"
  "s_addc_u32 s53, s51, 0\n"
  "s_add_u32 s54, s50, GEMM_LOOP_%=-HERE_%=\n"
  "s_addc_u32 s55, s51, 0\n"
  "s_lshr_b32 s29, s26, 2\n"
  "s_lshl_b32 s29, s29, 2\n"
  "s_lshr_b32 s30, s27, 3\n"
  "s_add_u32 s38, s29, s30\n"
  "s_and_b32 s29, s26, 3\n"
  "s_lshl_b32 s29, s29, 3\n"
  "s_and_b32 s30, s27, 7\n"
  "s_add_u32 s39, s29, s30\n"
  "s_lshr_b32 s42, s26, 1\n"
  "s_and_b32 s29, s26, 1\n"
  "s_lshl_b32 s30, s29, 5\n"
  "s_add_u32 s41, s30, s27\n"
  "s_lshl_b32 s29, s29, 2\n"
  "s_lshr_b32 s30, s27, 3\n"
  "s_add_u32 s89, s29, s30\n"
  "s_and_b32 s43, s27, 7\n"
  "s_lshl_b32 s24, s25, 12\n"
  "s_lshl_b32 s51, s28, 15\n"
  "s_add_u32 s51, s51, s24\n"
  "v_lshrrev_b32 v18, 4, v1\n"
  "v_and_b32 v19, 15, v1\n"
  "v_lshlrev_b32 v20, 12, v18\n"
  "v_lshl_add_u32 v20, v19, 4, v20\n"
  "s_lshl_b32 s29, s25, 10\n"
  "v_add_u32 v2, s29, v20\n"
  "v_bfe_u32 v19, v1, 2, 2\n"
  "v_lshl_add_u32 v20, v18, 2, v19\n"
  "v_and_b32 v21, 7, v20\n"
  "v_lshlrev_b32 v20, 8, v20\n"
  "v_and_b32 v19, 3, v1\n"
  "v_lshl_add_u32 v20, v19, 3, v20\n"
  "v_xor_b32 v19, 0, v21\n"
  "v_lshl_add_u32 v7, v19, 5, v20\n"
  "v_xor_b32 v19, 1, v21\n"
  "v_lshl_add_u32 v8, v19, 5, v20\n"
  "v_xor_b32 v19, 2, v21\n"
  "v_lshl_add_u32 v9, v19, 5, v20\n"
  "v_xor_b32 v19, 3, v21\n"
  "v_lshl_add_u32 v10, v19, 5, v20\n"
  "v_xor_b32 v19, 4, v21\n"
  "v_lshl_add_u32 v11, v19, 5, v20\n"
  "v_xor_b32 v19, 5, v21\n"
  "v_lshl_add_u32 v12, v19, 5, v20\n"
  "v_xor_b32 v19, 6, v21\n"
  "v_lshl_add_u32 v13, v19, 5, v20\n"
  "v_xor_b32 v19, 7, v21\n"
  "v_lshl_add_u32 v14, v19, 5, v20\n"
  "v_lshlrev_b32 v15, 4, v1\n"
  "v_mov_b32 v60, 0x3f803f80\n"
  "v_mov_b32 v61, 0x3f803f80\n"
  "v_mov_b32 v62, 0x3f803f80\n"
  "v_mov_b32 v63, 0x3f803f80\n"
  "s_waitcnt lgkmcnt(0)\n"
  "s_load_dwordx4 s[12:15], %0, 0x48\n"
  "s_waitcnt lgkmcnt(0)\n"
  "v_mov_b32 v56, 0\n"
  "global_load_dword v57, v56, s[80:81] sc1\n"
  "s_lshr_b32 s44, s28, 5\n"
  "s_and_b32 s44, s44, 3\n"
  "s_lshr_b32 s45, s28, 2\n"
  "s_and_b32 s45, s45, 7\n"
  "s_and_b32 s46, s28, 3\n"
  "s_lshl_b32 s46, s46, 2\n"
  "s_add_u32 s46, s46, s25\n"
  "s_lshl_b32 s29, s44, 11\n"
  "s_add_u32 s29, s29, 0x600\n"
  "s_lshl_b32 s30, s45, 6\n"
  "s_add_u32 s29, s29, s30\n"
  "v_and_b32 v18, 15, v1\n"
  "v_lshrrev_b32 v19, 4, v1\n"
  "s_cmp_ge_u32 s28, 0x80\n"
  "s_cbranch_scc1 PR_V_%=\n"
  "s_lshr_b32 s30, s46, 2\n"
  "s_lshl_b32 s30, s30, 4\n"
  "s_add_u32 s29, s29, s30\n"
  "s_lshl_b32 s29, s29, 8\n"
  "s_and_b32 s30, s46, 3\n"
  "s_lshl_b32 s30, s30, 6\n"
  "s_add_u32 s29, s29, s30\n"
  "s_add_u32 s16, s12, s29\n"
  "s_addc_u32 s17, s13, 0\n"
  "s_and_b32 s17, s17, 0xffff\n"
  "s_mov_b32 s18, 0xf40\n"
  "s_mov_b32 s19, 0x20000\n"
  "v_lshlrev_b32 v20, 8, v18\n"
  "v_lshl_add_u32 v20, v19, 3, v20\n"
  "buffer_load_dwordx2 v[112:113], v20, s[16:19], 0 offen\n"
  "buffer_load_dwordx2 v[114:115], v20, s[16:19], 0 offen offset:32\n"
  "s_branch PR_X_%=\n"
  "PR_V_%=:\n"
  "s_and_b32 s30, s46, 1\n"
  "s_lshl_b32 s30, s30, 5\n"
  "s_add_u32 s29, s29, s30\n"
  "s_lshl_b32 s29, s29, 8\n"
  "s_lshr_b32 s30, s46, 1\n"
  "s_lshl_b32 s30, s30, 5\n"
  "s_add_u32 s29, s29, s30\n"
  "s_add_u32 s16, s14, s29\n"
  "s_addc_u32 s17, s15, 0\n"
  "s_and_b32 s17, s17, 0xffff\n"
  "s_mov_b32 s18, 0x1f20\n"
  "s_mov_b32 s19, 0x20000\n"
  "v_lshlrev_b32 v20, 10, v19\n"
  "v_lshl_add_u32 v20, v18, 1, v20\n"
  "v_add_u32 v21, 0x1000, v20\n"
  "buffer_load_ushort v116, v20, s[16:19], 0 offen\n"
  "buffer_load_ushort v117, v20, s[16:19], 0 offen offset:256\n"
  "buffer_load_ushort v118, v20, s[16:19], 0 offen offset:512\n"
  "buffer_load_ushort v119, v20, s[16:19], 0 offen offset:768\n"
  "buffer_load_ushort v120, v21, s[16:19], 0 offen\n"
  "buffer_load_ushort v121, v21, s[16:19], 0 offen offset:256\n"
  "buffer_load_ushort v122, v21, s[16:19], 0 offen offset:512\n"
  "buffer_load_ushort v123, v21, s[16:19], 0 offen offset:768\n"
  "PR_X_%=:\n"
  "s_lshl_b32 s29, s28, 3\n"
  "s_lshl_b32 s30, s25, 1\n"
  "s_add_u32 s31, s29, s30\n"
  "s_lshl_b32 s29, s31, 11\n"
  "s_add_u32 s56, s64, s29\n"
  "s_addc_u32 s57, s65, 0\n"
  "s_and_b32 s57, s57, 0xffff\n"
  "s_mov_b32 s58, 0x1000\n"
  "s_mov_b32 s59, 0x20000\n"
  "v_lshrrev_b32 v20, 2, v1\n"
  "v_lshlrev_b32 v20, 6, v20\n"
  "v_and_b32 v21, 3, v1\n"
  "v_lshl_add_u32 v20, v21, 3, v20\n"
  "buffer_load_dwordx2 v[64:65], v20, s[56:59], 0 offen\n"
  "buffer_load_dwordx2 v[66:67], v20, s[56:59], 0 offen offset:32\n"
  "buffer_load_dwordx2 v[68:69], v20, s[56:59], 0 offen offset:1024\n"
  "buffer_load_dwordx2 v[70:71], v20, s[56:59], 0 offen offset:1056\n"
  "buffer_load_dwordx2 v[72:73], v20, s[56:59], 0 offen offset:2048\n"
  "buffer_load_dwordx2 v[74:75], v20, s[56:59], 0 offen offset:2080\n"
  "buffer_load_dwordx2 v[76:77], v20, s[56:59], 0 offen offset:3072\n"
  "buffer_load_dwordx2 v[78:79], v20, s[56:59], 0 offen offset:3104\n"
  "s_lshl_b32 s30, s39, 8\n"
  "s_add_u32 s8, s66, s30\n"
  "s_addc_u32 s9, s67, 0\n"
  "s_and_b32 s9, s9, 0xffff\n"
  "s_mov_b32 s10, 0x7fe100\n"
  "s_mov_b32 s11, 0x20000\n"
  "s_mov_b32 s21, 0\n"
  "s_mul_i32 s22, s25, 0x20000\n"
  "s_mov_b32 s23, 0x80000\n"
  "v_lshrrev_b32 v18, 4, v1\n"
  "v_bfe_u32 v19, v1, 1, 3\n"
  "v_and_b32 v20, 1, v1\n"
  "v_lshlrev_b32 v20, 4, v20\n"
  "s_mov_b32 s36, 0x2000\n"
  "v_add_u32 v21, 0, v18\n"
  "v_xor_b32 v21, v21, v19\n"
  "v_lshl_add_u32 v21, v21, 5, v20\n"
  "v_add_u32 v22, 0, v18\n"
  "v_mul_lo_u32 v22, v22, s36\n"
  "v_add_u32 v3, v22, v21\n"
  "v_add_u32 v21, 4, v18\n"
  "v_xor_b32 v21, v21, v19\n"
  "v_lshl_add_u32 v21, v21, 5, v20\n"
  "v_add_u32 v22, 4, v18\n"
  "v_mul_lo_u32 v22, v22, s36\n"
  "v_add_u32 v4, v22, v21\n"
  "v_subrev_u32 v4, 0x400, v4\n"
  "v_add_u32 v21, 0, v18\n"
  "v_xor_b32 v21, v21, v19\n"
  "v_lshl_add_u32 v21, v21, 5, v20\n"
  "v_add_u32 v22, 8, v18\n"
  "v_mul_lo_u32 v22, v22, s36\n"
  "v_add_u32 v5, v22, v21\n"
  "v_subrev_u32 v5, 0x800, v5\n"
  "v_add_u32 v21, 4, v18\n"
  "v_xor_b32 v21, v21, v19\n"
  "v_lshl_add_u32 v21, v21, 5, v20\n"
  "v_add_u32 v22, 12, v18\n"
  "v_mul_lo_u32 v22, v22, s36\n"
  "v_add_u32 v6, v22, v21\n"
  "v_subrev_u32 v6, 0xc00, v6\n"
  "s_add_u32 m0, s24, 0\n"
  "s_nop 0\n"
  "buffer_load_dwordx4 v3, s[8:11], s22 offen lds\n"
  "buffer_load_dwordx4 v4, s[8:11], s22 offen offset:1024 lds\n"
  "buffer_load_dwordx4 v5, s[8:11], s22 offen offset:2048 lds\n"
  "buffer_load_dwordx4 v6, s[8:11], s22 offen offset:3072 lds\n"
  "s_add_u32 s22, s22, s23\n"
  "s_add_u32 m0, s24, 0x4000\n"
  "s_nop 0\n"
  "buffer_load_dwordx4 v3, s[8:11], s22 offen lds\n"
  "buffer_load_dwordx4 v4, s[8:11], s22 offen offset:1024 lds\n"
  "buffer_load_dwordx4 v5, s[8:11], s22 offen offset:2048 lds\n"
  "buffer_load_dwordx4 v6, s[8:11], s22 offen offset:3072 lds\n"
  "s_add_u32 s22, s22, s23\n"
  "s_add_u32 m0, s24, 0x8000\n"
  "s_nop 0\n"
  "buffer_load_dwordx4 v3, s[8:11], s22 offen lds\n"
  "buffer_load_dwordx4 v4, s[8:11], s22 offen offset:1024 lds\n"
  "buffer_load_dwordx4 v5, s[8:11], s22 offen offset:2048 lds\n"
  "buffer_load_dwordx4 v6, s[8:11], s22 offen offset:3072 lds\n"
  "s_add_u32 s22, s22, s23\n"
  "s_add_u32 m0, s24, 0xc000\n"
  "s_nop 0\n"
  "buffer_load_dwordx4 v3, s[8:11], s22 offen lds\n"
  "buffer_load_dwordx4 v4, s[8:11], s22 offen offset:1024 lds\n"
  "buffer_load_dwordx4 v5, s[8:11], s22 offen offset:2048 lds\n"
  "buffer_load_dwordx4 v6, s[8:11], s22 offen offset:3072 lds\n"
  "s_add_u32 s22, s22, s23\n"
  "s_getpc_b64 s[30:31]\n"
  "CPF_%=:\n"
  "s_sub_u32 s30, s30, CPF_%=-KSTART_%=\n"
  "s_subb_u32 s31, s31, 0\n"
  "s_and_b32 s13, s31, 0xffff\n"
  "s_mov_b32 s12, s30\n"
  "s_mov_b32 s14, KEND_%=-KSTART_%=\n"
  "s_mov_b32 s15, 0x20000\n"
  "s_lshl_b32 s29, s27, 2\n"
  "s_add_u32 s29, s29, s25\n"
  "s_lshl_b32 s29, s29, 10\n"
  "buffer_load_dwordx4 v[48:51], v15, s[12:15], s29 offen\n"
  "s_lshr_b32 s29, s28, 5\n"
  "s_lshl_b32 s29, s29, 19\n"
  "s_and_b32 s30, s28, 31\n"
  "s_lshl_b32 s30, s30, 7\n"
  "s_add_u32 s29, s29, s30\n"
  "s_add_u32 s4, s80, 0x100000\n"
  "s_addc_u32 s5, s81, 0\n"
  "s_add_u32 s4, s4, s29\n"
  "s_addc_u32 s5, s5, 0\n"
  "s_and_b32 s5, s5, 0xffff\n"
  "s_mov_b32 s6, 0x7f080\n"
  "s_mov_b32 s7, 0x20000\n"
  "s_waitcnt vmcnt(17)\n"
  "v_readfirstlane_b32 s90, v57\n"
  "s_nop 1\n"
  "s_add_u32 s90, s90, 1\n"
  "s_lshl_b32 s29, s28, 2\n"
  "s_add_u32 s29, s29, 0x400\n"
  "s_cmp_lg_u32 s25, 0\n"
  "s_cbranch_scc1 PUBSKIP_1_%=\n"
  "v_mov_b32 v56, s29\n"
  "v_mov_b32 v57, s90\n"
  "s_mov_b64 exec, 1\n"
  "global_store_dword v56, v57, s[80:81] sc1\n"
  "s_mov_b64 exec, -1\n"
  "PUBSKIP_1_%=:\n"
  "v_lshlrev_b32 v80, 16, v64\n"
  "v_and_b32 v81, 0xffff0000, v64\n"
  "v_lshlrev_b32 v82, 16, v65\n"
  "v_and_b32 v83, 0xffff0000, v65\n"
  "v_lshlrev_b32 v84, 16, v66\n"
  "v_and_b32 v85, 0xffff0000, v66\n"
  "v_lshlrev_b32 v86, 16, v67\n"
  "v_and_b32 v87, 0xffff0000, v67\n"
  "v_lshlrev_b32 v88, 16, v68\n"
  "v_and_b32 v89, 0xffff0000, v68\n"
  "v_lshlrev_b32 v90, 16, v69\n"
  "v_and_b32 v91, 0xffff0000, v69\n"
  "v_lshlrev_b32 v92, 16, v70\n"
  "v_and_b32 v93, 0xffff0000, v70\n"
  "v_lshlrev_b32 v94, 16, v71\n"
  "v_and_b32 v95, 0xffff0000, v71\n"
  "v_lshlrev_b32 v96, 16, v72\n"
  "v_and_b32 v97, 0xffff0000, v72\n"
  "v_lshlrev_b32 v98, 16, v73\n"
  "v_and_b32 v99, 0xffff0000, v73\n"
  "v_lshlrev_b32 v100, 16, v74\n"
  "v_and_b32 v101, 0xffff0000, v74\n"
  "v_lshlrev_b32 v102, 16, v75\n"
  "v_and_b32 v103, 0xffff0000, v75\n"
  "v_lshlrev_b32 v104, 16, v76\n"
  "v_and_b32 v105, 0xffff0000, v76\n"
  "v_lshlrev_b32 v106, 16, v77\n"
  "v_and_b32 v107, 0xffff0000, v77\n"
  "v_lshlrev_b32 v108, 16, v78\n"
  "v_and_b32 v109, 0xffff0000, v78\n"
  "v_lshlrev_b32 v110, 16, v79\n"
  "v_and_b32 v111, 0xffff0000, v79\n"
  "v_add_f32 v22, v80, v81\n"
  "v_add_f32 v23, v96, v97\n"
  "v_add_f32 v22, v22, v82\n"
  "v_add_f32 v23, v23, v98\n"
  "v_add_f32 v22, v22, v83\n"
  "v_add_f32 v23, v23, v99\n"
  "v_add_f32 v22, v22, v84\n"
  "v_add_f32 v23, v23, v100\n"
  "v_add_f32 v22, v22, v85\n"
  "v_add_f32 v23, v23, v101\n"
  "v_add_f32 v22, v22, v86\n"
  "v_add_f32 v23, v23, v102\n"
  "v_add_f32 v22, v22, v87\n"
  "v_add_f32 v23, v23, v103\n"
  "v_add_f32 v22, v22, v88\n"
  "v_add_f32 v23, v23, v104\n"
  "v_add_f32 v22, v22, v89\n"
  "v_add_f32 v23, v23, v105\n"
  "v_add_f32 v22, v22, v90\n"
  "v_add_f32 v23, v23, v106\n"
  "v_add_f32 v22, v22, v91\n"
  "v_add_f32 v23, v23, v107\n"
  "v_add_f32 v22, v22, v92\n"
  "v_add_f32 v23, v23, v108\n"
  "v_add_f32 v22, v22, v93\n"
  "v_add_f32 v23, v23, v109\n"
  "v_add_f32 v22, v22, v94\n"
  "v_add_f32 v23, v23, v110\n"
  "v_add_f32 v22, v22, v95\n"
  "v_add_f32 v23, v23, v111\n"
  "s_nop 0\n"
  "v_add_f32_dpp v22, v22, v22 row_ror:8 row_mask:0xf bank_mask:0xf\n"
  "v_add_f32_dpp v23, v23, v23 row_ror:8 row_mask:0xf bank_mask:0xf\n"
  "s_nop 0\n"
  "v_add_f32_dpp v22, v22, v22 row_ror:4 row_mask:0xf bank_mask:0xf\n"
  "v_add_f32_dpp v23, v23, v23 row_ror:4 row_mask:0xf bank_mask:0xf\n"
  "s_nop 0\n"
  "v_add_f32_dpp v22, v22, v22 row_ror:2 row_mask:0xf bank_mask:0xf\n"
  "v_add_f32_dpp v23, v23, v23 row_ror:2 row_mask:0xf bank_mask:0xf\n"
  "s_nop 0\n"
  "v_add_f32_dpp v22, v22, v22 row_ror:1 row_mask:0xf bank_mask:0xf\n"
  "v_add_f32_dpp v23, v23, v23 row_ror:1 row_mask:0xf bank_mask:0xf\n"
  "v_mov_b32 v24, v22\n"
  "v_mov_b32 v25, v23\n"
  "s_nop 0\n"
  "v_permlane16_swap_b32 v22, v24\n"
  "v_permlane16_swap_b32 v23, v25\n"
  "v_add_f32 v22, v22, v24\n"
  "v_add_f32 v23, v23, v25\n"
  "v_mov_b32 v24, v22\n"
  "v_mov_b32 v25, v23\n"
  "s_nop 0\n"
  "v_permlane32_swap_b32 v22, v24\n"
  "v_permlane32_swap_b32 v23, v25\n"
  "v_add_f32 v22, v22, v24\n"
  "v_add_f32 v23, v23, v25\n"
  "v_mul_f32 v22, 0x3a800000, v22\n"
  "v_mul_f32 v23, 0x3a800000, v23\n"
  "v_sub_f32 v28, v80, v22\n"
  "v_sub_f32 v29, v96, v23\n"
  "v_mul_f32 v26, v28, v28\n"
  "v_mul_f32 v27, v29, v29\n"
  "v_sub_f32 v28, v81, v22\n"
  "v_sub_f32 v29, v97, v23\n"
  "v_fmac_f32 v26, v28, v28\n"
  "v_fmac_f32 v27, v29, v29\n"
  "v_sub_f32 v28, v82, v22\n"
  "v_sub_f32 v29, v98, v23\n"
  "v_fmac_f32 v26, v28, v28\n"
  "v_fmac_f32 v27, v29, v29\n"
  "v_sub_f32 v28, v83, v22\n"
  "v_sub_f32 v29, v99, v23\n"
  "v_fmac_f32 v26, v28, v28\n"
  "v_fmac_f32 v27, v29, v29\n"
  "v_sub_f32 v28, v84, v22\n"
  "v_sub_f32 v29, v100, v23\n"
  "v_fmac_f32 v26, v28, v28\n"
  "v_fmac_f32 v27, v29, v29\n"
  "v_sub_f32 v28, v85, v22\n"
  "v_sub_f32 v29, v101, v23\n"
  "v_fmac_f32 v26, v28, v28\n"
  "v_fmac_f32 v27, v29, v29\n"
  "v_sub_f32 v28, v86, v22\n"
  "v_sub_f32 v29, v102, v23\n"
  "v_fmac_f32 v26, v28, v28\n"
  "v_fmac_f32 v27, v29, v29\n"
  "v_sub_f32 v28, v87, v22\n"
  "v_sub_f32 v29, v103, v23\n"
  "v_fmac_f32 v26, v28, v28\n"
  "v_fmac_f32 v27, v29, v29\n"
  "v_sub_f32 v28, v88, v22\n"
  "v_sub_f32 v29, v104, v23\n"
  "v_fmac_f32 v26, v28, v28\n"
  "v_fmac_f32 v27, v29, v29\n"
  "v_sub_f32 v28, v89, v22\n"
  "v_sub_f32 v29, v105, v23\n"
  "v_fmac_f32 v26, v28, v28\n"
  "v_fmac_f32 v27, v29, v29\n"
  "v_sub_f32 v28, v90, v22\n"
  "v_sub_f32 v29, v106, v23\n"
  "v_fmac_f32 v26, v28, v28\n"
  "v_fmac_f32 v27, v29, v29\n"
  "v_sub_f32 v28, v91, v22\n"
  "v_sub_f32 v29, v107, v23\n"
  "v_fmac_f32 v26, v28, v28\n"
  "v_fmac_f32 v27, v29, v29\n"
  "v_sub_f32 v28, v92, v22\n"
  "v_sub_f32 v29, v108, v23\n"
  "v_fmac_f32 v26, v28, v28\n"
  "v_fmac_f32 v27, v29, v29\n"
  "v_sub_f32 v28, v93, v22\n"
  "v_sub_f32 v29, v109, v23\n"
  "v_fmac_f32 v26, v28, v28\n"
  "v_fmac_f32 v27, v29, v29\n"
  "v_sub_f32 v28, v94, v22\n"
  "v_sub_f32 v29, v110, v23\n"
  "v_fmac_f32 v26, v28, v28\n"
  "v_fmac_f32 v27, v29, v29\n"
  "v_sub_f32 v28, v95, v22\n"
  "v_sub_f32 v29, v111, v23\n"
  "v_fmac_f32 v26, v28, v28\n"
  "v_fmac_f32 v27, v29, v29\n"
  "s_nop 0\n"
  "v_add_f32_dpp v26, v26, v26 row_ror:8 row_mask:0xf bank_mask:0xf\n"
  "v_add_f32_dpp v27, v27, v27 row_ror:8 row_mask:0xf bank_mask:0xf\n"
  "s_nop 0\n"
  "v_add_f32_dpp v26, v26, v26 row_ror:4 row_mask:0xf bank_mask:0xf\n"
  "v_add_f32_dpp v27, v27, v27 row_ror:4 row_mask:0xf bank_mask:0xf\n"
  "s_nop 0\n"
  "v_add_f32_dpp v26, v26, v26 row_ror:2 row_mask:0xf bank_mask:0xf\n"
  "v_add_f32_dpp v27, v27, v27 row_ror:2 row_mask:0xf bank_mask:0xf\n"
  "s_nop 0\n"
  "v_add_f32_dpp v26, v26, v26 row_ror:1 row_mask:0xf bank_mask:0xf\n"
  "v_add_f32_dpp v27, v27, v27 row_ror:1 row_mask:0xf bank_mask:0xf\n"
  "v_mov_b32 v24, v26\n"
  "v_mov_b32 v25, v27\n"
  "s_nop 0\n"
  "v_permlane16_swap_b32 v26, v24\n"
  "v_permlane16_swap_b32 v27, v25\n"
  "v_add_f32 v26, v26, v24\n"
  "v_add_f32 v27, v27, v25\n"
  "v_mov_b32 v24, v26\n"
  "v_mov_b32 v25, v27\n"
  "s_nop 0\n"
  "v_permlane32_swap_b32 v26, v24\n"
  "v_permlane32_swap_b32 v27, v25\n"
  "v_add_f32 v26, v26, v24\n"
  "v_add_f32 v27, v27, v25\n"
  "v_mul_f32 v26, 0x3a800000, v26\n"
  "v_cvt_pk_bf16_f32 v26, v26, v26\n"
  "v_lshlrev_b32 v26, 16, v26\n"
  "v_add_f32 v26, 0x3727c5ac, v26\n"
  "v_cvt_pk_bf16_f32 v26, v26, v26\n"
  "v_lshlrev_b32 v26, 16, v26\n"
  "v_sqrt_f32 v26, v26\n"
  "s_nop 0\n"
  "v_cvt_pk_bf16_f32 v26, v26, v26\n"
  "v_lshlrev_b32 v26, 16, v26\n"
  "v_rcp_f32 v26, v26\n"
  "v_cvt_pk_bf16_f32 v22, v22, v22\n"
  "v_lshlrev_b32 v22, 16, v22\n"
  "v_mul_f32 v27, 0x3a800000, v27\n"
  "v_cvt_pk_bf16_f32 v27, v27, v27\n"
  "v_lshlrev_b32 v27, 16, v27\n"
  "v_add_f32 v27, 0x3727c5ac, v27\n"
  "v_cvt_pk_bf16_f32 v27, v27, v27\n"
  "v_lshlrev_b32 v27, 16, v27\n"
  "v_sqrt_f32 v27, v27\n"
  "s_nop 0\n"
  "v_cvt_pk_bf16_f32 v27, v27, v27\n"
  "v_lshlrev_b32 v27, 16, v27\n"
  "v_rcp_f32 v27, v27\n"
  "v_cvt_pk_bf16_f32 v23, v23, v23\n"
  "v_lshlrev_b32 v23, 16, v23\n"
  "v_lshlrev_b32 v20, 7, v1\n"
  "s_lshl_b32 s29, s25, 5\n"
  "s_add_u32 s29, s29, 0x10000\n"
  "v_add_u32 v20, s29, v20\n"
  "v_sub_f32 v80, v80, v22\n"
  "v_sub_f32 v81, v81, v22\n"
  "v_cvt_pk_bf16_f32 v64, v80, v81\n"
  "v_lshlrev_b32 v80, 16, v64\n"
  "v_and_b32 v81, 0xffff0000, v64\n"
  "v_mul_f32 v80, v80, v26\n"
  "v_mul_f32 v81, v81, v26\n"
  "v_cvt_pk_bf16_f32 v64, v80, v81\n"
  "v_sub_f32 v82, v82, v22\n"
  "v_sub_f32 v83, v83, v22\n"
  "v_cvt_pk_bf16_f32 v65, v82, v83\n"
  "v_lshlrev_b32 v82, 16, v65\n"
  "v_and_b32 v83, 0xffff0000, v65\n"
  "v_mul_f32 v82, v82, v26\n"
  "v_mul_f32 v83, v83, v26\n"
  "v_cvt_pk_bf16_f32 v65, v82, v83\n"
  "v_sub_f32 v84, v84, v22\n"
  "v_sub_f32 v85, v85, v22\n"
  "v_cvt_pk_bf16_f32 v66, v84, v85\n"
  "v_lshlrev_b32 v84, 16, v66\n"
  "v_and_b32 v85, 0xffff0000, v66\n"
  "v_mul_f32 v84, v84, v26\n"
  "v_mul_f32 v85, v85, v26\n"
  "v_cvt_pk_bf16_f32 v66, v84, v85\n"
  "v_sub_f32 v86, v86, v22\n"
  "v_sub_f32 v87, v87, v22\n"
  "v_cvt_pk_bf16_f32 v67, v86, v87\n"
  "v_lshlrev_b32 v86, 16, v67\n"
  "v_and_b32 v87, 0xffff0000, v67\n"
  "v_mul_f32 v86, v86, v26\n"
  "v_mul_f32 v87, v87, v26\n"
  "v_cvt_pk_bf16_f32 v67, v86, v87\n"
  "ds_write_b128 v20, v[64:67]\n"
  "v_sub_f32 v88, v88, v22\n"
  "v_sub_f32 v89, v89, v22\n"
  "v_cvt_pk_bf16_f32 v68, v88, v89\n"
  "v_lshlrev_b32 v88, 16, v68\n"
  "v_and_b32 v89, 0xffff0000, v68\n"
  "v_mul_f32 v88, v88, v26\n"
  "v_mul_f32 v89, v89, v26\n"
  "v_cvt_pk_bf16_f32 v68, v88, v89\n"
  "v_sub_f32 v90, v90, v22\n"
  "v_sub_f32 v91, v91, v22\n"
  "v_cvt_pk_bf16_f32 v69, v90, v91\n"
  "v_lshlrev_b32 v90, 16, v69\n"
  "v_and_b32 v91, 0xffff0000, v69\n"
  "v_mul_f32 v90, v90, v26\n"
  "v_mul_f32 v91, v91, v26\n"
  "v_cvt_pk_bf16_f32 v69, v90, v91\n"
  "v_sub_f32 v92, v92, v22\n"
  "v_sub_f32 v93, v93, v22\n"
  "v_cvt_pk_bf16_f32 v70, v92, v93\n"
  "v_lshlrev_b32 v92, 16, v70\n"
  "v_and_b32 v93, 0xffff0000, v70\n"
  "v_mul_f32 v92, v92, v26\n"
  "v_mul_f32 v93, v93, v26\n"
  "v_cvt_pk_bf16_f32 v70, v92, v93\n"
  "v_sub_f32 v94, v94, v22\n"
  "v_sub_f32 v95, v95, v22\n"
  "v_cvt_pk_bf16_f32 v71, v94, v95\n"
  "v_lshlrev_b32 v94, 16, v71\n"
  "v_and_b32 v95, 0xffff0000, v71\n"
  "v_mul_f32 v94, v94, v26\n"
  "v_mul_f32 v95, v95, v26\n"
  "v_cvt_pk_bf16_f32 v71, v94, v95\n"
  "ds_write_b128 v20, v[68:71] offset:8192\n"
  "v_sub_f32 v96, v96, v23\n"
  "v_sub_f32 v97, v97, v23\n"
  "v_cvt_pk_bf16_f32 v72, v96, v97\n"
  "v_lshlrev_b32 v96, 16, v72\n"
  "v_and_b32 v97, 0xffff0000, v72\n"
  "v_mul_f32 v96, v96, v27\n"
  "v_mul_f32 v97, v97, v27\n"
  "v_cvt_pk_bf16_f32 v72, v96, v97\n"
  "v_sub_f32 v98, v98, v23\n"
  "v_sub_f32 v99, v99, v23\n"
  "v_cvt_pk_bf16_f32 v73, v98, v99\n"
  "v_lshlrev_b32 v98, 16, v73\n"
  "v_and_b32 v99, 0xffff0000, v73\n"
  "v_mul_f32 v98, v98, v27\n"
  "v_mul_f32 v99, v99, v27\n"
  "v_cvt_pk_bf16_f32 v73, v98, v99\n"
  "v_sub_f32 v100, v100, v23\n"
  "v_sub_f32 v101, v101, v23\n"
  "v_cvt_pk_bf16_f32 v74, v100, v101\n"
  "v_lshlrev_b32 v100, 16, v74\n"
  "v_and_b32 v101, 0xffff0000, v74\n"
  "v_mul_f32 v100, v100, v27\n"
  "v_mul_f32 v101, v101, v27\n"
  "v_cvt_pk_bf16_f32 v74, v100, v101\n"
  "v_sub_f32 v102, v102, v23\n"
  "v_sub_f32 v103, v103, v23\n"
  "v_cvt_pk_bf16_f32 v75, v102, v103\n"
  "v_lshlrev_b32 v102, 16, v75\n"
  "v_and_b32 v103, 0xffff0000, v75\n"
  "v_mul_f32 v102, v102, v27\n"
  "v_mul_f32 v103, v103, v27\n"
  "v_cvt_pk_bf16_f32 v75, v102, v103\n"
  "ds_write_b128 v20, v[72:75] offset:16\n"
  "v_sub_f32 v104, v104, v23\n"
  "v_sub_f32 v105, v105, v23\n"
  "v_cvt_pk_bf16_f32 v76, v104, v105\n"
  "v_lshlrev_b32 v104, 16, v76\n"
  "v_and_b32 v105, 0xffff0000, v76\n"
  "v_mul_f32 v104, v104, v27\n"
  "v_mul_f32 v105, v105, v27\n"
  "v_cvt_pk_bf16_f32 v76, v104, v105\n"
  "v_sub_f32 v106, v106, v23\n"
  "v_sub_f32 v107, v107, v23\n"
  "v_cvt_pk_bf16_f32 v77, v106, v107\n"
  "v_lshlrev_b32 v106, 16, v77\n"
  "v_and_b32 v107, 0xffff0000, v77\n"
  "v_mul_f32 v106, v106, v27\n"
  "v_mul_f32 v107, v107, v27\n"
  "v_cvt_pk_bf16_f32 v77, v106, v107\n"
  "v_sub_f32 v108, v108, v23\n"
  "v_sub_f32 v109, v109, v23\n"
  "v_cvt_pk_bf16_f32 v78, v108, v109\n"
  "v_lshlrev_b32 v108, 16, v78\n"
  "v_and_b32 v109, 0xffff0000, v78\n"
  "v_mul_f32 v108, v108, v27\n"
  "v_mul_f32 v109, v109, v27\n"
  "v_cvt_pk_bf16_f32 v78, v108, v109\n"
  "v_sub_f32 v110, v110, v23\n"
  "v_sub_f32 v111, v111, v23\n"
  "v_cvt_pk_bf16_f32 v79, v110, v111\n"
  "v_lshlrev_b32 v110, 16, v79\n"
  "v_and_b32 v111, 0xffff0000, v79\n"
  "v_mul_f32 v110, v110, v27\n"
  "v_mul_f32 v111, v111, v27\n"
  "v_cvt_pk_bf16_f32 v79, v110, v111\n"
  "ds_write_b128 v20, v[76:79] offset:8208\n"
  "s_waitcnt lgkmcnt(0)\n"
  "s_barrier\n"
  "s_lshl_b32 s29, s25, 12\n"
  "s_add_u32 s29, s29, 0x10000\n"
  "v_add_u32 v20, s29, v15\n"
  "ds_read_b128 v[80:83], v20\n"
  "ds_read_b128 v[84:87], v20 offset:1024\n"
  "ds_read_b128 v[88:91], v20 offset:2048\n"
  "ds_read_b128 v[92:95], v20 offset:3072\n"
  "v_lshrrev_b32 v21, 3, v1\n"
  "v_lshlrev_b32 v21, 12, v21\n"
  "v_and_b32 v20, 7, v1\n"
  "v_lshl_add_u32 v21, v20, 4, v21\n"
  "s_lshl_b32 s29, s25, 17\n"
  "s_add_u32 s29, s29, 0\n"
  "s_waitcnt lgkmcnt(3)\n"
  "buffer_store_dwordx4 v[80:83], v21, s[4:7], s29 offen sc1\n"
  "s_lshl_b32 s29, s25, 17\n"
  "s_add_u32 s29, s29, 0x8000\n"
  "s_waitcnt lgkmcnt(2)\n"
  "buffer_store_dwordx4 v[84:87], v21, s[4:7], s29 offen sc1\n"
  "s_lshl_b32 s29, s25, 17\n"
  "s_add_u32 s29, s29, 0x10000\n"
  "s_waitcnt lgkmcnt(1)\n"
  "buffer_store_dwordx4 v[88:91], v21, s[4:7], s29 offen sc1\n"
  "s_lshl_b32 s29, s25, 17\n"
  "s_add_u32 s29, s29, 0x18000\n"
  "s_waitcnt lgkmcnt(0)\n"
  "buffer_store_dwordx4 v[92:95], v21, s[4:7], s29 offen sc1\n"
  "s_cmp_lt_u32 s28, 0x80\n"
  "s_cbranch_scc1 PR_KST_%=\n"
  "v_lshl_or_b32 v112, v117, 16, v116\n"
  "v_lshl_or_b32 v113, v119, 16, v118\n"
  "v_lshl_or_b32 v114, v121, 16, v120\n"
  "v_lshl_or_b32 v115, v123, 16, v122\n"
  "PR_KST_%=:\n"
  "s_lshr_b32 s29, s28, 7\n"
  "s_lshl_b32 s29, s29, 19\n"
  "s_lshl_b32 s30, s44, 17\n"
  "s_add_u32 s29, s29, s30\n"
  "s_lshl_b32 s30, s45, 14\n"
  "s_add_u32 s29, s29, s30\n"
  "s_add_u32 s16, s80, 0x500000\n"
  "s_addc_u32 s17, s81, 0\n"
  "s_add_u32 s16, s16, s29\n"
  "s_addc_u32 s17, s17, 0\n"
  "s_and_b32 s17, s17, 0xffff\n"
  "s_mov_b32 s18, 0x4000\n"
  "s_mov_b32 s19, 0x20000\n"
  "s_lshl_b32 s30, s46, 10\n"
  "buffer_store_dwordx4 v[112:115], v15, s[16:19], s30 offen sc1\n"
  "s_waitcnt vmcnt(0)\n"
  "s_barrier\n"
  "s_cmp_lg_u32 s25, 0\n"
  "s_cbranch_scc1 PR_PUB_%=\n"
  "s_lshl_b32 s29, s28, 2\n"
  "s_add_u32 s29, s29, 0x1000\n"
  "v_mov_b32 v56, s29\n"
  "v_mov_b32 v57, s90\n"
  "s_mov_b64 exec, 1\n"
  "global_store_dword v56, v57, s[80:81] sc1\n"
  "s_mov_b64 exec, -1\n"
  "s_lshl_b32 s29, s44, 8\n"
  "s_lshr_b32 s30, s28, 7\n"
  "s_lshl_b32 s30, s30, 7\n"
  "s_add_u32 s29, s29, s30\n"
  "s_and_b32 s30, s28, 31\n"
  "s_lshl_b32 s30, s30, 2\n"
  "s_add_u32 s29, s29, s30\n"
  "s_add_u32 s29, s29, 0x1800\n"
  "v_mov_b32 v56, s29\n"
  "v_mov_b32 v57, s90\n"
  "s_mov_b64 exec, 1\n"
  "global_store_dword v56, v57, s[80:81] sc1\n"
  "s_mov_b64 exec, -1\n"
  "PR_PUB_%=:\n"
  "s_lshl_b32 s29, s38, 19\n"
  "s_add_u32 s4, s80, 0x100000\n"
  "s_addc_u32 s5, s81, 0\n"
  "s_add_u32 s4, s4, s29\n"
  "s_addc_u32 s5, s5, 0\n"
  "s_and_b32 s5, s5, 0xffff\n"
  "s_mov_b32 s6, 0x80000\n"
  "s_mov_b32 s7, 0x20000\n"
  "s_mov_b32 s21, 0\n"
  "ds_read_b64_tr_b16 a[0:1], v7\n"
  "ds_read_b64_tr_b16 a[2:3], v7 offset:4096\n"
  "ds_read_b64_tr_b16 a[4:5], v8\n"
  "ds_read_b64_tr_b16 a[6:7], v8 offset:4096\n"
  "ds_read_b64_tr_b16 a[8:9], v9\n"
  "ds_read_b64_tr_b16 a[10:11], v9 offset:4096\n"
  "ds_read_b64_tr_b16 a[12:13], v10\n"
  "ds_read_b64_tr_b16 a[14:15], v10 offset:4096\n"
  "ds_read_b64_tr_b16 a[16:17], v11\n"
  "ds_read_b64_tr_b16 a[18:19], v11 offset:4096\n"
  "ds_read_b64_tr_b16 a[20:21], v12\n"
  "ds_read_b64_tr_b16 a[22:23], v12 offset:4096\n"
  "ds_read_b64_tr_b16 a[24:25], v13\n"
  "ds_read_b64_tr_b16 a[26:27], v13 offset:4096\n"
  "ds_read_b64_tr_b16 a[28:29], v14\n"
  "ds_read_b64_tr_b16 a[30:31], v14 offset:4096\n"
  "s_and_b32 s29, s26, 3\n"
  "s_lshl_b32 s29, s29, 17\n"
  "s_add_u32 s16, s80, 0x500000\n"
  "s_addc_u32 s17, s81, 0\n"
  "s_add_u32 s16, s16, s29\n"
  "s_addc_u32 s17, s17, 0\n"
  "s_and_b32 s17, s17, 0xffff\n"
  "s_mov_b32 s18, 0x20000\n"
  "s_mov_b32 s19, 0x20000\n"
  "s_add_u32 s56, s80, 0x580000\n"
  "s_addc_u32 s57, s81, 0\n"
  "s_add_u32 s56, s56, s29\n"
  "s_addc_u32 s57, s57, 0\n"
  "s_and_b32 s57, s57, 0xffff\n"
  "s_mov_b32 s58, 0x20000\n"
  "s_mov_b32 s59, 0x20000\n"
  "s_mov_b32 s88, -1\n"
  "s_mov_b32 s12, s80\n"
  "s_and_b32 s13, s81, 0xffff\n"
  "s_mov_b32 s14, 0\n"
  "s_mov_b32 s15, 0x20000\n"
  "s_mov_b32 s91, 0\n"
  "s_lshl_b32 s29, s38, 7\n"
  "s_add_u32 s29, s29, 0x1000\n"
  "v_min_u32 v56, 31, v1\n"
  "v_lshlrev_b32 v56, 2, v56\n"
  "v_add_u32 v56, s29, v56\n"
  "s_mov_b32 s63, 0x30d40\n"
  "POLL_2_%=:\n"
  "global_load_dword v57, v56, s[80:81] sc1\n"
  "s_waitcnt vmcnt(0)\n"
  "v_cmp_ne_u32 vcc, s90, v57\n"
  "s_nop 1\n"
  "s_cmp_eq_u64 vcc, 0\n"
  "s_cbranch_scc1 POLLOK_3_%=\n"
  "s_sleep 1\n"
  "s_sub_u32 s63, s63, 1\n"
  "s_cmp_lg_u32 s63, 0\n"
  "s_cbranch_scc1 POLL_2_%=\n"
  "POLLOK_3_%=:\n"
  "buffer_load_dwordx4 a[64:67], v2, s[4:7], s21 offen sc1\n"
  "buffer_load_dwordx4 a[68:71], v2, s[4:7], s21 offen offset:256 sc1\n"
  "buffer_load_dwordx4 a[72:75], v2, s[4:7], s21 offen offset:512 sc1\n"
  "buffer_load_dwordx4 a[76:79], v2, s[4:7], s21 offen offset:768 sc1\n"
  "s_add_u32 s21, s21, 0x4000\n"
  "buffer_load_dwordx4 a[80:83], v2, s[4:7], s21 offen sc1\n"
  "buffer_load_dwordx4 a[84:87], v2, s[4:7], s21 offen offset:256 sc1\n"
  "buffer_load_dwordx4 a[88:91], v2, s[4:7], s21 offen offset:512 sc1\n"
  "buffer_load_dwordx4 a[92:95], v2, s[4:7], s21 offen offset:768 sc1\n"
  "s_add_u32 s21, s21, 0x4000\n"
  "buffer_load_dwordx4 a[96:99], v2, s[4:7], s21 offen sc1\n"
  "buffer_load_dwordx4 a[100:103], v2, s[4:7], s21 offen offset:256 sc1\n"
  "buffer_load_dwordx4 a[104:107], v2, s[4:7], s21 offen offset:512 sc1\n"
  "buffer_load_dwordx4 a[108:111], v2, s[4:7], s21 offen offset:768 sc1\n"
  "s_add_u32 s21, s21, 0x4000\n"
  "buffer_load_dwordx4 a[112:115], v2, s[4:7], s21 offen sc1\n"
  "buffer_load_dwordx4 a[116:119], v2, s[4:7], s21 offen offset:256 sc1\n"
  "buffer_load_dwordx4 a[120:123], v2, s[4:7], s21 offen offset:512 sc1\n"
  "buffer_load_dwordx4 a[124:127], v2, s[4:7], s21 offen offset:768 sc1\n"
  "s_add_u32 s21, s21, 0x4000\n"
  "buffer_load_dwordx4 a[128:131], v2, s[4:7], s21 offen sc1\n"
  "buffer_load_dwordx4 a[132:135], v2, s[4:7], s21 offen offset:256 sc1\n"
  "buffer_load_dwordx4 a[136:139], v2, s[4:7], s21 offen offset:512 sc1\n"
  "buffer_load_dwordx4 a[140:143], v2, s[4:7], s21 offen offset:768 sc1\n"
  "s_add_u32 s21, s21, 0x4000\n"
  "buffer_load_dwordx4 a[144:147], v2, s[4:7], s21 offen sc1\n"
  "buffer_load_dwordx4 a[148:151], v2, s[4:7], s21 offen offset:256 sc1\n"
  "buffer_load_dwordx4 a[152:155], v2, s[4:7], s21 offen offset:512 sc1\n"
  "buffer_load_dwordx4 a[156:159], v2, s[4:7], s21 offen offset:768 sc1\n"
  "s_add_u32 s21, s21, 0x4000\n"
  "buffer_load_dwordx4 a[160:163], v2, s[4:7], s21 offen sc1\n"
  "buffer_load_dwordx4 a[164:167], v2, s[4:7], s21 offen offset:256 sc1\n"
  "buffer_load_dwordx4 a[168:171], v2, s[4:7], s21 offen offset:512 sc1\n"
  "buffer_load_dwordx4 a[172:175], v2, s[4:7], s21 offen offset:768 sc1\n"
  "s_add_u32 s21, s21, 0x4000\n"
  "s_and_b32 s29, s26, 3\n"
  "s_lshl_b32 s29, s29, 8\n"
  "s_add_u32 s29, s29, 0x1800\n"
  "v_min_u32 v54, 63, v1\n"
  "v_lshlrev_b32 v54, 2, v54\n"
  "v_add_u32 v54, s29, v54\n"
  "global_load_dword v59, v54, s[80:81] sc1\n"
  "s_waitcnt vmcnt(25)\n"
  "s_mov_b32 s20, 0\n"
  "s_waitcnt lgkmcnt(0)\n"
  "s_swappc_b64 s[48:49], s[52:53]\n"
  "s_nop 0\n"
  "v_cmp_ne_u32 vcc, s90, v59\n"
  "s_nop 1\n"
  "s_cmp_eq_u64 vcc, 0\n"
  "s_cbranch_scc1 FLAGOK_4_%=\n"
  "s_and_b32 s29, s26, 3\n"
  "s_lshl_b32 s29, s29, 8\n"
  "s_add_u32 s29, s29, 0x1800\n"
  "v_min_u32 v56, 63, v1\n"
  "v_lshlrev_b32 v56, 2, v56\n"
  "v_add_u32 v56, s29, v56\n"
  "s_mov_b32 s63, 0x30d40\n"
  "POLL_5_%=:\n"
  "global_load_dword v57, v56, s[80:81] sc1\n"
  "s_waitcnt vmcnt(0)\n"
  "v_cmp_ne_u32 vcc, s90, v57\n"
  "s_nop 1\n"
  "s_cmp_eq_u64 vcc, 0\n"
  "s_cbranch_scc1 POLLOK_6_%=\n"
  "s_sleep 1\n"
  "s_sub_u32 s63, s63, 1\n"
  "s_cmp_lg_u32 s63, 0\n"
  "s_cbranch_scc1 POLL_5_%=\n"
  "POLLOK_6_%=:\n"
  "FLAGOK_4_%=:\n"
  "s_add_u32 m0, s24, 0x10000\n"
  "s_nop 0\n"
  "buffer_load_dwordx4 v15, s[16:19], s24 offen sc1 lds\n"
  "buffer_load_dwordx4 v15, s[16:19], s24 offen offset:1024 sc1 lds\n"
  "buffer_load_dwordx4 v15, s[16:19], s24 offen offset:2048 sc1 lds\n"
  "buffer_load_dwordx4 v15, s[16:19], s24 offen offset:3072 sc1 lds\n"
  "s_mov_b32 s20, 1\n"
  "s_waitcnt lgkmcnt(0)\n"
  "s_swappc_b64 s[48:49], s[54:55]\n"
  "s_nop 0\n"
  "s_add_u32 m0, s24, 0x1c000\n"
  "s_nop 0\n"
  "buffer_load_dwordx4 v15, s[56:59], s24 offen sc1 lds\n"
  "buffer_load_dwordx4 v15, s[56:59], s24 offen offset:1024 sc1 lds\n"
  "buffer_load_dwordx4 v15, s[56:59], s24 offen offset:2048 sc1 lds\n"
  "buffer_load_dwordx4 v15, s[56:59], s24 offen offset:3072 sc1 lds\n"
  "s_mov_b32 s20, 1\n"
  "s_waitcnt lgkmcnt(0)\n"
  "s_swappc_b64 s[48:49], s[54:55]\n"
  "s_nop 0\n"
  "s_add_u32 s29, s24, 0x4000\n"
  "s_add_u32 m0, s24, 0x14000\n"
  "s_nop 0\n"
  "buffer_load_dwordx4 v15, s[16:19], s29 offen sc1 lds\n"
  "buffer_load_dwordx4 v15, s[16:19], s29 offen offset:1024 sc1 lds\n"
  "buffer_load_dwordx4 v15, s[16:19], s29 offen offset:2048 sc1 lds\n"
  "buffer_load_dwordx4 v15, s[16:19], s29 offen offset:3072 sc1 lds\n"
  "s_mov_b32 s20, 1\n"
  "s_waitcnt lgkmcnt(0)\n"
  "s_swappc_b64 s[48:49], s[54:55]\n"
  "s_nop 0\n"
  "s_mov_b32 s82, 0x3e0293ee\n"
  "s_mov_b32 s83, 0x3e0293ee\n"
  "v_pk_mul_f32 v[128:129], v[128:129], s[82:83]\n"
  "v_pk_mul_f32 v[130:131], v[130:131], s[82:83]\n"
  "v_pk_mul_f32 v[132:133], v[132:133], s[82:83]\n"
  "v_pk_mul_f32 v[134:135], v[134:135], s[82:83]\n"
  "v_pk_mul_f32 v[136:137], v[136:137], s[82:83]\n"
  "v_pk_mul_f32 v[138:139], v[138:139], s[82:83]\n"
  "v_pk_mul_f32 v[140:141], v[140:141], s[82:83]\n"
  "v_pk_mul_f32 v[142:143], v[142:143], s[82:83]\n"
  "v_cvt_pk_bf16_f32 v64, v128, v129\n"
  "v_cvt_pk_bf16_f32 v65, v130, v131\n"
  "v_cvt_pk_bf16_f32 v80, v132, v133\n"
  "v_cvt_pk_bf16_f32 v81, v134, v135\n"
  "v_cvt_pk_bf16_f32 v96, v136, v137\n"
  "v_cvt_pk_bf16_f32 v97, v138, v139\n"
  "v_cvt_pk_bf16_f32 v112, v140, v141\n"
  "v_cvt_pk_bf16_f32 v113, v142, v143\n"
  "v_pk_mul_f32 v[144:145], v[144:145], s[82:83]\n"
  "v_pk_mul_f32 v[146:147], v[146:147], s[82:83]\n"
  "v_pk_mul_f32 v[148:149], v[148:149], s[82:83]\n"
  "v_pk_mul_f32 v[150:151], v[150:151], s[82:83]\n"
  "v_pk_mul_f32 v[152:153], v[152:153], s[82:83]\n"
  "v_pk_mul_f32 v[154:155], v[154:155], s[82:83]\n"
  "v_pk_mul_f32 v[156:157], v[156:157], s[82:83]\n"
  "v_pk_mul_f32 v[158:159], v[158:159], s[82:83]\n"
  "v_cvt_pk_bf16_f32 v66, v144, v145\n"
  "v_cvt_pk_bf16_f32 v67, v146, v147\n"
  "v_cvt_pk_bf16_f32 v82, v148, v149\n"
  "v_cvt_pk_bf16_f32 v83, v150, v151\n"
  "v_cvt_pk_bf16_f32 v98, v152, v153\n"
  "v_cvt_pk_bf16_f32 v99, v154, v155\n"
  "v_cvt_pk_bf16_f32 v114, v156, v157\n"
  "v_cvt_pk_bf16_f32 v115, v158, v159\n"
  "v_pk_mul_f32 v[160:161], v[160:161], s[82:83]\n"
  "v_pk_mul_f32 v[162:163], v[162:163], s[82:83]\n"
  "v_pk_mul_f32 v[164:165], v[164:165], s[82:83]\n"
  "v_pk_mul_f32 v[166:167], v[166:167], s[82:83]\n"
  "v_pk_mul_f32 v[168:169], v[168:169], s[82:83]\n"
  "v_pk_mul_f32 v[170:171], v[170:171], s[82:83]\n"
  "v_pk_mul_f32 v[172:173], v[172:173], s[82:83]\n"
  "v_pk_mul_f32 v[174:175], v[174:175], s[82:83]\n"
  "v_cvt_pk_bf16_f32 v68, v160, v161\n"
  "v_cvt_pk_bf16_f32 v69, v162, v163\n"
  "v_cvt_pk_bf16_f32 v84, v164, v165\n"
  "v_cvt_pk_bf16_f32 v85, v166, v167\n"
  "v_cvt_pk_bf16_f32 v100, v168, v169\n"
  "v_cvt_pk_bf16_f32 v101, v170, v171\n"
  "v_cvt_pk_bf16_f32 v116, v172, v173\n"
  "v_cvt_pk_bf16_f32 v117, v174, v175\n"
  "v_pk_mul_f32 v[176:177], v[176:177], s[82:83]\n"
  "v_pk_mul_f32 v[178:179], v[178:179], s[82:83]\n"
  "v_pk_mul_f32 v[180:181], v[180:181], s[82:83]\n"
  "v_pk_mul_f32 v[182:183], v[182:183], s[82:83]\n"
  "v_pk_mul_f32 v[184:185], v[184:185], s[82:83]\n"
  "v_pk_mul_f32 v[186:187], v[186:187], s[82:83]\n"
  "v_pk_mul_f32 v[188:189], v[188:189], s[82:83]\n"
  "v_pk_mul_f32 v[190:191], v[190:191], s[82:83]\n"
  "v_cvt_pk_bf16_f32 v70, v176, v177\n"
  "v_cvt_pk_bf16_f32 v71, v178, v179\n"
  "v_cvt_pk_bf16_f32 v86, v180, v181\n"
  "v_cvt_pk_bf16_f32 v87, v182, v183\n"
  "v_cvt_pk_bf16_f32 v102, v184, v185\n"
  "v_cvt_pk_bf16_f32 v103, v186, v187\n"
  "v_cvt_pk_bf16_f32 v118, v188, v189\n"
  "v_cvt_pk_bf16_f32 v119, v190, v191\n"
  "v_pk_mul_f32 v[192:193], v[192:193], s[82:83]\n"
  "v_pk_mul_f32 v[194:195], v[194:195], s[82:83]\n"
  "v_pk_mul_f32 v[196:197], v[196:197], s[82:83]\n"
  "v_pk_mul_f32 v[198:199], v[198:199], s[82:83]\n"
  "v_pk_mul_f32 v[200:201], v[200:201], s[82:83]\n"
  "v_pk_mul_f32 v[202:203], v[202:203], s[82:83]\n"
  "v_pk_mul_f32 v[204:205], v[204:205], s[82:83]\n"
  "v_pk_mul_f32 v[206:207], v[206:207], s[82:83]\n"
  "v_cvt_pk_bf16_f32 v72, v192, v193\n"
  "v_cvt_pk_bf16_f32 v73, v194, v195\n"
  "v_cvt_pk_bf16_f32 v88, v196, v197\n"
  "v_cvt_pk_bf16_f32 v89, v198, v199\n"
  "v_cvt_pk_bf16_f32 v104, v200, v201\n"
  "v_cvt_pk_bf16_f32 v105, v202, v203\n"
  "v_cvt_pk_bf16_f32 v120, v204, v205\n"
  "v_cvt_pk_bf16_f32 v121, v206, v207\n"
  "v_pk_mul_f32 v[208:209], v[208:209], s[82:83]\n"
  "v_pk_mul_f32 v[210:211], v[210:211], s[82:83]\n"
  "v_pk_mul_f32 v[212:213], v[212:213], s[82:83]\n"
  "v_pk_mul_f32 v[214:215], v[214:215], s[82:83]\n"
  "v_pk_mul_f32 v[216:217], v[216:217], s[82:83]\n"
  "v_pk_mul_f32 v[218:219], v[218:219], s[82:83]\n"
  "v_pk_mul_f32 v[220:221], v[220:221], s[82:83]\n"
  "v_pk_mul_f32 v[222:223], v[222:223], s[82:83]\n"
  "v_cvt_pk_bf16_f32 v74, v208, v209\n"
  "v_cvt_pk_bf16_f32 v75, v210, v211\n"
  "v_cvt_pk_bf16_f32 v90, v212, v213\n"
  "v_cvt_pk_bf16_f32 v91, v214, v215\n"
  "v_cvt_pk_bf16_f32 v106, v216, v217\n"
  "v_cvt_pk_bf16_f32 v107, v218, v219\n"
  "v_cvt_pk_bf16_f32 v122, v220, v221\n"
  "v_cvt_pk_bf16_f32 v123, v222, v223\n"
  "v_pk_mul_f32 v[224:225], v[224:225], s[82:83]\n"
  "v_pk_mul_f32 v[226:227], v[226:227], s[82:83]\n"
  "v_pk_mul_f32 v[228:229], v[228:229], s[82:83]\n"
  "v_pk_mul_f32 v[230:231], v[230:231], s[82:83]\n"
  "v_pk_mul_f32 v[232:233], v[232:233], s[82:83]\n"
  "v_pk_mul_f32 v[234:235], v[234:235], s[82:83]\n"
  "v_pk_mul_f32 v[236:237], v[236:237], s[82:83]\n"
  "v_pk_mul_f32 v[238:239], v[238:239], s[82:83]\n"
  "v_cvt_pk_bf16_f32 v76, v224, v225\n"
  "v_cvt_pk_bf16_f32 v77, v226, v227\n"
  "v_cvt_pk_bf16_f32 v92, v228, v229\n"
  "v_cvt_pk_bf16_f32 v93, v230, v231\n"
  "v_cvt_pk_bf16_f32 v108, v232, v233\n"
  "v_cvt_pk_bf16_f32 v109, v234, v235\n"
  "v_cvt_pk_bf16_f32 v124, v236, v237\n"
  "v_cvt_pk_bf16_f32 v125, v238, v239\n"
  "v_pk_mul_f32 v[240:241], v[240:241], s[82:83]\n"
  "v_pk_mul_f32 v[242:243], v[242:243], s[82:83]\n"
  "v_pk_mul_f32 v[244:245], v[244:245], s[82:83]\n"
  "v_pk_mul_f32 v[246:247], v[246:247], s[82:83]\n"
  "v_pk_mul_f32 v[248:249], v[248:249], s[82:83]\n"
  "v_pk_mul_f32 v[250:251], v[250:251], s[82:83]\n"
  "v_pk_mul_f32 v[252:253], v[252:253], s[82:83]\n"
  "v_pk_mul_f32 v[254:255], v[254:255], s[82:83]\n"
  "v_cvt_pk_bf16_f32 v78, v240, v241\n"
  "v_cvt_pk_bf16_f32 v79, v242, v243\n"
  "v_cvt_pk_bf16_f32 v94, v244, v245\n"
  "v_cvt_pk_bf16_f32 v95, v246, v247\n"
  "v_cvt_pk_bf16_f32 v110, v248, v249\n"
  "v_cvt_pk_bf16_f32 v111, v250, v251\n"
  "v_cvt_pk_bf16_f32 v126, v252, v253\n"
  "v_cvt_pk_bf16_f32 v127, v254, v255\n"
  "s_mul_i32 s29, s27, 37\n"
  "s_lshr_b32 s29, s29, 8\n"
  "s_mul_i32 s30, s29, 7\n"
  "s_sub_u32 s30, s27, s30\n"
  "s_and_b32 s31, s26, 1\n"
  "s_lshl_b32 s31, s31, 2\n"
  "s_add_u32 s34, s31, s29\n"
  "s_and_b32 s31, s26, 2\n"
  "s_add_u32 s31, s31, 1\n"
  "s_lshr_b32 s29, s26, 2\n"
  "s_sub_u32 s31, s31, s29\n"
  "s_mul_i32 s31, s31, 7\n"
  "s_add_u32 s40, s31, s30\n"
  "s_mov_b32 s99, s34\n"
  "s_lshl_b32 s29, s99, 19\n"
  "s_lshl_b32 s30, s40, 8\n"
  "s_add_u32 s4, s80, 0x100000\n"
  "s_addc_u32 s5, s81, 0\n"
  "s_add_u32 s4, s4, s29\n"
  "s_addc_u32 s5, s5, 0\n"
  "s_and_b32 s5, s5, 0xffff\n"
  "s_mov_b32 s6, 0x80000\n"
  "s_mov_b32 s7, 0x20000\n"
  "s_add_u32 s8, s70, s30\n"
  "s_addc_u32 s9, s71, 0\n"
  "s_and_b32 s9, s9, 0xffff\n"
  "s_mov_b32 s10, 0x6fe500\n"
  "s_mov_b32 s11, 0x20000\n"
  "s_mov_b32 s21, 0\n"
  "s_mul_i32 s22, s25, 0x1c000\n"
  "s_mov_b32 s23, 0x70000\n"
  "v_lshrrev_b32 v18, 4, v1\n"
  "v_bfe_u32 v19, v1, 1, 3\n"
  "v_and_b32 v20, 1, v1\n"
  "v_lshlrev_b32 v20, 4, v20\n"
  "s_mov_b32 s36, 0x1c00\n"
  "v_add_u32 v21, 0, v18\n"
  "v_xor_b32 v21, v21, v19\n"
  "v_lshl_add_u32 v21, v21, 5, v20\n"
  "v_add_u32 v22, 0, v18\n"
  "v_mul_lo_u32 v22, v22, s36\n"
  "v_add_u32 v3, v22, v21\n"
  "v_add_u32 v21, 4, v18\n"
  "v_xor_b32 v21, v21, v19\n"
  "v_lshl_add_u32 v21, v21, 5, v20\n"
  "v_add_u32 v22, 4, v18\n"
  "v_mul_lo_u32 v22, v22, s36\n"
  "v_add_u32 v4, v22, v21\n"
  "v_subrev_u32 v4, 0x400, v4\n"
  "v_add_u32 v21, 0, v18\n"
  "v_xor_b32 v21, v21, v19\n"
  "v_lshl_add_u32 v21, v21, 5, v20\n"
  "v_add_u32 v22, 8, v18\n"
  "v_mul_lo_u32 v22, v22, s36\n"
  "v_add_u32 v5, v22, v21\n"
  "v_subrev_u32 v5, 0x800, v5\n"
  "v_add_u32 v21, 4, v18\n"
  "v_xor_b32 v21, v21, v19\n"
  "v_lshl_add_u32 v21, v21, 5, v20\n"
  "v_add_u32 v22, 12, v18\n"
  "v_mul_lo_u32 v22, v22, s36\n"
  "v_add_u32 v6, v22, v21\n"
  "v_subrev_u32 v6, 0xc00, v6\n"
  "s_cmp_ge_u32 s27, 28\n"
  "s_cselect_b32 s6, 0, s6\n"
  "s_cselect_b32 s10, 0, s10\n"
  "s_mov_b32 s94, 0\n"
  "v_mov_b32 v26, 0xff800000\n"
  "v_mov_b32 v27, 0xff800000\n"
  "v_mov_b32 v28, 0xff800000\n"
  "v_mov_b32 v29, 0xff800000\n"
  "ATT_START_%=:\n"
  "v_add_u32 v16, 0x10000, v15\n"
  "v_add_u32 v17, 0x1c000, v15\n"
  "s_add_u32 s95, s24, 0x10000\n"
  "s_add_u32 s96, s24, 0x14000\n"
  "s_add_u32 s97, s24, 0x18000\n"
  "s_add_u32 s60, s24, 0x1c000\n"
  "s_add_u32 s61, s24, 0x20000\n"
  "s_add_u32 s62, s24, 0x24000\n"
  "s_add_u32 s46, s24, 0xc000\n"
  "s_add_u32 s47, s24, 0xc000\n"
  "s_mov_b32 s12, s70\n"
  "s_and_b32 s13, s71, 0xffff\n"
  "s_mov_b32 s14, 0x700000\n"
  "s_mov_b32 s15, 0x20000\n"
  "s_and_b32 s30, s69, 0xffff\n"
  "s_and_b32 s31, s75, 0xffff\n"
  "s_mov_b32 s50, 0\n"
  "ds_read_b128 a[0:3], v16\n"
  "ds_read_b128 a[4:7], v16 offset:1024\n"
  "ds_read_b128 a[8:11], v16 offset:2048\n"
  "ds_read_b128 a[12:15], v16 offset:3072\n"
  "ds_read_b128 a[16:19], v16 offset:4096\n"
  "ds_read_b128 a[20:23], v16 offset:5120\n"
  "ds_read_b128 a[24:27], v16 offset:6144\n"
  "ds_read_b128 a[28:31], v16 offset:7168\n"
  "s_add_u32 s34, s24, 0x8000\n"
  "s_add_u32 s35, s24, 0x4000\n"
  "s_waitcnt lgkmcnt(7)\n"
  "v_mfma_f32_16x16x32_bf16 v[128:131], a[0:3], v[64:67], 0\n"
  "v_mfma_f32_16x16x32_bf16 v[132:135], a[0:3], v[80:83], 0\n"
  "s_add_u32 m0, s24, 0x18000\n"
  "v_mfma_f32_16x16x32_bf16 v[136:139], a[0:3], v[96:99], 0\n"
  "buffer_load_dwordx4 v15, s[16:19], s34 offen sc1 lds\n"
  "v_mfma_f32_16x16x32_bf16 v[140:143], a[0:3], v[112:115], 0\n"
  "s_waitcnt lgkmcnt(6)\n"
  "v_mfma_f32_16x16x32_bf16 v[128:131], a[4:7], v[68:71], v[128:131]\n"
  "v_mfma_f32_16x16x32_bf16 v[132:135], a[4:7], v[84:87], v[132:135]\n"
  "v_mfma_f32_16x16x32_bf16 v[136:139], a[4:7], v[100:103], v[136:139]\n"
  "buffer_load_dwordx4 v15, s[16:19], s34 offen offset:1024 sc1 lds\n"
  "v_mfma_f32_16x16x32_bf16 v[140:143], a[4:7], v[116:119], v[140:143]\n"
  "s_waitcnt lgkmcnt(5)\n"
  "v_mfma_f32_16x16x32_bf16 v[128:131], a[8:11], v[72:75], v[128:131]\n"
  "v_mfma_f32_16x16x32_bf16 v[132:135], a[8:11], v[88:91], v[132:135]\n"
  "v_mfma_f32_16x16x32_bf16 v[136:139], a[8:11], v[104:107], v[136:139]\n"
  "buffer_load_dwordx4 v15, s[16:19], s34 offen offset:2048 sc1 lds\n"
  "v_mfma_f32_16x16x32_bf16 v[140:143], a[8:11], v[120:123], v[140:143]\n"
  "s_waitcnt lgkmcnt(4)\n"
  "v_mfma_f32_16x16x32_bf16 v[128:131], a[12:15], v[76:79], v[128:131]\n"
  "v_mfma_f32_16x16x32_bf16 v[132:135], a[12:15], v[92:95], v[132:135]\n"
  "v_mfma_f32_16x16x32_bf16 v[136:139], a[12:15], v[108:111], v[136:139]\n"
  "buffer_load_dwordx4 v15, s[16:19], s34 offen offset:3072 sc1 lds\n"
  "v_mfma_f32_16x16x32_bf16 v[140:143], a[12:15], v[124:127], v[140:143]\n"
  "s_waitcnt lgkmcnt(3)\n"
  "v_mfma_f32_16x16x32_bf16 v[144:147], a[16:19], v[64:67], 0\n"
  "v_mfma_f32_16x16x32_bf16 v[148:151], a[16:19], v[80:83], 0\n"
  "s_add_u32 m0, s24, 0x20000\n"
  "v_mfma_f32_16x16x32_bf16 v[152:155], a[16:19], v[96:99], 0\n"
  "buffer_load_dwordx4 v15, s[56:59], s35 offen sc1 lds\n"
  "v_mfma_f32_16x16x32_bf16 v[156:159], a[16:19], v[112:115], 0\n"
  "s_waitcnt lgkmcnt(2)\n"
  "v_mfma_f32_16x16x32_bf16 v[144:147], a[20:23], v[68:71], v[144:147]\n"
  "v_mfma_f32_16x16x32_bf16 v[148:151], a[20:23], v[84:87], v[148:151]\n"
  "v_mfma_f32_16x16x32_bf16 v[152:155], a[20:23], v[100:103], v[152:155]\n"
  "buffer_load_dwordx4 v15, s[56:59], s35 offen offset:1024 sc1 lds\n"
  "v_mfma_f32_16x16x32_bf16 v[156:159], a[20:23], v[116:119], v[156:159]\n"
  "s_waitcnt lgkmcnt(1)\n"
  "v_mfma_f32_16x16x32_bf16 v[144:147], a[24:27], v[72:75], v[144:147]\n"
  "v_mfma_f32_16x16x32_bf16 v[148:151], a[24:27], v[88:91], v[148:151]\n"
  "v_mfma_f32_16x16x32_bf16 v[152:155], a[24:27], v[104:107], v[152:155]\n"
  "buffer_load_dwordx4 v15, s[56:59], s35 offen offset:2048 sc1 lds\n"
  "v_mfma_f32_16x16x32_bf16 v[156:159], a[24:27], v[120:123], v[156:159]\n"
  "s_waitcnt lgkmcnt(0)\n"
  "v_mfma_f32_16x16x32_bf16 v[144:147], a[28:31], v[76:79], v[144:147]\n"
  "v_mfma_f32_16x16x32_bf16 v[148:151], a[28:31], v[92:95], v[148:151]\n"
  "v_mfma_f32_16x16x32_bf16 v[152:155], a[28:31], v[108:111], v[152:155]\n"
  "buffer_load_dwordx4 v15, s[56:59], s35 offen offset:3072 sc1 lds\n"
  "v_mfma_f32_16x16x32_bf16 v[156:159], a[28:31], v[124:127], v[156:159]\n"
  "v_max3_f32 v30, v128, v129, v130\n"
  "s_nop 2\n"
  "v_max3_f32 v30, v30, v131, v144\n"
  "v_max3_f32 v30, v30, v145, v146\n"
  "v_max3_f32 v30, v30, v147, v26\n"
  "v_max3_f32 v31, v132, v133, v134\n"
  "v_max3_f32 v31, v31, v135, v148\n"
  "v_max3_f32 v31, v31, v149, v150\n"
  "v_max3_f32 v31, v31, v151, v27\n"
  "v_max3_f32 v32, v136, v137, v138\n"
  "v_max3_f32 v32, v32, v139, v152\n"
  "v_max3_f32 v32, v32, v153, v154\n"
  "v_max3_f32 v32, v32, v155, v28\n"
  "v_max3_f32 v33, v140, v141, v142\n"
  "v_max3_f32 v33, v33, v143, v156\n"
  "v_max3_f32 v33, v33, v157, v158\n"
  "v_max3_f32 v33, v33, v159, v29\n"
  "ds_read_b128 a[0:3], v16 offset:8192\n"
  "ds_read_b128 a[4:7], v16 offset:9216\n"
  "ds_read_b128 a[8:11], v16 offset:10240\n"
  "ds_read_b128 a[12:15], v16 offset:11264\n"
  "ds_read_b128 a[16:19], v16 offset:12288\n"
  "ds_read_b128 a[20:23], v16 offset:13312\n"
  "ds_read_b128 a[24:27], v16 offset:14336\n"
  "ds_read_b128 a[28:31], v16 offset:15360\n"
  "v_mov_b32 v36, v30\n"
  "s_nop 1\n"
  "v_permlane32_swap_b32 v30, v36\n"
  "v_max_f32 v30, v30, v36\n"
  "v_mov_b32 v36, v30\n"
  "s_nop 1\n"
  "v_permlane16_swap_b32 v30, v36\n"
  "v_max_f32 v30, v30, v36\n"
  "v_mov_b32 v37, v31\n"
  "s_nop 1\n"
  "v_permlane32_swap_b32 v31, v37\n"
  "v_max_f32 v31, v31, v37\n"
  "v_mov_b32 v37, v31\n"
  "s_nop 1\n"
  "v_permlane16_swap_b32 v31, v37\n"
  "v_max_f32 v31, v31, v37\n"
  "v_mov_b32 v38, v32\n"
  "s_nop 1\n"
  "v_permlane32_swap_b32 v32, v38\n"
  "v_max_f32 v32, v32, v38\n"
  "v_mov_b32 v38, v32\n"
  "s_nop 1\n"
  "v_permlane16_swap_b32 v32, v38\n"
  "v_max_f32 v32, v32, v38\n"
  "v_mov_b32 v39, v33\n"
  "s_nop 1\n"
  "v_permlane32_swap_b32 v33, v39\n"
  "v_max_f32 v33, v33, v39\n"
  "v_mov_b32 v39, v33\n"
  "s_nop 1\n"
  "v_permlane16_swap_b32 v33, v39\n"
  "v_max_f32 v33, v33, v39\n"
  "v_mul_f32 v224, -1.0, v30\n"
  "v_mul_f32 v225, -1.0, v30\n"
  "v_mul_f32 v226, -1.0, v30\n"
  "v_mul_f32 v227, -1.0, v30\n"
  "v_mul_f32 v228, -1.0, v31\n"
  "v_mul_f32 v229, -1.0, v31\n"
  "v_mul_f32 v230, -1.0, v31\n"
  "v_mul_f32 v231, -1.0, v31\n"
  "v_mul_f32 v232, -1.0, v32\n"
  "v_mul_f32 v233, -1.0, v32\n"
  "v_mul_f32 v234, -1.0, v32\n"
  "v_mul_f32 v235, -1.0, v32\n"
  "v_mul_f32 v236, -1.0, v33\n"
  "v_mul_f32 v237, -1.0, v33\n"
  "v_mul_f32 v238, -1.0, v33\n"
  "v_mul_f32 v239, -1.0, v33\n"
  "v_add_f32 v128, v128, v224\n"
  "v_add_f32 v129, v129, v225\n"
  "v_add_f32 v130, v130, v226\n"
  "v_add_f32 v131, v131, v227\n"
  "v_add_f32 v132, v132, v228\n"
  "v_add_f32 v133, v133, v229\n"
  "v_add_f32 v134, v134, v230\n"
  "v_add_f32 v135, v135, v231\n"
  "v_add_f32 v136, v136, v232\n"
  "v_add_f32 v137, v137, v233\n"
  "v_add_f32 v138, v138, v234\n"
  "v_add_f32 v139, v139, v235\n"
  "v_add_f32 v140, v140, v236\n"
  "v_add_f32 v141, v141, v237\n"
  "v_add_f32 v142, v142, v238\n"
  "v_add_f32 v143, v143, v239\n"
  "v_add_f32 v144, v144, v224\n"
  "v_add_f32 v145, v145, v225\n"
  "v_add_f32 v146, v146, v226\n"
  "v_add_f32 v147, v147, v227\n"
  "v_add_f32 v148, v148, v228\n"
  "v_add_f32 v149, v149, v229\n"
  "v_add_f32 v150, v150, v230\n"
  "v_add_f32 v151, v151, v231\n"
  "v_add_f32 v152, v152, v232\n"
  "v_add_f32 v153, v153, v233\n"
  "v_add_f32 v154, v154, v234\n"
  "v_add_f32 v155, v155, v235\n"
  "v_add_f32 v156, v156, v236\n"
  "v_add_f32 v157, v157, v237\n"
  "v_add_f32 v158, v158, v238\n"
  "v_add_f32 v159, v159, v239\n"
  "v_exp_f32 v128, v128\n"
  "v_exp_f32 v129, v129\n"
  "v_exp_f32 v130, v130\n"
  "v_exp_f32 v131, v131\n"
  "v_cvt_pk_bf16_f32 v192, v128, v129\n"
  "v_cvt_pk_bf16_f32 v193, v130, v131\n"
  "v_exp_f32 v132, v132\n"
  "v_exp_f32 v133, v133\n"
  "v_exp_f32 v134, v134\n"
  "v_exp_f32 v135, v135\n"
  "v_cvt_pk_bf16_f32 v196, v132, v133\n"
  "v_cvt_pk_bf16_f32 v197, v134, v135\n"
  "v_exp_f32 v136, v136\n"
  "v_exp_f32 v137, v137\n"
  "v_exp_f32 v138, v138\n"
  "v_exp_f32 v139, v139\n"
  "v_cvt_pk_bf16_f32 v200, v136, v137\n"
  "v_cvt_pk_bf16_f32 v201, v138, v139\n"
  "v_exp_f32 v140, v140\n"
  "v_exp_f32 v141, v141\n"
  "v_exp_f32 v142, v142\n"
  "v_exp_f32 v143, v143\n"
  "v_cvt_pk_bf16_f32 v204, v140, v141\n"
  "v_cvt_pk_bf16_f32 v205, v142, v143\n"
  "s_waitcnt vmcnt(0) lgkmcnt(0)\n"
  "s_barrier\n"
  "s_sub_u32 s44, s96, s95\n"
  "v_add_u32 v16, s44, v16\n"
  "v_mfma_f32_16x16x32_bf16 v[160:163], a[0:3], v[64:67], v[224:227]\n"
  "v_exp_f32 v144, v144\n"
  "v_mfma_f32_16x16x32_bf16 v[164:167], a[0:3], v[80:83], v[228:231]\n"
  "v_exp_f32 v145, v145\n"
  "v_mfma_f32_16x16x32_bf16 v[168:171], a[0:3], v[96:99], v[232:235]\n"
  "v_exp_f32 v146, v146\n"
  "v_mfma_f32_16x16x32_bf16 v[172:175], a[0:3], v[112:115], v[236:239]\n"
  "v_exp_f32 v147, v147\n"
  "v_mfma_f32_16x16x32_bf16 v[160:163], a[4:7], v[68:71], v[160:163]\n"
  "ds_read_b128 a[32:35], v17\n"
  "v_cvt_pk_bf16_f32 v194, v144, v145\n"
  "v_mfma_f32_16x16x32_bf16 v[164:167], a[4:7], v[84:87], v[164:167]\n"
  "ds_read_b128 a[36:39], v17 offset:2048\n"
  "v_cvt_pk_bf16_f32 v195, v146, v147\n"
  "v_mfma_f32_16x16x32_bf16 v[168:171], a[4:7], v[100:103], v[168:171]\n"
  "v_exp_f32 v148, v148\n"
  "v_mfma_f32_16x16x32_bf16 v[172:175], a[4:7], v[116:119], v[172:175]\n"
  "v_exp_f32 v149, v149\n"
  "v_mfma_f32_16x16x32_bf16 v[160:163], a[8:11], v[72:75], v[160:163]\n"
  "v_exp_f32 v150, v150\n"
  "v_mfma_f32_16x16x32_bf16 v[164:167], a[8:11], v[88:91], v[164:167]\n"
  "v_exp_f32 v151, v151\n"
  "v_mfma_f32_16x16x32_bf16 v[168:171], a[8:11], v[104:107], v[168:171]\n"
  "ds_read_b128 a[40:43], v17 offset:4096\n"
  "v_cvt_pk_bf16_f32 v198, v148, v149\n"
  "v_mfma_f32_16x16x32_bf16 v[172:175], a[8:11], v[120:123], v[172:175]\n"
  "ds_read_b128 a[44:47], v17 offset:6144\n"
  "v_cvt_pk_bf16_f32 v199, v150, v151\n"
  "v_mfma_f32_16x16x32_bf16 v[160:163], a[12:15], v[76:79], v[160:163]\n"
  "v_exp_f32 v152, v152\n"
  "v_mfma_f32_16x16x32_bf16 v[164:167], a[12:15], v[92:95], v[164:167]\n"
  "v_exp_f32 v153, v153\n"
  "v_mfma_f32_16x16x32_bf16 v[168:171], a[12:15], v[108:111], v[168:171]\n"
  "v_exp_f32 v154, v154\n"
  "v_mfma_f32_16x16x32_bf16 v[172:175], a[12:15], v[124:127], v[172:175]\n"
  "v_exp_f32 v155, v155\n"
  "v_mfma_f32_16x16x32_bf16 v[176:179], a[16:19], v[64:67], v[224:227]\n"
  "ds_read_b128 a[48:51], v17 offset:8192\n"
  "v_cvt_pk_bf16_f32 v202, v152, v153\n"
  "v_mfma_f32_16x16x32_bf16 v[180:183], a[16:19], v[80:83], v[228:231]\n"
  "ds_read_b128 a[52:55], v17 offset:10240\n"
  "v_cvt_pk_bf16_f32 v203, v154, v155\n"
  "v_mfma_f32_16x16x32_bf16 v[184:187], a[16:19], v[96:99], v[232:235]\n"
  "v_exp_f32 v156, v156\n"
  "v_mfma_f32_16x16x32_bf16 v[188:191], a[16:19], v[112:115], v[236:239]\n"
  "v_exp_f32 v157, v157\n"
  "v_mfma_f32_16x16x32_bf16 v[176:179], a[20:23], v[68:71], v[176:179]\n"
  "v_exp_f32 v158, v158\n"
  "v_mfma_f32_16x16x32_bf16 v[180:183], a[20:23], v[84:87], v[180:183]\n"
  "v_exp_f32 v159, v159\n"
  "v_mfma_f32_16x16x32_bf16 v[184:187], a[20:23], v[100:103], v[184:187]\n"
  "ds_read_b128 a[56:59], v17 offset:12288\n"
  "v_cvt_pk_bf16_f32 v206, v156, v157\n"
  "v_mfma_f32_16x16x32_bf16 v[188:191], a[20:23], v[116:119], v[188:191]\n"
  "ds_read_b128 a[60:63], v17 offset:14336\n"
  "v_cvt_pk_bf16_f32 v207, v158, v159\n"
  "v_mfma_f32_16x16x32_bf16 v[176:179], a[24:27], v[72:75], v[176:179]\n"
  "s_mov_b32 m0, s95\n"
  "v_mfma_f32_16x16x32_bf16 v[180:183], a[24:27], v[88:91], v[180:183]\n"
  "buffer_load_dwordx4 v15, s[16:19], s46 offen sc1 lds\n"
  "v_mfma_f32_16x16x32_bf16 v[184:187], a[24:27], v[104:107], v[184:187]\n"
  "v_mfma_f32_16x16x32_bf16 v[188:191], a[24:27], v[120:123], v[188:191]\n"
  "buffer_load_dwordx4 v15, s[16:19], s46 offen offset:1024 sc1 lds\n"
  "v_mfma_f32_16x16x32_bf16 v[176:179], a[28:31], v[76:79], v[176:179]\n"
  "v_mfma_f32_16x16x32_bf16 v[180:183], a[28:31], v[92:95], v[180:183]\n"
  "buffer_load_dwordx4 v15, s[16:19], s46 offen offset:2048 sc1 lds\n"
  "v_mfma_f32_16x16x32_bf16 v[184:187], a[28:31], v[108:111], v[184:187]\n"
  "buffer_load_dwordx4 v15, s[16:19], s46 offen offset:3072 sc1 lds\n"
  "v_mfma_f32_16x16x32_bf16 v[188:191], a[28:31], v[124:127], v[188:191]\n"
  "s_add_u32 s46, s46, 0x4000\n"
  "s_waitcnt lgkmcnt(0)\n"
  "v_mfma_f32_16x16x32_bf16 a[128:131], a[32:35], v[192:195], 0\n"
  "v_exp_f32 v160, v160\n"
  "v_mfma_f32_16x16x32_bf16 a[132:135], a[32:35], v[196:199], 0\n"
  "v_exp_f32 v161, v161\n"
  "v_mfma_f32_16x16x32_bf16 a[136:139], a[32:35], v[200:203], 0\n"
  "v_exp_f32 v162, v162\n"
  "v_mfma_f32_16x16x32_bf16 a[140:143], a[32:35], v[204:207], 0\n"
  "v_exp_f32 v163, v163\n"
  "v_mfma_f32_16x16x32_bf16 a[144:147], a[36:39], v[192:195], 0\n"
  "ds_read_b128 a[0:3], v16\n"
  "v_cvt_pk_bf16_f32 v208, v160, v161\n"
  "v_mfma_f32_16x16x32_bf16 a[148:151], a[36:39], v[196:199], 0\n"
  "ds_read_b128 a[4:7], v16 offset:1024\n"
  "v_cvt_pk_bf16_f32 v209, v162, v163\n"
  "v_mfma_f32_16x16x32_bf16 a[152:155], a[36:39], v[200:203], 0\n"
  "v_exp_f32 v164, v164\n"
  "v_mfma_f32_16x16x32_bf16 a[156:159], a[36:39], v[204:207], 0\n"
  "v_exp_f32 v165, v165\n"
  "v_mfma_f32_16x16x32_bf16 a[160:163], a[40:43], v[192:195], 0\n"
  "v_exp_f32 v166, v166\n"
  "v_mfma_f32_16x16x32_bf16 a[164:167], a[40:43], v[196:199], 0\n"
  "v_exp_f32 v167, v167\n"
  "v_mfma_f32_16x16x32_bf16 a[168:171], a[40:43], v[200:203], 0\n"
  "ds_read_b128 a[8:11], v16 offset:2048\n"
  "v_cvt_pk_bf16_f32 v212, v164, v165\n"
  "v_mfma_f32_16x16x32_bf16 a[172:175], a[40:43], v[204:207], 0\n"
  "ds_read_b128 a[12:15], v16 offset:3072\n"
  "v_cvt_pk_bf16_f32 v213, v166, v167\n"
  "v_mfma_f32_16x16x32_bf16 a[176:179], a[44:47], v[192:195], 0\n"
  "v_exp_f32 v168, v168\n"
  "v_mfma_f32_16x16x32_bf16 a[180:183], a[44:47], v[196:199], 0\n"
  "v_exp_f32 v169, v169\n"
  "v_mfma_f32_16x16x32_bf16 a[184:187], a[44:47], v[200:203], 0\n"
  "v_exp_f32 v170, v170\n"
  "v_mfma_f32_16x16x32_bf16 a[188:191], a[44:47], v[204:207], 0\n"
  "v_exp_f32 v171, v171\n"
  "v_mfma_f32_16x16x32_bf16 a[192:195], a[48:51], v[192:195], 0\n"
  "ds_read_b128 a[16:19], v16 offset:4096\n"
  "v_cvt_pk_bf16_f32 v216, v168, v169\n"
  "v_mfma_f32_16x16x32_bf16 a[196:199], a[48:51], v[196:199], 0\n"
  "ds_read_b128 a[20:23], v16 offset:5120\n"
  "v_cvt_pk_bf16_f32 v217, v170, v171\n"
  "v_mfma_f32_16x16x32_bf16 a[200:203], a[48:51], v[200:203], 0\n"
  "v_exp_f32 v172, v172\n"
  "v_mfma_f32_16x16x32_bf16 a[204:207], a[48:51], v[204:207], 0\n"
  "v_exp_f32 v173, v173\n"
  "v_mfma_f32_16x16x32_bf16 a[208:211], a[52:55], v[192:195], 0\n"
  "v_exp_f32 v174, v174\n"
  "v_mfma_f32_16x16x32_bf16 a[212:215], a[52:55], v[196:199], 0\n"
  "v_exp_f32 v175, v175\n"
  "v_mfma_f32_16x16x32_bf16 a[216:219], a[52:55], v[200:203], 0\n"
  "ds_read_b128 a[24:27], v16 offset:6144\n"
  "v_cvt_pk_bf16_f32 v220, v172, v173\n"
  "v_mfma_f32_16x16x32_bf16 a[220:223], a[52:55], v[204:207], 0\n"
  "ds_read_b128 a[28:31], v16 offset:7168\n"
  "v_cvt_pk_bf16_f32 v221, v174, v175\n"
  "v_mfma_f32_16x16x32_bf16 a[224:227], a[56:59], v[192:195], 0\n"
  "s_add_u32 m0, s24, 0x24000\n"
  "v_mfma_f32_16x16x32_bf16 a[228:231], a[56:59], v[196:199], 0\n"
  "buffer_load_dwordx4 v15, s[56:59], s34 offen sc1 lds\n"
  "v_mfma_f32_16x16x32_bf16 a[232:235], a[56:59], v[200:203], 0\n"
  "v_mfma_f32_16x16x32_bf16 a[236:239], a[56:59], v[204:207], 0\n"
  "buffer_load_dwordx4 v15, s[56:59], s34 offen offset:1024 sc1 lds\n"
  "v_mfma_f32_16x16x32_bf16 a[240:243], a[60:63], v[192:195], 0\n"
  "v_mfma_f32_16x16x32_bf16 a[244:247], a[60:63], v[196:199], 0\n"
  "buffer_load_dwordx4 v15, s[56:59], s34 offen offset:2048 sc1 lds\n"
  "v_mfma_f32_16x16x32_bf16 a[248:251], a[60:63], v[200:203], 0\n"
  "v_mfma_f32_16x16x32_bf16 a[252:255], a[60:63], v[204:207], 0\n"
  "buffer_load_dwordx4 v15, s[56:59], s34 offen offset:3072 sc1 lds\n"
  "v_mfma_f32_16x16x32_bf16 v[240:243], v[60:63], v[192:195], 0\n"
  "s_mov_b32 s37, s95\n"
  "s_mov_b32 s95, s96\n"
  "s_mov_b32 s96, s97\n"
  "v_mfma_f32_16x16x32_bf16 v[244:247], v[60:63], v[196:199], 0\n"
  "s_mov_b32 s97, s37\n"
  "v_mfma_f32_16x16x32_bf16 v[248:251], v[60:63], v[200:203], 0\n"
  "v_mfma_f32_16x16x32_bf16 v[252:255], v[60:63], v[204:207], 0\n"
  "s_waitcnt lgkmcnt(0)\n"
  "s_mov_b32 s20, 5\n"
  "ATT_LOOP_%=:\n"
  "v_mfma_f32_16x16x32_bf16 v[128:131], a[0:3], v[64:67], v[224:227]\n"
  "v_exp_f32 v176, v176\n"
  "v_mfma_f32_16x16x32_bf16 v[132:135], a[0:3], v[80:83], v[228:231]\n"
  "v_exp_f32 v177, v177\n"
  "v_mfma_f32_16x16x32_bf16 v[136:139], a[0:3], v[96:99], v[232:235]\n"
  "v_exp_f32 v178, v178\n"
  "v_mfma_f32_16x16x32_bf16 v[140:143], a[0:3], v[112:115], v[236:239]\n"
  "v_exp_f32 v179, v179\n"
  "v_mfma_f32_16x16x32_bf16 v[128:131], a[4:7], v[68:71], v[128:131]\n"
  "ds_read_b128 a[32:35], v17 offset:1024\n"
  "v_cvt_pk_bf16_f32 v210, v176, v177\n"
  "v_mfma_f32_16x16x32_bf16 v[132:135], a[4:7], v[84:87], v[132:135]\n"
  "ds_read_b128 a[36:39], v17 offset:3072\n"
  "v_cvt_pk_bf16_f32 v211, v178, v179\n"
  "v_mfma_f32_16x16x32_bf16 v[136:139], a[4:7], v[100:103], v[136:139]\n"
  "v_exp_f32 v180, v180\n"
  "v_mfma_f32_16x16x32_bf16 v[140:143], a[4:7], v[116:119], v[140:143]\n"
  "v_exp_f32 v181, v181\n"
  "v_mfma_f32_16x16x32_bf16 v[128:131], a[8:11], v[72:75], v[128:131]\n"
  "v_exp_f32 v182, v182\n"
  "v_mfma_f32_16x16x32_bf16 v[132:135], a[8:11], v[88:91], v[132:135]\n"
  "v_exp_f32 v183, v183\n"
  "v_mfma_f32_16x16x32_bf16 v[136:139], a[8:11], v[104:107], v[136:139]\n"
  "ds_read_b128 a[40:43], v17 offset:5120\n"
  "v_cvt_pk_bf16_f32 v214, v180, v181\n"
  "v_mfma_f32_16x16x32_bf16 v[140:143], a[8:11], v[120:123], v[140:143]\n"
  "ds_read_b128 a[44:47], v17 offset:7168\n"
  "v_cvt_pk_bf16_f32 v215, v182, v183\n"
  "v_mfma_f32_16x16x32_bf16 v[128:131], a[12:15], v[76:79], v[128:131]\n"
  "v_exp_f32 v184, v184\n"
  "v_mfma_f32_16x16x32_bf16 v[132:135], a[12:15], v[92:95], v[132:135]\n"
  "v_exp_f32 v185, v185\n"
  "v_mfma_f32_16x16x32_bf16 v[136:139], a[12:15], v[108:111], v[136:139]\n"
  "v_exp_f32 v186, v186\n"
  "v_mfma_f32_16x16x32_bf16 v[140:143], a[12:15], v[124:127], v[140:143]\n"
  "v_exp_f32 v187, v187\n"
  "v_mfma_f32_16x16x32_bf16 v[144:147], a[16:19], v[64:67], v[224:227]\n"
  "ds_read_b128 a[48:51], v17 offset:9216\n"
  "v_cvt_pk_bf16_f32 v218, v184, v185\n"
  "v_mfma_f32_16x16x32_bf16 v[148:151], a[16:19], v[80:83], v[228:231]\n"
  "ds_read_b128 a[52:55], v17 offset:11264\n"
  "v_cvt_pk_bf16_f32 v219, v186, v187\n"
  "v_mfma_f32_16x16x32_bf16 v[152:155], a[16:19], v[96:99], v[232:235]\n"
  "v_exp_f32 v188, v188\n"
  "v_mfma_f32_16x16x32_bf16 v[156:159], a[16:19], v[112:115], v[236:239]\n"
  "v_exp_f32 v189, v189\n"
  "v_mfma_f32_16x16x32_bf16 v[144:147], a[20:23], v[68:71], v[144:147]\n"
  "v_exp_f32 v190, v190\n"
  "v_mfma_f32_16x16x32_bf16 v[148:151], a[20:23], v[84:87], v[148:151]\n"
  "v_exp_f32 v191, v191\n"
  "v_mfma_f32_16x16x32_bf16 v[152:155], a[20:23], v[100:103], v[152:155]\n"
  "ds_read_b128 a[56:59], v17 offset:13312\n"
  "v_cvt_pk_bf16_f32 v222, v188, v189\n"
  "v_mfma_f32_16x16x32_bf16 v[156:159], a[20:23], v[116:119], v[156:159]\n"
  "ds_read_b128 a[60:63], v17 offset:15360\n"
  "v_cvt_pk_bf16_f32 v223, v190, v191\n"
  "v_mfma_f32_16x16x32_bf16 v[144:147], a[24:27], v[72:75], v[144:147]\n"
  "buffer_load_dwordx4 v[48:51], v15, s[12:15], s51 offen\n"
  "v_mfma_f32_16x16x32_bf16 v[148:151], a[24:27], v[88:91], v[148:151]\n"
  "v_mfma_f32_16x16x32_bf16 v[152:155], a[24:27], v[104:107], v[152:155]\n"
  "buffer_load_dwordx4 v[48:51], v15, s[12:15], s51 offen offset:1024\n"
  "v_mfma_f32_16x16x32_bf16 v[156:159], a[24:27], v[120:123], v[156:159]\n"
  "v_mfma_f32_16x16x32_bf16 v[144:147], a[28:31], v[76:79], v[144:147]\n"
  "buffer_load_dwordx4 v[48:51], v15, s[12:15], s51 offen offset:2048\n"
  "v_mfma_f32_16x16x32_bf16 v[148:151], a[28:31], v[92:95], v[148:151]\n"
  "v_mfma_f32_16x16x32_bf16 v[152:155], a[28:31], v[108:111], v[152:155]\n"
  "buffer_load_dwordx4 v[48:51], v15, s[12:15], s51 offen offset:3072\n"
  "v_mfma_f32_16x16x32_bf16 v[156:159], a[28:31], v[124:127], v[156:159]\n"
  "s_waitcnt lgkmcnt(0)\n"
  "v_mfma_f32_16x16x32_bf16 a[128:131], a[32:35], v[208:211], a[128:131]\n"
  "v_exp_f32 v128, v128\n"
  "v_mfma_f32_16x16x32_bf16 a[132:135], a[32:35], v[212:215], a[132:135]\n"
  "v_exp_f32 v129, v129\n"
  "v_mfma_f32_16x16x32_bf16 a[136:139], a[32:35], v[216:219], a[136:139]\n"
  "v_exp_f32 v130, v130\n"
  "v_mfma_f32_16x16x32_bf16 a[140:143], a[32:35], v[220:223], a[140:143]\n"
  "v_exp_f32 v131, v131\n"
  "v_mfma_f32_16x16x32_bf16 a[144:147], a[36:39], v[208:211], a[144:147]\n"
  "ds_read_b128 a[0:3], v16 offset:8192\n"
  "v_cvt_pk_bf16_f32 v192, v128, v129\n"
  "v_mfma_f32_16x16x32_bf16 a[148:151], a[36:39], v[212:215], a[148:151]\n"
  "ds_read_b128 a[4:7], v16 offset:9216\n"
  "v_cvt_pk_bf16_f32 v193, v130, v131\n"
  "v_mfma_f32_16x16x32_bf16 a[152:155], a[36:39], v[216:219], a[152:155]\n"
  "v_exp_f32 v132, v132\n"
  "v_mfma_f32_16x16x32_bf16 a[156:159], a[36:39], v[220:223], a[156:159]\n"
  "v_exp_f32 v133, v133\n"
  "v_mfma_f32_16x16x32_bf16 a[160:163], a[40:43], v[208:211], a[160:163]\n"
  "v_exp_f32 v134, v134\n"
  "v_mfma_f32_16x16x32_bf16 a[164:167], a[40:43], v[212:215], a[164:167]\n"
  "v_exp_f32 v135, v135\n"
  "v_mfma_f32_16x16x32_bf16 a[168:171], a[40:43], v[216:219], a[168:171]\n"
  "ds_read_b128 a[8:11], v16 offset:10240\n"
  "v_cvt_pk_bf16_f32 v196, v132, v133\n"
  "v_mfma_f32_16x16x32_bf16 a[172:175], a[40:43], v[220:223], a[172:175]\n"
  "ds_read_b128 a[12:15], v16 offset:11264\n"
  "v_cvt_pk_bf16_f32 v197, v134, v135\n"
  "v_mfma_f32_16x16x32_bf16 a[176:179], a[44:47], v[208:211], a[176:179]\n"
  "v_exp_f32 v136, v136\n"
  "v_mfma_f32_16x16x32_bf16 a[180:183], a[44:47], v[212:215], a[180:183]\n"
  "v_exp_f32 v137, v137\n"
  "v_mfma_f32_16x16x32_bf16 a[184:187], a[44:47], v[216:219], a[184:187]\n"
  "v_exp_f32 v138, v138\n"
  "v_mfma_f32_16x16x32_bf16 a[188:191], a[44:47], v[220:223], a[188:191]\n"
  "v_exp_f32 v139, v139\n"
  "v_mfma_f32_16x16x32_bf16 a[192:195], a[48:51], v[208:211], a[192:195]\n"
  "ds_read_b128 a[16:19], v16 offset:12288\n"
  "v_cvt_pk_bf16_f32 v200, v136, v137\n"
  "v_mfma_f32_16x16x32_bf16 a[196:199], a[48:51], v[212:215], a[196:199]\n"
  "ds_read_b128 a[20:23], v16 offset:13312\n"
  "v_cvt_pk_bf16_f32 v201, v138, v139\n"
  "v_mfma_f32_16x16x32_bf16 a[200:203], a[48:51], v[216:219], a[200:203]\n"
  "v_exp_f32 v140, v140\n"
  "v_mfma_f32_16x16x32_bf16 a[204:207], a[48:51], v[220:223], a[204:207]\n"
  "v_exp_f32 v141, v141\n"
  "v_mfma_f32_16x16x32_bf16 a[208:211], a[52:55], v[208:211], a[208:211]\n"
  "v_exp_f32 v142, v142\n"
  "v_mfma_f32_16x16x32_bf16 a[212:215], a[52:55], v[212:215], a[212:215]\n"
  "v_exp_f32 v143, v143\n"
  "v_mfma_f32_16x16x32_bf16 a[216:219], a[52:55], v[216:219], a[216:219]\n"
  "ds_read_b128 a[24:27], v16 offset:14336\n"
  "v_cvt_pk_bf16_f32 v204, v140, v141\n"
  "v_mfma_f32_16x16x32_bf16 a[220:223], a[52:55], v[220:223], a[220:223]\n"
  "ds_read_b128 a[28:31], v16 offset:15360\n"
  "v_cvt_pk_bf16_f32 v205, v142, v143\n"
  "v_mfma_f32_16x16x32_bf16 a[224:227], a[56:59], v[208:211], a[224:227]\n"
  "s_add_u32 s50, s50, 1\n"
  "s_xor_b32 s51, s51, 0x4000\n"
  "s_cmp_eq_u32 s50, 2\n"
  "v_mfma_f32_16x16x32_bf16 a[228:231], a[56:59], v[212:215], a[228:231]\n"
  "s_cselect_b32 s12, s68, s12\n"
  "s_cselect_b32 s13, s30, s13\n"
  "s_cselect_b32 s14, 0x800000, s14\n"
  "v_mfma_f32_16x16x32_bf16 a[232:235], a[56:59], v[216:219], a[232:235]\n"
  "s_cmp_eq_u32 s50, 4\n"
  "s_cselect_b32 s12, s74, s12\n"
  "s_cselect_b32 s13, s31, s13\n"
  "v_mfma_f32_16x16x32_bf16 a[236:239], a[56:59], v[220:223], a[236:239]\n"
  "s_cselect_b32 s14, 0x700000, s14\n"
  "s_cmp_eq_u32 s50, 6\n"
  "s_cselect_b32 s14, 0, s14\n"
  "v_mfma_f32_16x16x32_bf16 a[240:243], a[60:63], v[208:211], a[240:243]\n"
  "v_mfma_f32_16x16x32_bf16 a[244:247], a[60:63], v[212:215], a[244:247]\n"
  "v_mfma_f32_16x16x32_bf16 a[248:251], a[60:63], v[216:219], a[248:251]\n"
  "v_mfma_f32_16x16x32_bf16 a[252:255], a[60:63], v[220:223], a[252:255]\n"
  "v_mfma_f32_16x16x32_bf16 v[240:243], v[60:63], v[208:211], v[240:243]\n"
  "v_mfma_f32_16x16x32_bf16 v[244:247], v[60:63], v[212:215], v[244:247]\n"
  "v_mfma_f32_16x16x32_bf16 v[248:251], v[60:63], v[216:219], v[248:251]\n"
  "v_mfma_f32_16x16x32_bf16 v[252:255], v[60:63], v[220:223], v[252:255]\n"
  "s_waitcnt vmcnt(16) lgkmcnt(0)\n"
  "s_barrier\n"
  "s_sub_u32 s45, s61, s60\n"
  "v_add_u32 v17, s45, v17\n"
  "s_sub_u32 s44, s96, s95\n"
  "v_add_u32 v16, s44, v16\n"
  "v_mfma_f32_16x16x32_bf16 v[160:163], a[0:3], v[64:67], v[224:227]\n"
  "v_exp_f32 v144, v144\n"
  "v_mfma_f32_16x16x32_bf16 v[164:167], a[0:3], v[80:83], v[228:231]\n"
  "v_exp_f32 v145, v145\n"
  "v_mfma_f32_16x16x32_bf16 v[168:171], a[0:3], v[96:99], v[232:235]\n"
  "v_exp_f32 v146, v146\n"
  "v_mfma_f32_16x16x32_bf16 v[172:175], a[0:3], v[112:115], v[236:239]\n"
  "v_exp_f32 v147, v147\n"
  "v_mfma_f32_16x16x32_bf16 v[160:163], a[4:7], v[68:71], v[160:163]\n"
  "ds_read_b128 a[32:35], v17\n"
  "v_cvt_pk_bf16_f32 v194, v144, v145\n"
  "v_mfma_f32_16x16x32_bf16 v[164:167], a[4:7], v[84:87], v[164:167]\n"
  "ds_read_b128 a[36:39], v17 offset:2048\n"
  "v_cvt_pk_bf16_f32 v195, v146, v147\n"
  "v_mfma_f32_16x16x32_bf16 v[168:171], a[4:7], v[100:103], v[168:171]\n"
  "v_exp_f32 v148, v148\n"
  "v_mfma_f32_16x16x32_bf16 v[172:175], a[4:7], v[116:119], v[172:175]\n"
  "v_exp_f32 v149, v149\n"
  "v_mfma_f32_16x16x32_bf16 v[160:163], a[8:11], v[72:75], v[160:163]\n"
  "v_exp_f32 v150, v150\n"
  "v_mfma_f32_16x16x32_bf16 v[164:167], a[8:11], v[88:91], v[164:167]\n"
  "v_exp_f32 v151, v151\n"
  "v_mfma_f32_16x16x32_bf16 v[168:171], a[8:11], v[104:107], v[168:171]\n"
  "ds_read_b128 a[40:43], v17 offset:4096\n"
  "v_cvt_pk_bf16_f32 v198, v148, v149\n"
  "v_mfma_f32_16x16x32_bf16 v[172:175], a[8:11], v[120:123], v[172:175]\n"
  "ds_read_b128 a[44:47], v17 offset:6144\n"
  "v_cvt_pk_bf16_f32 v199, v150, v151\n"
  "v_mfma_f32_16x16x32_bf16 v[160:163], a[12:15], v[76:79], v[160:163]\n"
  "v_exp_f32 v152, v152\n"
  "v_mfma_f32_16x16x32_bf16 v[164:167], a[12:15], v[92:95], v[164:167]\n"
  "v_exp_f32 v153, v153\n"
  "v_mfma_f32_16x16x32_bf16 v[168:171], a[12:15], v[108:111], v[168:171]\n"
  "v_exp_f32 v154, v154\n"
  "v_mfma_f32_16x16x32_bf16 v[172:175], a[12:15], v[124:127], v[172:175]\n"
  "v_exp_f32 v155, v155\n"
  "v_mfma_f32_16x16x32_bf16 v[176:179], a[16:19], v[64:67], v[224:227]\n"
  "ds_read_b128 a[48:51], v17 offset:8192\n"
  "v_cvt_pk_bf16_f32 v202, v152, v153\n"
  "v_mfma_f32_16x16x32_bf16 v[180:183], a[16:19], v[80:83], v[228:231]\n"
  "ds_read_b128 a[52:55], v17 offset:10240\n"
  "v_cvt_pk_bf16_f32 v203, v154, v155\n"
  "v_mfma_f32_16x16x32_bf16 v[184:187], a[16:19], v[96:99], v[232:235]\n"
  "v_exp_f32 v156, v156\n"
  "v_mfma_f32_16x16x32_bf16 v[188:191], a[16:19], v[112:115], v[236:239]\n"
  "v_exp_f32 v157, v157\n"
  "v_mfma_f32_16x16x32_bf16 v[176:179], a[20:23], v[68:71], v[176:179]\n"
  "v_exp_f32 v158, v158\n"
  "v_mfma_f32_16x16x32_bf16 v[180:183], a[20:23], v[84:87], v[180:183]\n"
  "v_exp_f32 v159, v159\n"
  "v_mfma_f32_16x16x32_bf16 v[184:187], a[20:23], v[100:103], v[184:187]\n"
  "ds_read_b128 a[56:59], v17 offset:12288\n"
  "v_cvt_pk_bf16_f32 v206, v156, v157\n"
  "v_mfma_f32_16x16x32_bf16 v[188:191], a[20:23], v[116:119], v[188:191]\n"
  "ds_read_b128 a[60:63], v17 offset:14336\n"
  "v_cvt_pk_bf16_f32 v207, v158, v159\n"
  "v_mfma_f32_16x16x32_bf16 v[176:179], a[24:27], v[72:75], v[176:179]\n"
  "s_mov_b32 m0, s95\n"
  "v_mfma_f32_16x16x32_bf16 v[180:183], a[24:27], v[88:91], v[180:183]\n"
  "buffer_load_dwordx4 v15, s[16:19], s46 offen sc1 lds\n"
  "v_mfma_f32_16x16x32_bf16 v[184:187], a[24:27], v[104:107], v[184:187]\n"
  "v_mfma_f32_16x16x32_bf16 v[188:191], a[24:27], v[120:123], v[188:191]\n"
  "buffer_load_dwordx4 v15, s[16:19], s46 offen offset:1024 sc1 lds\n"
  "v_mfma_f32_16x16x32_bf16 v[176:179], a[28:31], v[76:79], v[176:179]\n"
  "v_mfma_f32_16x16x32_bf16 v[180:183], a[28:31], v[92:95], v[180:183]\n"
  "buffer_load_dwordx4 v15, s[16:19], s46 offen offset:2048 sc1 lds\n"
  "v_mfma_f32_16x16x32_bf16 v[184:187], a[28:31], v[108:111], v[184:187]\n"
  "buffer_load_dwordx4 v15, s[16:19], s46 offen offset:3072 sc1 lds\n"
  "v_mfma_f32_16x16x32_bf16 v[188:191], a[28:31], v[124:127], v[188:191]\n"
  "s_add_u32 s46, s46, 0x4000\n"
  "s_waitcnt lgkmcnt(0)\n"
  "v_mfma_f32_16x16x32_bf16 a[128:131], a[32:35], v[192:195], a[128:131]\n"
  "v_exp_f32 v160, v160\n"
  "v_mfma_f32_16x16x32_bf16 a[132:135], a[32:35], v[196:199], a[132:135]\n"
  "v_exp_f32 v161, v161\n"
  "v_mfma_f32_16x16x32_bf16 a[136:139], a[32:35], v[200:203], a[136:139]\n"
  "v_exp_f32 v162, v162\n"
  "v_mfma_f32_16x16x32_bf16 a[140:143], a[32:35], v[204:207], a[140:143]\n"
  "v_exp_f32 v163, v163\n"
  "v_mfma_f32_16x16x32_bf16 a[144:147], a[36:39], v[192:195], a[144:147]\n"
  "ds_read_b128 a[0:3], v16\n"
  "v_cvt_pk_bf16_f32 v208, v160, v161\n"
  "v_mfma_f32_16x16x32_bf16 a[148:151], a[36:39], v[196:199], a[148:151]\n"
  "ds_read_b128 a[4:7], v16 offset:1024\n"
  "v_cvt_pk_bf16_f32 v209, v162, v163\n"
  "v_mfma_f32_16x16x32_bf16 a[152:155], a[36:39], v[200:203], a[152:155]\n"
  "v_exp_f32 v164, v164\n"
  "v_mfma_f32_16x16x32_bf16 a[156:159], a[36:39], v[204:207], a[156:159]\n"
  "v_exp_f32 v165, v165\n"
  "v_mfma_f32_16x16x32_bf16 a[160:163], a[40:43], v[192:195], a[160:163]\n"
  "v_exp_f32 v166, v166\n"
  "v_mfma_f32_16x16x32_bf16 a[164:167], a[40:43], v[196:199], a[164:167]\n"
  "v_exp_f32 v167, v167\n"
  "v_mfma_f32_16x16x32_bf16 a[168:171], a[40:43], v[200:203], a[168:171]\n"
  "ds_read_b128 a[8:11], v16 offset:2048\n"
  "v_cvt_pk_bf16_f32 v212, v164, v165\n"
  "v_mfma_f32_16x16x32_bf16 a[172:175], a[40:43], v[204:207], a[172:175]\n"
  "ds_read_b128 a[12:15], v16 offset:3072\n"
  "v_cvt_pk_bf16_f32 v213, v166, v167\n"
  "v_mfma_f32_16x16x32_bf16 a[176:179], a[44:47], v[192:195], a[176:179]\n"
  "v_exp_f32 v168, v168\n"
  "v_mfma_f32_16x16x32_bf16 a[180:183], a[44:47], v[196:199], a[180:183]\n"
  "v_exp_f32 v169, v169\n"
  "v_mfma_f32_16x16x32_bf16 a[184:187], a[44:47], v[200:203], a[184:187]\n"
  "v_exp_f32 v170, v170\n"
  "v_mfma_f32_16x16x32_bf16 a[188:191], a[44:47], v[204:207], a[188:191]\n"
  "v_exp_f32 v171, v171\n"
  "v_mfma_f32_16x16x32_bf16 a[192:195], a[48:51], v[192:195], a[192:195]\n"
  "ds_read_b128 a[16:19], v16 offset:4096\n"
  "v_cvt_pk_bf16_f32 v216, v168, v169\n"
  "v_mfma_f32_16x16x32_bf16 a[196:199], a[48:51], v[196:199], a[196:199]\n"
  "ds_read_b128 a[20:23], v16 offset:5120\n"
  "v_cvt_pk_bf16_f32 v217, v170, v171\n"
  "v_mfma_f32_16x16x32_bf16 a[200:203], a[48:51], v[200:203], a[200:203]\n"
  "v_exp_f32 v172, v172\n"
  "v_mfma_f32_16x16x32_bf16 a[204:207], a[48:51], v[204:207], a[204:207]\n"
  "v_exp_f32 v173, v173\n"
  "v_mfma_f32_16x16x32_bf16 a[208:211], a[52:55], v[192:195], a[208:211]\n"
  "v_exp_f32 v174, v174\n"
  "v_mfma_f32_16x16x32_bf16 a[212:215], a[52:55], v[196:199], a[212:215]\n"
  "v_exp_f32 v175, v175\n"
  "v_mfma_f32_16x16x32_bf16 a[216:219], a[52:55], v[200:203], a[216:219]\n"
  "ds_read_b128 a[24:27], v16 offset:6144\n"
  "v_cvt_pk_bf16_f32 v220, v172, v173\n"
  "v_mfma_f32_16x16x32_bf16 a[220:223], a[52:55], v[204:207], a[220:223]\n"
  "ds_read_b128 a[28:31], v16 offset:7168\n"
  "v_cvt_pk_bf16_f32 v221, v174, v175\n"
  "v_mfma_f32_16x16x32_bf16 a[224:227], a[56:59], v[192:195], a[224:227]\n"
  "s_mov_b32 m0, s60\n"
  "v_mfma_f32_16x16x32_bf16 a[228:231], a[56:59], v[196:199], a[228:231]\n"
  "buffer_load_dwordx4 v15, s[56:59], s47 offen sc1 lds\n"
  "v_mfma_f32_16x16x32_bf16 a[232:235], a[56:59], v[200:203], a[232:235]\n"
  "v_mfma_f32_16x16x32_bf16 a[236:239], a[56:59], v[204:207], a[236:239]\n"
  "buffer_load_dwordx4 v15, s[56:59], s47 offen offset:1024 sc1 lds\n"
  "v_mfma_f32_16x16x32_bf16 a[240:243], a[60:63], v[192:195], a[240:243]\n"
  "v_mfma_f32_16x16x32_bf16 a[244:247], a[60:63], v[196:199], a[244:247]\n"
  "buffer_load_dwordx4 v15, s[56:59], s47 offen offset:2048 sc1 lds\n"
  "v_mfma_f32_16x16x32_bf16 a[248:251], a[60:63], v[200:203], a[248:251]\n"
  "v_mfma_f32_16x16x32_bf16 a[252:255], a[60:63], v[204:207], a[252:255]\n"
  "buffer_load_dwordx4 v15, s[56:59], s47 offen offset:3072 sc1 lds\n"
  "v_mfma_f32_16x16x32_bf16 v[240:243], v[60:63], v[192:195], v[240:243]\n"
  "s_add_u32 s47, s47, 0x4000\n"
  "s_mov_b32 s37, s95\n"
  "s_mov_b32 s95, s96\n"
  "s_mov_b32 s96, s97\n"
  "v_mfma_f32_16x16x32_bf16 v[244:247], v[60:63], v[196:199], v[244:247]\n"
  "s_mov_b32 s97, s37\n"
  "s_mov_b32 s37, s60\n"
  "s_mov_b32 s60, s61\n"
  "v_mfma_f32_16x16x32_bf16 v[248:251], v[60:63], v[200:203], v[248:251]\n"
  "s_mov_b32 s61, s62\n"
  "s_mov_b32 s62, s37\n"
  "v_mfma_f32_16x16x32_bf16 v[252:255], v[60:63], v[204:207], v[252:255]\n"
  "s_sub_u32 s20, s20, 1\n"
  "s_waitcnt lgkmcnt(0)\n"
  "s_cmp_lg_u32 s20, 0\n"
  "s_cbranch_scc1 ATT_LOOP_%=\n"
  "v_mfma_f32_16x16x32_bf16 v[128:131], a[0:3], v[64:67], v[224:227]\n"
  "v_exp_f32 v176, v176\n"
  "v_mfma_f32_16x16x32_bf16 v[132:135], a[0:3], v[80:83], v[228:231]\n"
  "v_exp_f32 v177, v177\n"
  "v_mfma_f32_16x16x32_bf16 v[136:139], a[0:3], v[96:99], v[232:235]\n"
  "v_exp_f32 v178, v178\n"
  "v_mfma_f32_16x16x32_bf16 v[140:143], a[0:3], v[112:115], v[236:239]\n"
  "v_exp_f32 v179, v179\n"
  "v_mfma_f32_16x16x32_bf16 v[128:131], a[4:7], v[68:71], v[128:131]\n"
  "ds_read_b128 a[32:35], v17 offset:1024\n"
  "v_cvt_pk_bf16_f32 v210, v176, v177\n"
  "v_mfma_f32_16x16x32_bf16 v[132:135], a[4:7], v[84:87], v[132:135]\n"
  "ds_read_b128 a[36:39], v17 offset:3072\n"
  "v_cvt_pk_bf16_f32 v211, v178, v179\n"
  "v_mfma_f32_16x16x32_bf16 v[136:139], a[4:7], v[100:103], v[136:139]\n"
  "v_exp_f32 v180, v180\n"
  "v_mfma_f32_16x16x32_bf16 v[140:143], a[4:7], v[116:119], v[140:143]\n"
  "v_exp_f32 v181, v181\n"
  "v_mfma_f32_16x16x32_bf16 v[128:131], a[8:11], v[72:75], v[128:131]\n"
  "v_exp_f32 v182, v182\n"
  "v_mfma_f32_16x16x32_bf16 v[132:135], a[8:11], v[88:91], v[132:135]\n"
  "v_exp_f32 v183, v183\n"
  "v_mfma_f32_16x16x32_bf16 v[136:139], a[8:11], v[104:107], v[136:139]\n"
  "ds_read_b128 a[40:43], v17 offset:5120\n"
  "v_cvt_pk_bf16_f32 v214, v180, v181\n"
  "v_mfma_f32_16x16x32_bf16 v[140:143], a[8:11], v[120:123], v[140:143]\n"
  "ds_read_b128 a[44:47], v17 offset:7168\n"
  "v_cvt_pk_bf16_f32 v215, v182, v183\n"
  "v_mfma_f32_16x16x32_bf16 v[128:131], a[12:15], v[76:79], v[128:131]\n"
  "v_exp_f32 v184, v184\n"
  "v_mfma_f32_16x16x32_bf16 v[132:135], a[12:15], v[92:95], v[132:135]\n"
  "v_exp_f32 v185, v185\n"
  "v_mfma_f32_16x16x32_bf16 v[136:139], a[12:15], v[108:111], v[136:139]\n"
  "v_exp_f32 v186, v186\n"
  "v_mfma_f32_16x16x32_bf16 v[140:143], a[12:15], v[124:127], v[140:143]\n"
  "v_exp_f32 v187, v187\n"
  "v_mfma_f32_16x16x32_bf16 v[144:147], a[16:19], v[64:67], v[224:227]\n"
  "ds_read_b128 a[48:51], v17 offset:9216\n"
  "v_cvt_pk_bf16_f32 v218, v184, v185\n"
  "v_mfma_f32_16x16x32_bf16 v[148:151], a[16:19], v[80:83], v[228:231]\n"
  "ds_read_b128 a[52:55], v17 offset:11264\n"
  "v_cvt_pk_bf16_f32 v219, v186, v187\n"
  "v_mfma_f32_16x16x32_bf16 v[152:155], a[16:19], v[96:99], v[232:235]\n"
  "v_exp_f32 v188, v188\n"
  "v_mfma_f32_16x16x32_bf16 v[156:159], a[16:19], v[112:115], v[236:239]\n"
  "v_exp_f32 v189, v189\n"
  "v_mfma_f32_16x16x32_bf16 v[144:147], a[20:23], v[68:71], v[144:147]\n"
  "v_exp_f32 v190, v190\n"
  "v_mfma_f32_16x16x32_bf16 v[148:151], a[20:23], v[84:87], v[148:151]\n"
  "v_exp_f32 v191, v191\n"
  "v_mfma_f32_16x16x32_bf16 v[152:155], a[20:23], v[100:103], v[152:155]\n"
  "ds_read_b128 a[56:59], v17 offset:13312\n"
  "v_cvt_pk_bf16_f32 v222, v188, v189\n"
  "v_mfma_f32_16x16x32_bf16 v[156:159], a[20:23], v[116:119], v[156:159]\n"
  "ds_read_b128 a[60:63], v17 offset:15360\n"
  "v_cvt_pk_bf16_f32 v223, v190, v191\n"
  "v_mfma_f32_16x16x32_bf16 v[144:147], a[24:27], v[72:75], v[144:147]\n"
  "buffer_load_dwordx4 v[48:51], v15, s[12:15], s51 offen\n"
  "s_add_u32 m0, s24, 0\n"
  "v_mfma_f32_16x16x32_bf16 v[148:151], a[24:27], v[88:91], v[148:151]\n"
  "buffer_load_dwordx4 v3, s[8:11], s22 offen lds\n"
  "v_mfma_f32_16x16x32_bf16 v[152:155], a[24:27], v[104:107], v[152:155]\n"
  "buffer_load_dwordx4 v[48:51], v15, s[12:15], s51 offen offset:1024\n"
  "v_mfma_f32_16x16x32_bf16 v[156:159], a[24:27], v[120:123], v[156:159]\n"
  "buffer_load_dwordx4 v4, s[8:11], s22 offen offset:1024 lds\n"
  "v_mfma_f32_16x16x32_bf16 v[144:147], a[28:31], v[76:79], v[144:147]\n"
  "buffer_load_dwordx4 v[48:51], v15, s[12:15], s51 offen offset:2048\n"
  "v_mfma_f32_16x16x32_bf16 v[148:151], a[28:31], v[92:95], v[148:151]\n"
  "buffer_load_dwordx4 v5, s[8:11], s22 offen offset:2048 lds\n"
  "v_mfma_f32_16x16x32_bf16 v[152:155], a[28:31], v[108:111], v[152:155]\n"
  "buffer_load_dwordx4 v[48:51], v15, s[12:15], s51 offen offset:3072\n"
  "v_mfma_f32_16x16x32_bf16 v[156:159], a[28:31], v[124:127], v[156:159]\n"
  "buffer_load_dwordx4 v6, s[8:11], s22 offen offset:3072 lds\n"
  "s_add_u32 s22, s22, s23\n"
  "s_waitcnt lgkmcnt(0)\n"
  "v_mfma_f32_16x16x32_bf16 a[128:131], a[32:35], v[208:211], a[128:131]\n"
  "v_exp_f32 v128, v128\n"
  "v_mfma_f32_16x16x32_bf16 a[132:135], a[32:35], v[212:215], a[132:135]\n"
  "v_exp_f32 v129, v129\n"
  "v_mfma_f32_16x16x32_bf16 a[136:139], a[32:35], v[216:219], a[136:139]\n"
  "v_exp_f32 v130, v130\n"
  "v_mfma_f32_16x16x32_bf16 a[140:143], a[32:35], v[220:223], a[140:143]\n"
  "v_exp_f32 v131, v131\n"
  "v_mfma_f32_16x16x32_bf16 a[144:147], a[36:39], v[208:211], a[144:147]\n"
  "ds_read_b128 a[0:3], v16 offset:8192\n"
  "v_cvt_pk_bf16_f32 v192, v128, v129\n"
  "v_mfma_f32_16x16x32_bf16 a[148:151], a[36:39], v[212:215], a[148:151]\n"
  "ds_read_b128 a[4:7], v16 offset:9216\n"
  "v_cvt_pk_bf16_f32 v193, v130, v131\n"
  "v_mfma_f32_16x16x32_bf16 a[152:155], a[36:39], v[216:219], a[152:155]\n"
  "v_exp_f32 v132, v132\n"
  "v_mfma_f32_16x16x32_bf16 a[156:159], a[36:39], v[220:223], a[156:159]\n"
  "v_exp_f32 v133, v133\n"
  "v_mfma_f32_16x16x32_bf16 a[160:163], a[40:43], v[208:211], a[160:163]\n"
  "v_exp_f32 v134, v134\n"
  "v_mfma_f32_16x16x32_bf16 a[164:167], a[40:43], v[212:215], a[164:167]\n"
  "v_exp_f32 v135, v135\n"
  "v_mfma_f32_16x16x32_bf16 a[168:171], a[40:43], v[216:219], a[168:171]\n"
  "ds_read_b128 a[8:11], v16 offset:10240\n"
  "v_cvt_pk_bf16_f32 v196, v132, v133\n"
  "v_mfma_f32_16x16x32_bf16 a[172:175], a[40:43], v[220:223], a[172:175]\n"
  "ds_read_b128 a[12:15], v16 offset:11264\n"
  "v_cvt_pk_bf16_f32 v197, v134, v135\n"
  "v_mfma_f32_16x16x32_bf16 a[176:179], a[44:47], v[208:211], a[176:179]\n"
  "v_exp_f32 v136, v136\n"
  "v_mfma_f32_16x16x32_bf16 a[180:183], a[44:47], v[212:215], a[180:183]\n"
  "v_exp_f32 v137, v137\n"
  "v_mfma_f32_16x16x32_bf16 a[184:187], a[44:47], v[216:219], a[184:187]\n"
  "v_exp_f32 v138, v138\n"
  "v_mfma_f32_16x16x32_bf16 a[188:191], a[44:47], v[220:223], a[188:191]\n"
  "v_exp_f32 v139, v139\n"
  "v_mfma_f32_16x16x32_bf16 a[192:195], a[48:51], v[208:211], a[192:195]\n"
  "ds_read_b128 a[16:19], v16 offset:12288\n"
  "v_cvt_pk_bf16_f32 v200, v136, v137\n"
  "v_mfma_f32_16x16x32_bf16 a[196:199], a[48:51], v[212:215], a[196:199]\n"
  "ds_read_b128 a[20:23], v16 offset:13312\n"
  "v_cvt_pk_bf16_f32 v201, v138, v139\n"
  "v_mfma_f32_16x16x32_bf16 a[200:203], a[48:51], v[216:219], a[200:203]\n"
  "v_exp_f32 v140, v140\n"
  "v_mfma_f32_16x16x32_bf16 a[204:207], a[48:51], v[220:223], a[204:207]\n"
  "v_exp_f32 v141, v141\n"
  "v_mfma_f32_16x16x32_bf16 a[208:211], a[52:55], v[208:211], a[208:211]\n"
  "v_exp_f32 v142, v142\n"
  "v_mfma_f32_16x16x32_bf16 a[212:215], a[52:55], v[212:215], a[212:215]\n"
  "v_exp_f32 v143, v143\n"
  "v_mfma_f32_16x16x32_bf16 a[216:219], a[52:55], v[216:219], a[216:219]\n"
  "ds_read_b128 a[24:27], v16 offset:14336\n"
  "v_cvt_pk_bf16_f32 v204, v140, v141\n"
  "v_mfma_f32_16x16x32_bf16 a[220:223], a[52:55], v[220:223], a[220:223]\n"
  "ds_read_b128 a[28:31], v16 offset:15360\n"
  "v_cvt_pk_bf16_f32 v205, v142, v143\n"
  "v_mfma_f32_16x16x32_bf16 a[224:227], a[56:59], v[208:211], a[224:227]\n"
  "s_add_u32 m0, s24, 0x4000\n"
  "v_mfma_f32_16x16x32_bf16 a[228:231], a[56:59], v[212:215], a[228:231]\n"
  "buffer_load_dwordx4 v3, s[8:11], s22 offen lds\n"
  "v_mfma_f32_16x16x32_bf16 a[232:235], a[56:59], v[216:219], a[232:235]\n"
  "v_mfma_f32_16x16x32_bf16 a[236:239], a[56:59], v[220:223], a[236:239]\n"
  "buffer_load_dwordx4 v4, s[8:11], s22 offen offset:1024 lds\n"
  "v_mfma_f32_16x16x32_bf16 a[240:243], a[60:63], v[208:211], a[240:243]\n"
  "v_mfma_f32_16x16x32_bf16 a[244:247], a[60:63], v[212:215], a[244:247]\n"
  "buffer_load_dwordx4 v5, s[8:11], s22 offen offset:2048 lds\n"
  "v_mfma_f32_16x16x32_bf16 a[248:251], a[60:63], v[216:219], a[248:251]\n"
  "v_mfma_f32_16x16x32_bf16 a[252:255], a[60:63], v[220:223], a[252:255]\n"
  "buffer_load_dwordx4 v6, s[8:11], s22 offen offset:3072 lds\n"
  "v_mfma_f32_16x16x32_bf16 v[240:243], v[60:63], v[208:211], v[240:243]\n"
  "s_add_u32 s22, s22, s23\n"
  "v_mfma_f32_16x16x32_bf16 v[244:247], v[60:63], v[212:215], v[244:247]\n"
  "v_mfma_f32_16x16x32_bf16 v[248:251], v[60:63], v[216:219], v[248:251]\n"
  "v_mfma_f32_16x16x32_bf16 v[252:255], v[60:63], v[220:223], v[252:255]\n"
  "s_waitcnt vmcnt(24) lgkmcnt(0)\n"
  "s_barrier\n"
  "s_sub_u32 s45, s61, s60\n"
  "v_add_u32 v17, s45, v17\n"
  "s_sub_u32 s44, s96, s95\n"
  "v_add_u32 v16, s44, v16\n"
  "v_mfma_f32_16x16x32_bf16 v[160:163], a[0:3], v[64:67], v[224:227]\n"
  "v_exp_f32 v144, v144\n"
  "v_mfma_f32_16x16x32_bf16 v[164:167], a[0:3], v[80:83], v[228:231]\n"
  "v_exp_f32 v145, v145\n"
  "v_mfma_f32_16x16x32_bf16 v[168:171], a[0:3], v[96:99], v[232:235]\n"
  "v_exp_f32 v146, v146\n"
  "v_mfma_f32_16x16x32_bf16 v[172:175], a[0:3], v[112:115], v[236:239]\n"
  "v_exp_f32 v147, v147\n"
  "v_mfma_f32_16x16x32_bf16 v[160:163], a[4:7], v[68:71], v[160:163]\n"
  "ds_read_b128 a[32:35], v17\n"
  "v_cvt_pk_bf16_f32 v194, v144, v145\n"
  "v_mfma_f32_16x16x32_bf16 v[164:167], a[4:7], v[84:87], v[164:167]\n"
  "ds_read_b128 a[36:39], v17 offset:2048\n"
  "v_cvt_pk_bf16_f32 v195, v146, v147\n"
  "v_mfma_f32_16x16x32_bf16 v[168:171], a[4:7], v[100:103], v[168:171]\n"
  "v_exp_f32 v148, v148\n"
  "v_mfma_f32_16x16x32_bf16 v[172:175], a[4:7], v[116:119], v[172:175]\n"
  "v_exp_f32 v149, v149\n"
  "v_mfma_f32_16x16x32_bf16 v[160:163], a[8:11], v[72:75], v[160:163]\n"
  "v_exp_f32 v150, v150\n"
  "v_mfma_f32_16x16x32_bf16 v[164:167], a[8:11], v[88:91], v[164:167]\n"
  "v_exp_f32 v151, v151\n"
  "v_mfma_f32_16x16x32_bf16 v[168:171], a[8:11], v[104:107], v[168:171]\n"
  "ds_read_b128 a[40:43], v17 offset:4096\n"
  "v_cvt_pk_bf16_f32 v198, v148, v149\n"
  "v_mfma_f32_16x16x32_bf16 v[172:175], a[8:11], v[120:123], v[172:175]\n"
  "ds_read_b128 a[44:47], v17 offset:6144\n"
  "v_cvt_pk_bf16_f32 v199, v150, v151\n"
  "v_mfma_f32_16x16x32_bf16 v[160:163], a[12:15], v[76:79], v[160:163]\n"
  "v_exp_f32 v152, v152\n"
  "v_mfma_f32_16x16x32_bf16 v[164:167], a[12:15], v[92:95], v[164:167]\n"
  "v_exp_f32 v153, v153\n"
  "v_mfma_f32_16x16x32_bf16 v[168:171], a[12:15], v[108:111], v[168:171]\n"
  "v_exp_f32 v154, v154\n"
  "v_mfma_f32_16x16x32_bf16 v[172:175], a[12:15], v[124:127], v[172:175]\n"
  "v_exp_f32 v155, v155\n"
  "v_mfma_f32_16x16x32_bf16 v[176:179], a[16:19], v[64:67], v[224:227]\n"
  "ds_read_b128 a[48:51], v17 offset:8192\n"
  "v_cvt_pk_bf16_f32 v202, v152, v153\n"
  "v_mfma_f32_16x16x32_bf16 v[180:183], a[16:19], v[80:83], v[228:231]\n"
  "ds_read_b128 a[52:55], v17 offset:10240\n"
  "v_cvt_pk_bf16_f32 v203, v154, v155\n"
  "v_mfma_f32_16x16x32_bf16 v[184:187], a[16:19], v[96:99], v[232:235]\n"
  "v_exp_f32 v156, v156\n"
  "v_mfma_f32_16x16x32_bf16 v[188:191], a[16:19], v[112:115], v[236:239]\n"
  "v_exp_f32 v157, v157\n"
  "v_mfma_f32_16x16x32_bf16 v[176:179], a[20:23], v[68:71], v[176:179]\n"
  "v_exp_f32 v158, v158\n"
  "v_mfma_f32_16x16x32_bf16 v[180:183], a[20:23], v[84:87], v[180:183]\n"
  "v_exp_f32 v159, v159\n"
  "v_mfma_f32_16x16x32_bf16 v[184:187], a[20:23], v[100:103], v[184:187]\n"
  "ds_read_b128 a[56:59], v17 offset:12288\n"
  "v_cvt_pk_bf16_f32 v206, v156, v157\n"
  "v_mfma_f32_16x16x32_bf16 v[188:191], a[20:23], v[116:119], v[188:191]\n"
  "ds_read_b128 a[60:63], v17 offset:14336\n"
  "v_cvt_pk_bf16_f32 v207, v158, v159\n"
  "v_mfma_f32_16x16x32_bf16 v[176:179], a[24:27], v[72:75], v[176:179]\n"
  "s_add_u32 m0, s24, 0x8000\n"
  "v_mfma_f32_16x16x32_bf16 v[180:183], a[24:27], v[88:91], v[180:183]\n"
  "buffer_load_dwordx4 v3, s[8:11], s22 offen lds\n"
  "v_mfma_f32_16x16x32_bf16 v[184:187], a[24:27], v[104:107], v[184:187]\n"
  "v_mfma_f32_16x16x32_bf16 v[188:191], a[24:27], v[120:123], v[188:191]\n"
  "buffer_load_dwordx4 v4, s[8:11], s22 offen offset:1024 lds\n"
  "v_mfma_f32_16x16x32_bf16 v[176:179], a[28:31], v[76:79], v[176:179]\n"
  "v_mfma_f32_16x16x32_bf16 v[180:183], a[28:31], v[92:95], v[180:183]\n"
  "buffer_load_dwordx4 v5, s[8:11], s22 offen offset:2048 lds\n"
  "v_mfma_f32_16x16x32_bf16 v[184:187], a[28:31], v[108:111], v[184:187]\n"
  "v_mfma_f32_16x16x32_bf16 v[188:191], a[28:31], v[124:127], v[188:191]\n"
  "buffer_load_dwordx4 v6, s[8:11], s22 offen offset:3072 lds\n"
  "s_add_u32 s22, s22, s23\n"
  "s_waitcnt lgkmcnt(0)\n"
  "v_mfma_f32_16x16x32_bf16 a[128:131], a[32:35], v[192:195], a[128:131]\n"
  "v_exp_f32 v160, v160\n"
  "v_mfma_f32_16x16x32_bf16 a[132:135], a[32:35], v[196:199], a[132:135]\n"
  "v_exp_f32 v161, v161\n"
  "v_mfma_f32_16x16x32_bf16 a[136:139], a[32:35], v[200:203], a[136:139]\n"
  "v_exp_f32 v162, v162\n"
  "v_mfma_f32_16x16x32_bf16 a[140:143], a[32:35], v[204:207], a[140:143]\n"
  "v_exp_f32 v163, v163\n"
  "v_mfma_f32_16x16x32_bf16 a[144:147], a[36:39], v[192:195], a[144:147]\n"
  "ds_read_b128 a[0:3], v16\n"
  "v_cvt_pk_bf16_f32 v208, v160, v161\n"
  "v_mfma_f32_16x16x32_bf16 a[148:151], a[36:39], v[196:199], a[148:151]\n"
  "ds_read_b128 a[4:7], v16 offset:1024\n"
  "v_cvt_pk_bf16_f32 v209, v162, v163\n"
  "v_mfma_f32_16x16x32_bf16 a[152:155], a[36:39], v[200:203], a[152:155]\n"
  "v_exp_f32 v164, v164\n"
  "v_mfma_f32_16x16x32_bf16 a[156:159], a[36:39], v[204:207], a[156:159]\n"
  "v_exp_f32 v165, v165\n"
  "v_mfma_f32_16x16x32_bf16 a[160:163], a[40:43], v[192:195], a[160:163]\n"
  "v_exp_f32 v166, v166\n"
  "v_mfma_f32_16x16x32_bf16 a[164:167], a[40:43], v[196:199], a[164:167]\n"
  "v_exp_f32 v167, v167\n"
  "v_mfma_f32_16x16x32_bf16 a[168:171], a[40:43], v[200:203], a[168:171]\n"
  "ds_read_b128 a[8:11], v16 offset:2048\n"
  "v_cvt_pk_bf16_f32 v212, v164, v165\n"
  "v_mfma_f32_16x16x32_bf16 a[172:175], a[40:43], v[204:207], a[172:175]\n"
  "ds_read_b128 a[12:15], v16 offset:3072\n"
  "v_cvt_pk_bf16_f32 v213, v166, v167\n"
  "v_mfma_f32_16x16x32_bf16 a[176:179], a[44:47], v[192:195], a[176:179]\n"
  "v_exp_f32 v168, v168\n"
  "v_mfma_f32_16x16x32_bf16 a[180:183], a[44:47], v[196:199], a[180:183]\n"
  "v_exp_f32 v169, v169\n"
  "v_mfma_f32_16x16x32_bf16 a[184:187], a[44:47], v[200:203], a[184:187]\n"
  "v_exp_f32 v170, v170\n"
  "v_mfma_f32_16x16x32_bf16 a[188:191], a[44:47], v[204:207], a[188:191]\n"
  "v_exp_f32 v171, v171\n"
  "v_mfma_f32_16x16x32_bf16 a[192:195], a[48:51], v[192:195], a[192:195]\n"
  "ds_read_b128 a[16:19], v16 offset:4096\n"
  "v_cvt_pk_bf16_f32 v216, v168, v169\n"
  "v_mfma_f32_16x16x32_bf16 a[196:199], a[48:51], v[196:199], a[196:199]\n"
  "ds_read_b128 a[20:23], v16 offset:5120\n"
  "v_cvt_pk_bf16_f32 v217, v170, v171\n"
  "v_mfma_f32_16x16x32_bf16 a[200:203], a[48:51], v[200:203], a[200:203]\n"
  "v_exp_f32 v172, v172\n"
  "v_mfma_f32_16x16x32_bf16 a[204:207], a[48:51], v[204:207], a[204:207]\n"
  "v_exp_f32 v173, v173\n"
  "v_mfma_f32_16x16x32_bf16 a[208:211], a[52:55], v[192:195], a[208:211]\n"
  "v_exp_f32 v174, v174\n"
  "v_mfma_f32_16x16x32_bf16 a[212:215], a[52:55], v[196:199], a[212:215]\n"
  "v_exp_f32 v175, v175\n"
  "v_mfma_f32_16x16x32_bf16 a[216:219], a[52:55], v[200:203], a[216:219]\n"
  "ds_read_b128 a[24:27], v16 offset:6144\n"
  "v_cvt_pk_bf16_f32 v220, v172, v173\n"
  "v_mfma_f32_16x16x32_bf16 a[220:223], a[52:55], v[204:207], a[220:223]\n"
  "ds_read_b128 a[28:31], v16 offset:7168\n"
  "v_cvt_pk_bf16_f32 v221, v174, v175\n"
  "v_mfma_f32_16x16x32_bf16 a[224:227], a[56:59], v[192:195], a[224:227]\n"
  "s_add_u32 m0, s24, 0xc000\n"
  "v_mfma_f32_16x16x32_bf16 a[228:231], a[56:59], v[196:199], a[228:231]\n"
  "buffer_load_dwordx4 v3, s[8:11], s22 offen lds\n"
  "v_mfma_f32_16x16x32_bf16 a[232:235], a[56:59], v[200:203], a[232:235]\n"
  "v_mfma_f32_16x16x32_bf16 a[236:239], a[56:59], v[204:207], a[236:239]\n"
  "buffer_load_dwordx4 v4, s[8:11], s22 offen offset:1024 lds\n"
  "v_mfma_f32_16x16x32_bf16 a[240:243], a[60:63], v[192:195], a[240:243]\n"
  "v_mfma_f32_16x16x32_bf16 a[244:247], a[60:63], v[196:199], a[244:247]\n"
  "buffer_load_dwordx4 v5, s[8:11], s22 offen offset:2048 lds\n"
  "v_mfma_f32_16x16x32_bf16 a[248:251], a[60:63], v[200:203], a[248:251]\n"
  "v_mfma_f32_16x16x32_bf16 a[252:255], a[60:63], v[204:207], a[252:255]\n"
  "buffer_load_dwordx4 v6, s[8:11], s22 offen offset:3072 lds\n"
  "v_mfma_f32_16x16x32_bf16 v[240:243], v[60:63], v[192:195], v[240:243]\n"
  "s_add_u32 s22, s22, s23\n"
  "v_mfma_f32_16x16x32_bf16 v[244:247], v[60:63], v[196:199], v[244:247]\n"
  "s_mov_b32 s60, s61\n"
  "s_mov_b32 s61, s62\n"
  "v_mfma_f32_16x16x32_bf16 v[248:251], v[60:63], v[200:203], v[248:251]\n"
  "v_mfma_f32_16x16x32_bf16 v[252:255], v[60:63], v[204:207], v[252:255]\n"
  "s_waitcnt lgkmcnt(0)\n"
  "v_mfma_f32_16x16x32_bf16 v[128:131], a[0:3], v[64:67], v[224:227]\n"
  "v_exp_f32 v176, v176\n"
  "v_mfma_f32_16x16x32_bf16 v[132:135], a[0:3], v[80:83], v[228:231]\n"
  "v_exp_f32 v177, v177\n"
  "v_mfma_f32_16x16x32_bf16 v[136:139], a[0:3], v[96:99], v[232:235]\n"
  "v_exp_f32 v178, v178\n"
  "v_mfma_f32_16x16x32_bf16 v[140:143], a[0:3], v[112:115], v[236:239]\n"
  "v_exp_f32 v179, v179\n"
  "v_mfma_f32_16x16x32_bf16 v[128:131], a[4:7], v[68:71], v[128:131]\n"
  "ds_read_b128 a[32:35], v17 offset:1024\n"
  "v_cvt_pk_bf16_f32 v210, v176, v177\n"
  "v_mfma_f32_16x16x32_bf16 v[132:135], a[4:7], v[84:87], v[132:135]\n"
  "ds_read_b128 a[36:39], v17 offset:3072\n"
  "v_cvt_pk_bf16_f32 v211, v178, v179\n"
  "v_mfma_f32_16x16x32_bf16 v[136:139], a[4:7], v[100:103], v[136:139]\n"
  "v_exp_f32 v180, v180\n"
  "v_mfma_f32_16x16x32_bf16 v[140:143], a[4:7], v[116:119], v[140:143]\n"
  "v_exp_f32 v181, v181\n"
  "v_mfma_f32_16x16x32_bf16 v[128:131], a[8:11], v[72:75], v[128:131]\n"
  "v_exp_f32 v182, v182\n"
  "v_mfma_f32_16x16x32_bf16 v[132:135], a[8:11], v[88:91], v[132:135]\n"
  "v_exp_f32 v183, v183\n"
  "v_mfma_f32_16x16x32_bf16 v[136:139], a[8:11], v[104:107], v[136:139]\n"
  "ds_read_b128 a[40:43], v17 offset:5120\n"
  "v_cvt_pk_bf16_f32 v214, v180, v181\n"
  "v_mfma_f32_16x16x32_bf16 v[140:143], a[8:11], v[120:123], v[140:143]\n"
  "ds_read_b128 a[44:47], v17 offset:7168\n"
  "v_cvt_pk_bf16_f32 v215, v182, v183\n"
  "v_mfma_f32_16x16x32_bf16 v[128:131], a[12:15], v[76:79], v[128:131]\n"
  "v_exp_f32 v184, v184\n"
  "v_mfma_f32_16x16x32_bf16 v[132:135], a[12:15], v[92:95], v[132:135]\n"
  "v_exp_f32 v185, v185\n"
  "v_mfma_f32_16x16x32_bf16 v[136:139], a[12:15], v[108:111], v[136:139]\n"
  "v_exp_f32 v186, v186\n"
  "v_mfma_f32_16x16x32_bf16 v[140:143], a[12:15], v[124:127], v[140:143]\n"
  "v_exp_f32 v187, v187\n"
  "v_mfma_f32_16x16x32_bf16 v[144:147], a[16:19], v[64:67], v[224:227]\n"
  "ds_read_b128 a[48:51], v17 offset:9216\n"
  "v_cvt_pk_bf16_f32 v218, v184, v185\n"
  "v_mfma_f32_16x16x32_bf16 v[148:151], a[16:19], v[80:83], v[228:231]\n"
  "ds_read_b128 a[52:55], v17 offset:11264\n"
  "v_cvt_pk_bf16_f32 v219, v186, v187\n"
  "v_mfma_f32_16x16x32_bf16 v[152:155], a[16:19], v[96:99], v[232:235]\n"
  "v_exp_f32 v188, v188\n"
  "v_mfma_f32_16x16x32_bf16 v[156:159], a[16:19], v[112:115], v[236:239]\n"
  "v_exp_f32 v189, v189\n"
  "v_mfma_f32_16x16x32_bf16 v[144:147], a[20:23], v[68:71], v[144:147]\n"
  "v_exp_f32 v190, v190\n"
  "v_mfma_f32_16x16x32_bf16 v[148:151], a[20:23], v[84:87], v[148:151]\n"
  "v_exp_f32 v191, v191\n"
  "v_mfma_f32_16x16x32_bf16 v[152:155], a[20:23], v[100:103], v[152:155]\n"
  "ds_read_b128 a[56:59], v17 offset:13312\n"
  "v_cvt_pk_bf16_f32 v222, v188, v189\n"
  "v_mfma_f32_16x16x32_bf16 v[156:159], a[20:23], v[116:119], v[156:159]\n"
  "ds_read_b128 a[60:63], v17 offset:15360\n"
  "v_cvt_pk_bf16_f32 v223, v190, v191\n"
  "v_mfma_f32_16x16x32_bf16 v[144:147], a[24:27], v[72:75], v[144:147]\n"
  "v_mfma_f32_16x16x32_bf16 v[148:151], a[24:27], v[88:91], v[148:151]\n"
  "buffer_load_dwordx4 a[64:67], v2, s[4:7], s21 offen sc1\n"
  "v_mfma_f32_16x16x32_bf16 v[152:155], a[24:27], v[104:107], v[152:155]\n"
  "v_mfma_f32_16x16x32_bf16 v[156:159], a[24:27], v[120:123], v[156:159]\n"
  "buffer_load_dwordx4 a[68:71], v2, s[4:7], s21 offen offset:256 sc1\n"
  "v_mfma_f32_16x16x32_bf16 v[144:147], a[28:31], v[76:79], v[144:147]\n"
  "v_mfma_f32_16x16x32_bf16 v[148:151], a[28:31], v[92:95], v[148:151]\n"
  "buffer_load_dwordx4 a[72:75], v2, s[4:7], s21 offen offset:512 sc1\n"
  "v_mfma_f32_16x16x32_bf16 v[152:155], a[28:31], v[108:111], v[152:155]\n"
  "v_mfma_f32_16x16x32_bf16 v[156:159], a[28:31], v[124:127], v[156:159]\n"
  "buffer_load_dwordx4 a[76:79], v2, s[4:7], s21 offen offset:768 sc1\n"
  "s_add_u32 s21, s21, 0x4000\n"
  "s_waitcnt lgkmcnt(0)\n"
  "v_mfma_f32_16x16x32_bf16 a[128:131], a[32:35], v[208:211], a[128:131]\n"
  "v_exp_f32 v128, v128\n"
  "v_mfma_f32_16x16x32_bf16 a[132:135], a[32:35], v[212:215], a[132:135]\n"
  "v_exp_f32 v129, v129\n"
  "v_mfma_f32_16x16x32_bf16 a[136:139], a[32:35], v[216:219], a[136:139]\n"
  "v_exp_f32 v130, v130\n"
  "v_mfma_f32_16x16x32_bf16 a[140:143], a[32:35], v[220:223], a[140:143]\n"
  "v_exp_f32 v131, v131\n"
  "v_mfma_f32_16x16x32_bf16 a[144:147], a[36:39], v[208:211], a[144:147]\n"
  "ds_read_b128 a[0:3], v16 offset:8192\n"
  "v_cvt_pk_bf16_f32 v192, v128, v129\n"
  "v_mfma_f32_16x16x32_bf16 a[148:151], a[36:39], v[212:215], a[148:151]\n"
  "ds_read_b128 a[4:7], v16 offset:9216\n"
  "v_cvt_pk_bf16_f32 v193, v130, v131\n"
  "v_mfma_f32_16x16x32_bf16 a[152:155], a[36:39], v[216:219], a[152:155]\n"
  "v_exp_f32 v132, v132\n"
  "v_mfma_f32_16x16x32_bf16 a[156:159], a[36:39], v[220:223], a[156:159]\n"
  "v_exp_f32 v133, v133\n"
  "v_mfma_f32_16x16x32_bf16 a[160:163], a[40:43], v[208:211], a[160:163]\n"
  "v_exp_f32 v134, v134\n"
  "v_mfma_f32_16x16x32_bf16 a[164:167], a[40:43], v[212:215], a[164:167]\n"
  "v_exp_f32 v135, v135\n"
  "v_mfma_f32_16x16x32_bf16 a[168:171], a[40:43], v[216:219], a[168:171]\n"
  "ds_read_b128 a[8:11], v16 offset:10240\n"
  "v_cvt_pk_bf16_f32 v196, v132, v133\n"
  "v_mfma_f32_16x16x32_bf16 a[172:175], a[40:43], v[220:223], a[172:175]\n"
  "ds_read_b128 a[12:15], v16 offset:11264\n"
  "v_cvt_pk_bf16_f32 v197, v134, v135\n"
  "v_mfma_f32_16x16x32_bf16 a[176:179], a[44:47], v[208:211], a[176:179]\n"
  "v_exp_f32 v136, v136\n"
  "v_mfma_f32_16x16x32_bf16 a[180:183], a[44:47], v[212:215], a[180:183]\n"
  "v_exp_f32 v137, v137\n"
  "v_mfma_f32_16x16x32_bf16 a[184:187], a[44:47], v[216:219], a[184:187]\n"
  "v_exp_f32 v138, v138\n"
  "v_mfma_f32_16x16x32_bf16 a[188:191], a[44:47], v[220:223], a[188:191]\n"
  "v_exp_f32 v139, v139\n"
  "v_mfma_f32_16x16x32_bf16 a[192:195], a[48:51], v[208:211], a[192:195]\n"
  "ds_read_b128 a[16:19], v16 offset:12288\n"
  "v_cvt_pk_bf16_f32 v200, v136, v137\n"
  "v_mfma_f32_16x16x32_bf16 a[196:199], a[48:51], v[212:215], a[196:199]\n"
  "ds_read_b128 a[20:23], v16 offset:13312\n"
  "v_cvt_pk_bf16_f32 v201, v138, v139\n"
  "v_mfma_f32_16x16x32_bf16 a[200:203], a[48:51], v[216:219], a[200:203]\n"
  "v_exp_f32 v140, v140\n"
  "v_mfma_f32_16x16x32_bf16 a[204:207], a[48:51], v[220:223], a[204:207]\n"
  "v_exp_f32 v141, v141\n"
  "v_mfma_f32_16x16x32_bf16 a[208:211], a[52:55], v[208:211], a[208:211]\n"
  "v_exp_f32 v142, v142\n"
  "v_mfma_f32_16x16x32_bf16 a[212:215], a[52:55], v[212:215], a[212:215]\n"
  "v_exp_f32 v143, v143\n"
  "v_mfma_f32_16x16x32_bf16 a[216:219], a[52:55], v[216:219], a[216:219]\n"
  "ds_read_b128 a[24:27], v16 offset:14336\n"
  "v_cvt_pk_bf16_f32 v204, v140, v141\n"
  "v_mfma_f32_16x16x32_bf16 a[220:223], a[52:55], v[220:223], a[220:223]\n"
  "ds_read_b128 a[28:31], v16 offset:15360\n"
  "v_cvt_pk_bf16_f32 v205, v142, v143\n"
  "v_mfma_f32_16x16x32_bf16 a[224:227], a[56:59], v[208:211], a[224:227]\n"
  "v_mfma_f32_16x16x32_bf16 a[228:231], a[56:59], v[212:215], a[228:231]\n"
  "buffer_load_dwordx4 a[80:83], v2, s[4:7], s21 offen sc1\n"
  "v_mfma_f32_16x16x32_bf16 a[232:235], a[56:59], v[216:219], a[232:235]\n"
  "v_mfma_f32_16x16x32_bf16 a[236:239], a[56:59], v[220:223], a[236:239]\n"
  "buffer_load_dwordx4 a[84:87], v2, s[4:7], s21 offen offset:256 sc1\n"
  "v_mfma_f32_16x16x32_bf16 a[240:243], a[60:63], v[208:211], a[240:243]\n"
  "v_mfma_f32_16x16x32_bf16 a[244:247], a[60:63], v[212:215], a[244:247]\n"
  "buffer_load_dwordx4 a[88:91], v2, s[4:7], s21 offen offset:512 sc1\n"
  "v_mfma_f32_16x16x32_bf16 a[248:251], a[60:63], v[216:219], a[248:251]\n"
  "v_mfma_f32_16x16x32_bf16 a[252:255], a[60:63], v[220:223], a[252:255]\n"
  "buffer_load_dwordx4 a[92:95], v2, s[4:7], s21 offen offset:768 sc1\n"
  "v_mfma_f32_16x16x32_bf16 v[240:243], v[60:63], v[208:211], v[240:243]\n"
  "s_add_u32 s21, s21, 0x4000\n"
  "v_mfma_f32_16x16x32_bf16 v[244:247], v[60:63], v[212:215], v[244:247]\n"
  "v_mfma_f32_16x16x32_bf16 v[248:251], v[60:63], v[216:219], v[248:251]\n"
  "v_mfma_f32_16x16x32_bf16 v[252:255], v[60:63], v[220:223], v[252:255]\n"
  "s_waitcnt vmcnt(28) lgkmcnt(0)\n"
  "s_barrier\n"
  "s_sub_u32 s45, s61, s60\n"
  "v_add_u32 v17, s45, v17\n"
  "v_mfma_f32_16x16x32_bf16 v[160:163], a[0:3], v[64:67], v[224:227]\n"
  "v_exp_f32 v144, v144\n"
  "v_mfma_f32_16x16x32_bf16 v[164:167], a[0:3], v[80:83], v[228:231]\n"
  "v_exp_f32 v145, v145\n"
  "v_mfma_f32_16x16x32_bf16 v[168:171], a[0:3], v[96:99], v[232:235]\n"
  "v_exp_f32 v146, v146\n"
  "v_mfma_f32_16x16x32_bf16 v[172:175], a[0:3], v[112:115], v[236:239]\n"
  "v_exp_f32 v147, v147\n"
  "v_mfma_f32_16x16x32_bf16 v[160:163], a[4:7], v[68:71], v[160:163]\n"
  "ds_read_b128 a[32:35], v17\n"
  "v_cvt_pk_bf16_f32 v194, v144, v145\n"
  "v_mfma_f32_16x16x32_bf16 v[164:167], a[4:7], v[84:87], v[164:167]\n"
  "ds_read_b128 a[36:39], v17 offset:2048\n"
  "v_cvt_pk_bf16_f32 v195, v146, v147\n"
  "v_mfma_f32_16x16x32_bf16 v[168:171], a[4:7], v[100:103], v[168:171]\n"
  "v_exp_f32 v148, v148\n"
  "v_mfma_f32_16x16x32_bf16 v[172:175], a[4:7], v[116:119], v[172:175]\n"
  "v_exp_f32 v149, v149\n"
  "v_mfma_f32_16x16x32_bf16 v[160:163], a[8:11], v[72:75], v[160:163]\n"
  "v_exp_f32 v150, v150\n"
  "v_mfma_f32_16x16x32_bf16 v[164:167], a[8:11], v[88:91], v[164:167]\n"
  "v_exp_f32 v151, v151\n"
  "v_mfma_f32_16x16x32_bf16 v[168:171], a[8:11], v[104:107], v[168:171]\n"
  "ds_read_b128 a[40:43], v17 offset:4096\n"
  "v_cvt_pk_bf16_f32 v198, v148, v149\n"
  "v_mfma_f32_16x16x32_bf16 v[172:175], a[8:11], v[120:123], v[172:175]\n"
  "ds_read_b128 a[44:47], v17 offset:6144\n"
  "v_cvt_pk_bf16_f32 v199, v150, v151\n"
  "v_mfma_f32_16x16x32_bf16 v[160:163], a[12:15], v[76:79], v[160:163]\n"
  "v_exp_f32 v152, v152\n"
  "v_mfma_f32_16x16x32_bf16 v[164:167], a[12:15], v[92:95], v[164:167]\n"
  "v_exp_f32 v153, v153\n"
  "v_mfma_f32_16x16x32_bf16 v[168:171], a[12:15], v[108:111], v[168:171]\n"
  "v_exp_f32 v154, v154\n"
  "v_mfma_f32_16x16x32_bf16 v[172:175], a[12:15], v[124:127], v[172:175]\n"
  "v_exp_f32 v155, v155\n"
  "v_mfma_f32_16x16x32_bf16 v[176:179], a[16:19], v[64:67], v[224:227]\n"
  "ds_read_b128 a[48:51], v17 offset:8192\n"
  "v_cvt_pk_bf16_f32 v202, v152, v153\n"
  "v_mfma_f32_16x16x32_bf16 v[180:183], a[16:19], v[80:83], v[228:231]\n"
  "ds_read_b128 a[52:55], v17 offset:10240\n"
  "v_cvt_pk_bf16_f32 v203, v154, v155\n"
  "v_mfma_f32_16x16x32_bf16 v[184:187], a[16:19], v[96:99], v[232:235]\n"
  "v_exp_f32 v156, v156\n"
  "v_mfma_f32_16x16x32_bf16 v[188:191], a[16:19], v[112:115], v[236:239]\n"
  "v_exp_f32 v157, v157\n"
  "v_mfma_f32_16x16x32_bf16 v[176:179], a[20:23], v[68:71], v[176:179]\n"
  "v_exp_f32 v158, v158\n"
  "v_mfma_f32_16x16x32_bf16 v[180:183], a[20:23], v[84:87], v[180:183]\n"
  "v_exp_f32 v159, v159\n"
  "v_mfma_f32_16x16x32_bf16 v[184:187], a[20:23], v[100:103], v[184:187]\n"
  "ds_read_b128 a[56:59], v17 offset:12288\n"
  "v_cvt_pk_bf16_f32 v206, v156, v157\n"
  "v_mfma_f32_16x16x32_bf16 v[188:191], a[20:23], v[116:119], v[188:191]\n"
  "ds_read_b128 a[60:63], v17 offset:14336\n"
  "v_cvt_pk_bf16_f32 v207, v158, v159\n"
  "v_mfma_f32_16x16x32_bf16 v[176:179], a[24:27], v[72:75], v[176:179]\n"
  "v_mfma_f32_16x16x32_bf16 v[180:183], a[24:27], v[88:91], v[180:183]\n"
  "buffer_load_dwordx4 a[96:99], v2, s[4:7], s21 offen sc1\n"
  "v_mfma_f32_16x16x32_bf16 v[184:187], a[24:27], v[104:107], v[184:187]\n"
  "v_mfma_f32_16x16x32_bf16 v[188:191], a[24:27], v[120:123], v[188:191]\n"
  "buffer_load_dwordx4 a[100:103], v2, s[4:7], s21 offen offset:256 sc1\n"
  "v_mfma_f32_16x16x32_bf16 v[176:179], a[28:31], v[76:79], v[176:179]\n"
  "v_mfma_f32_16x16x32_bf16 v[180:183], a[28:31], v[92:95], v[180:183]\n"
  "buffer_load_dwordx4 a[104:107], v2, s[4:7], s21 offen offset:512 sc1\n"
  "v_mfma_f32_16x16x32_bf16 v[184:187], a[28:31], v[108:111], v[184:187]\n"
  "v_mfma_f32_16x16x32_bf16 v[188:191], a[28:31], v[124:127], v[188:191]\n"
  "buffer_load_dwordx4 a[108:111], v2, s[4:7], s21 offen offset:768 sc1\n"
  "s_add_u32 s21, s21, 0x4000\n"
  "s_waitcnt lgkmcnt(0)\n"
  "v_mfma_f32_16x16x32_bf16 a[128:131], a[32:35], v[192:195], a[128:131]\n"
  "v_exp_f32 v160, v160\n"
  "v_mfma_f32_16x16x32_bf16 a[132:135], a[32:35], v[196:199], a[132:135]\n"
  "v_exp_f32 v161, v161\n"
  "v_mfma_f32_16x16x32_bf16 a[136:139], a[32:35], v[200:203], a[136:139]\n"
  "v_exp_f32 v162, v162\n"
  "v_mfma_f32_16x16x32_bf16 a[140:143], a[32:35], v[204:207], a[140:143]\n"
  "v_exp_f32 v163, v163\n"
  "v_mfma_f32_16x16x32_bf16 a[144:147], a[36:39], v[192:195], a[144:147]\n"
  "v_cvt_pk_bf16_f32 v208, v160, v161\n"
  "v_mfma_f32_16x16x32_bf16 a[148:151], a[36:39], v[196:199], a[148:151]\n"
  "v_cvt_pk_bf16_f32 v209, v162, v163\n"
  "v_mfma_f32_16x16x32_bf16 a[152:155], a[36:39], v[200:203], a[152:155]\n"
  "v_exp_f32 v164, v164\n"
  "v_mfma_f32_16x16x32_bf16 a[156:159], a[36:39], v[204:207], a[156:159]\n"
  "v_exp_f32 v165, v165\n"
  "v_mfma_f32_16x16x32_bf16 a[160:163], a[40:43], v[192:195], a[160:163]\n"
  "v_exp_f32 v166, v166\n"
  "v_mfma_f32_16x16x32_bf16 a[164:167], a[40:43], v[196:199], a[164:167]\n"
  "v_exp_f32 v167, v167\n"
  "v_mfma_f32_16x16x32_bf16 a[168:171], a[40:43], v[200:203], a[168:171]\n"
  "v_cvt_pk_bf16_f32 v212, v164, v165\n"
  "v_mfma_f32_16x16x32_bf16 a[172:175], a[40:43], v[204:207], a[172:175]\n"
  "v_cvt_pk_bf16_f32 v213, v166, v167\n"
  "v_mfma_f32_16x16x32_bf16 a[176:179], a[44:47], v[192:195], a[176:179]\n"
  "v_exp_f32 v168, v168\n"
  "v_mfma_f32_16x16x32_bf16 a[180:183], a[44:47], v[196:199], a[180:183]\n"
  "v_exp_f32 v169, v169\n"
  "v_mfma_f32_16x16x32_bf16 a[184:187], a[44:47], v[200:203], a[184:187]\n"
  "v_exp_f32 v170, v170\n"
  "v_mfma_f32_16x16x32_bf16 a[188:191], a[44:47], v[204:207], a[188:191]\n"
  "v_exp_f32 v171, v171\n"
  "v_mfma_f32_16x16x32_bf16 a[192:195], a[48:51], v[192:195], a[192:195]\n"
  "v_cvt_pk_bf16_f32 v216, v168, v169\n"
  "v_mfma_f32_16x16x32_bf16 a[196:199], a[48:51], v[196:199], a[196:199]\n"
  "v_cvt_pk_bf16_f32 v217, v170, v171\n"
  "v_mfma_f32_16x16x32_bf16 a[200:203], a[48:51], v[200:203], a[200:203]\n"
  "v_exp_f32 v172, v172\n"
  "v_mfma_f32_16x16x32_bf16 a[204:207], a[48:51], v[204:207], a[204:207]\n"
  "v_exp_f32 v173, v173\n"
  "v_mfma_f32_16x16x32_bf16 a[208:211], a[52:55], v[192:195], a[208:211]\n"
  "v_exp_f32 v174, v174\n"
  "v_mfma_f32_16x16x32_bf16 a[212:215], a[52:55], v[196:199], a[212:215]\n"
  "v_exp_f32 v175, v175\n"
  "v_mfma_f32_16x16x32_bf16 a[216:219], a[52:55], v[200:203], a[216:219]\n"
  "v_cvt_pk_bf16_f32 v220, v172, v173\n"
  "v_mfma_f32_16x16x32_bf16 a[220:223], a[52:55], v[204:207], a[220:223]\n"
  "v_cvt_pk_bf16_f32 v221, v174, v175\n"
  "v_mfma_f32_16x16x32_bf16 a[224:227], a[56:59], v[192:195], a[224:227]\n"
  "v_mfma_f32_16x16x32_bf16 a[228:231], a[56:59], v[196:199], a[228:231]\n"
  "buffer_load_dwordx4 a[112:115], v2, s[4:7], s21 offen sc1\n"
  "v_mfma_f32_16x16x32_bf16 a[232:235], a[56:59], v[200:203], a[232:235]\n"
  "v_mfma_f32_16x16x32_bf16 a[236:239], a[56:59], v[204:207], a[236:239]\n"
  "buffer_load_dwordx4 a[116:119], v2, s[4:7], s21 offen offset:256 sc1\n"
  "v_mfma_f32_16x16x32_bf16 a[240:243], a[60:63], v[192:195], a[240:243]\n"
  "v_mfma_f32_16x16x32_bf16 a[244:247], a[60:63], v[196:199], a[244:247]\n"
  "buffer_load_dwordx4 a[120:123], v2, s[4:7], s21 offen offset:512 sc1\n"
  "v_mfma_f32_16x16x32_bf16 a[248:251], a[60:63], v[200:203], a[248:251]\n"
  "v_mfma_f32_16x16x32_bf16 a[252:255], a[60:63], v[204:207], a[252:255]\n"
  "buffer_load_dwordx4 a[124:127], v2, s[4:7], s21 offen offset:768 sc1\n"
  "v_mfma_f32_16x16x32_bf16 v[240:243], v[60:63], v[192:195], v[240:243]\n"
  "s_add_u32 s21, s21, 0x4000\n"
  "v_mfma_f32_16x16x32_bf16 v[244:247], v[60:63], v[196:199], v[244:247]\n"
  "v_mfma_f32_16x16x32_bf16 v[248:251], v[60:63], v[200:203], v[248:251]\n"
  "v_mfma_f32_16x16x32_bf16 v[252:255], v[60:63], v[204:207], v[252:255]\n"
  "s_waitcnt lgkmcnt(0)\n"
  "v_exp_f32 v176, v176\n"
  "v_exp_f32 v177, v177\n"
  "v_exp_f32 v178, v178\n"
  "v_exp_f32 v179, v179\n"
  "ds_read_b128 a[32:35], v17 offset:1024\n"
  "v_cvt_pk_bf16_f32 v210, v176, v177\n"
  "ds_read_b128 a[36:39], v17 offset:3072\n"
  "v_cvt_pk_bf16_f32 v211, v178, v179\n"
  "v_exp_f32 v180, v180\n"
  "v_exp_f32 v181, v181\n"
  "v_exp_f32 v182, v182\n"
  "v_exp_f32 v183, v183\n"
  "ds_read_b128 a[40:43], v17 offset:5120\n"
  "v_cvt_pk_bf16_f32 v214, v180, v181\n"
  "ds_read_b128 a[44:47], v17 offset:7168\n"
  "v_cvt_pk_bf16_f32 v215, v182, v183\n"
  "v_exp_f32 v184, v184\n"
  "v_exp_f32 v185, v185\n"
  "v_exp_f32 v186, v186\n"
  "v_exp_f32 v187, v187\n"
  "ds_read_b128 a[48:51], v17 offset:9216\n"
  "v_cvt_pk_bf16_f32 v218, v184, v185\n"
  "ds_read_b128 a[52:55], v17 offset:11264\n"
  "v_cvt_pk_bf16_f32 v219, v186, v187\n"
  "v_exp_f32 v188, v188\n"
  "v_exp_f32 v189, v189\n"
  "v_exp_f32 v190, v190\n"
  "v_exp_f32 v191, v191\n"
  "ds_read_b128 a[56:59], v17 offset:13312\n"
  "v_cvt_pk_bf16_f32 v222, v188, v189\n"
  "ds_read_b128 a[60:63], v17 offset:15360\n"
  "v_cvt_pk_bf16_f32 v223, v190, v191\n"
  "s_waitcnt lgkmcnt(7)\n"
  "v_mfma_f32_16x16x32_bf16 a[128:131], a[32:35], v[208:211], a[128:131]\n"
  "v_mfma_f32_16x16x32_bf16 a[132:135], a[32:35], v[212:215], a[132:135]\n"
  "v_mfma_f32_16x16x32_bf16 a[136:139], a[32:35], v[216:219], a[136:139]\n"
  "v_mfma_f32_16x16x32_bf16 a[140:143], a[32:35], v[220:223], a[140:143]\n"
  "s_waitcnt lgkmcnt(6)\n"
  "v_mfma_f32_16x16x32_bf16 a[144:147], a[36:39], v[208:211], a[144:147]\n"
  "v_mfma_f32_16x16x32_bf16 a[148:151], a[36:39], v[212:215], a[148:151]\n"
  "v_mfma_f32_16x16x32_bf16 a[152:155], a[36:39], v[216:219], a[152:155]\n"
  "v_mfma_f32_16x16x32_bf16 a[156:159], a[36:39], v[220:223], a[156:159]\n"
  "s_waitcnt lgkmcnt(5)\n"
  "v_mfma_f32_16x16x32_bf16 a[160:163], a[40:43], v[208:211], a[160:163]\n"
  "v_mfma_f32_16x16x32_bf16 a[164:167], a[40:43], v[212:215], a[164:167]\n"
  "v_mfma_f32_16x16x32_bf16 a[168:171], a[40:43], v[216:219], a[168:171]\n"
  "v_mfma_f32_16x16x32_bf16 a[172:175], a[40:43], v[220:223], a[172:175]\n"
  "s_waitcnt lgkmcnt(4)\n"
  "v_mfma_f32_16x16x32_bf16 a[176:179], a[44:47], v[208:211], a[176:179]\n"
  "v_mfma_f32_16x16x32_bf16 a[180:183], a[44:47], v[212:215], a[180:183]\n"
  "v_mfma_f32_16x16x32_bf16 a[184:187], a[44:47], v[216:219], a[184:187]\n"
  "v_mfma_f32_16x16x32_bf16 a[188:191], a[44:47], v[220:223], a[188:191]\n"
  "s_waitcnt lgkmcnt(3)\n"
  "v_mfma_f32_16x16x32_bf16 a[192:195], a[48:51], v[208:211], a[192:195]\n"
  "v_mfma_f32_16x16x32_bf16 a[196:199], a[48:51], v[212:215], a[196:199]\n"
  "v_mfma_f32_16x16x32_bf16 a[200:203], a[48:51], v[216:219], a[200:203]\n"
  "v_mfma_f32_16x16x32_bf16 a[204:207], a[48:51], v[220:223], a[204:207]\n"
  "s_waitcnt lgkmcnt(2)\n"
  "v_mfma_f32_16x16x32_bf16 a[208:211], a[52:55], v[208:211], a[208:211]\n"
  "v_mfma_f32_16x16x32_bf16 a[212:215], a[52:55], v[212:215], a[212:215]\n"
  "v_mfma_f32_16x16x32_bf16 a[216:219], a[52:55], v[216:219], a[216:219]\n"
  "v_mfma_f32_16x16x32_bf16 a[220:223], a[52:55], v[220:223], a[220:223]\n"
  "s_waitcnt lgkmcnt(1)\n"
  "v_mfma_f32_16x16x32_bf16 a[224:227], a[56:59], v[208:211], a[224:227]\n"
  "v_mfma_f32_16x16x32_bf16 a[228:231], a[56:59], v[212:215], a[228:231]\n"
  "v_mfma_f32_16x16x32_bf16 a[232:235], a[56:59], v[216:219], a[232:235]\n"
  "v_mfma_f32_16x16x32_bf16 a[236:239], a[56:59], v[220:223], a[236:239]\n"
  "s_waitcnt lgkmcnt(0)\n"
  "v_mfma_f32_16x16x32_bf16 a[240:243], a[60:63], v[208:211], a[240:243]\n"
  "v_mfma_f32_16x16x32_bf16 a[244:247], a[60:63], v[212:215], a[244:247]\n"
  "v_mfma_f32_16x16x32_bf16 a[248:251], a[60:63], v[216:219], a[248:251]\n"
  "v_mfma_f32_16x16x32_bf16 a[252:255], a[60:63], v[220:223], a[252:255]\n"
  "v_mfma_f32_16x16x32_bf16 v[240:243], v[60:63], v[208:211], v[240:243]\n"
  "v_mfma_f32_16x16x32_bf16 v[244:247], v[60:63], v[212:215], v[244:247]\n"
  "v_mfma_f32_16x16x32_bf16 v[248:251], v[60:63], v[216:219], v[248:251]\n"
  "v_mfma_f32_16x16x32_bf16 v[252:255], v[60:63], v[220:223], v[252:255]\n"
  "s_waitcnt lgkmcnt(0)\n"
  "s_cmp_lg_u32 s94, 0\n"
  "s_cbranch_scc1 ATT_DONE_%=\n"
  "v_mov_b32 v37, 0x4e800000\n"
  "v_mov_b32 v34, 0x18000\n"
  "s_mov_b64 s[30:31], 0\n"
  "s_nop 4\n"
  "v_cmp_nlt_f32 vcc, v240, v37\n"
  "s_nop 1\n"
  "s_or_b64 s[30:31], s[30:31], vcc\n"
  "v_cmp_nlt_f32 vcc, v244, v37\n"
  "s_nop 1\n"
  "s_or_b64 s[30:31], s[30:31], vcc\n"
  "v_cmp_nlt_f32 vcc, v248, v37\n"
  "s_nop 1\n"
  "s_or_b64 s[30:31], s[30:31], vcc\n"
  "v_cmp_nlt_f32 vcc, v252, v37\n"
  "s_nop 1\n"
  "s_or_b64 s[30:31], s[30:31], vcc\n"
  "s_cmp_eq_u64 s[30:31], 0\n"
  "s_cbranch_scc1 ATT_NOV_%=\n"
  "v_mov_b32 v37, 1\n"
  "ds_write_b32 v34, v37\n"
  "ATT_NOV_%=:\n"
  "s_waitcnt lgkmcnt(0)\n"
  "s_barrier\n"
  "ds_read_b32 v37, v34\n"
  "s_waitcnt lgkmcnt(0)\n"
  "v_readfirstlane_b32 s29, v37\n"
  "s_nop 1\n"
  "s_cmp_eq_u32 s29, 0\n"
  "s_cbranch_scc1 ATT_DONE_%=\n"
  "s_mov_b32 s94, 1\n"
  "s_waitcnt vmcnt(0)\n"
  "s_barrier\n"
  "v_add_u32 v16, 0x10000, v15\n"
  "s_mov_b32 s46, s24\n"
  "s_mov_b32 s20, 8\n"
  "ATT_MAXLOOP_%=:\n"
  "s_add_u32 m0, s24, 0x10000\n"
  "s_nop 0\n"
  "buffer_load_dwordx4 v15, s[16:19], s46 offen sc1 lds\n"
  "buffer_load_dwordx4 v15, s[16:19], s46 offen offset:1024 sc1 lds\n"
  "buffer_load_dwordx4 v15, s[16:19], s46 offen offset:2048 sc1 lds\n"
  "buffer_load_dwordx4 v15, s[16:19], s46 offen offset:3072 sc1 lds\n"
  "s_add_u32 s46, s46, 0x4000\n"
  "s_waitcnt vmcnt(0)\n"
  "s_barrier\n"
  "ds_read_b128 a[0:3], v16\n"
  "ds_read_b128 a[4:7], v16 offset:1024\n"
  "ds_read_b128 a[8:11], v16 offset:2048\n"
  "ds_read_b128 a[12:15], v16 offset:3072\n"
  "ds_read_b128 a[16:19], v16 offset:4096\n"
  "ds_read_b128 a[20:23], v16 offset:5120\n"
  "ds_read_b128 a[24:27], v16 offset:6144\n"
  "ds_read_b128 a[28:31], v16 offset:7168\n"
  "s_waitcnt lgkmcnt(0)\n"
  "v_mfma_f32_16x16x32_bf16 v[128:131], a[0:3], v[64:67], 0\n"
  "v_mfma_f32_16x16x32_bf16 v[132:135], a[0:3], v[80:83], 0\n"
  "v_mfma_f32_16x16x32_bf16 v[136:139], a[0:3], v[96:99], 0\n"
  "v_mfma_f32_16x16x32_bf16 v[140:143], a[0:3], v[112:115], 0\n"
  "v_mfma_f32_16x16x32_bf16 v[128:131], a[4:7], v[68:71], v[128:131]\n"
  "v_mfma_f32_16x16x32_bf16 v[132:135], a[4:7], v[84:87], v[132:135]\n"
  "v_mfma_f32_16x16x32_bf16 v[136:139], a[4:7], v[100:103], v[136:139]\n"
  "v_mfma_f32_16x16x32_bf16 v[140:143], a[4:7], v[116:119], v[140:143]\n"
  "v_mfma_f32_16x16x32_bf16 v[128:131], a[8:11], v[72:75], v[128:131]\n"
  "v_mfma_f32_16x16x32_bf16 v[132:135], a[8:11], v[88:91], v[132:135]\n"
  "v_mfma_f32_16x16x32_bf16 v[136:139], a[8:11], v[104:107], v[136:139]\n"
  "v_mfma_f32_16x16x32_bf16 v[140:143], a[8:11], v[120:123], v[140:143]\n"
  "v_mfma_f32_16x16x32_bf16 v[128:131], a[12:15], v[76:79], v[128:131]\n"
  "v_mfma_f32_16x16x32_bf16 v[132:135], a[12:15], v[92:95], v[132:135]\n"
  "v_mfma_f32_16x16x32_bf16 v[136:139], a[12:15], v[108:111], v[136:139]\n"
  "v_mfma_f32_16x16x32_bf16 v[140:143], a[12:15], v[124:127], v[140:143]\n"
  "v_mfma_f32_16x16x32_bf16 v[144:147], a[16:19], v[64:67], 0\n"
  "v_mfma_f32_16x16x32_bf16 v[148:151], a[16:19], v[80:83], 0\n"
  "v_mfma_f32_16x16x32_bf16 v[152:155], a[16:19], v[96:99], 0\n"
  "v_mfma_f32_16x16x32_bf16 v[156:159], a[16:19], v[112:115], 0\n"
  "v_mfma_f32_16x16x32_bf16 v[144:147], a[20:23], v[68:71], v[144:147]\n"
  "v_mfma_f32_16x16x32_bf16 v[148:151], a[20:23], v[84:87], v[148:151]\n"
  "v_mfma_f32_16x16x32_bf16 v[152:155], a[20:23], v[100:103], v[152:155]\n"
  "v_mfma_f32_16x16x32_bf16 v[156:159], a[20:23], v[116:119], v[156:159]\n"
  "v_mfma_f32_16x16x32_bf16 v[144:147], a[24:27], v[72:75], v[144:147]\n"
  "v_mfma_f32_16x16x32_bf16 v[148:151], a[24:27], v[88:91], v[148:151]\n"
  "v_mfma_f32_16x16x32_bf16 v[152:155], a[24:27], v[104:107], v[152:155]\n"
  "v_mfma_f32_16x16x32_bf16 v[156:159], a[24:27], v[120:123], v[156:159]\n"
  "v_mfma_f32_16x16x32_bf16 v[144:147], a[28:31], v[76:79], v[144:147]\n"
  "v_mfma_f32_16x16x32_bf16 v[148:151], a[28:31], v[92:95], v[148:151]\n"
  "v_mfma_f32_16x16x32_bf16 v[152:155], a[28:31], v[108:111], v[152:155]\n"
  "v_mfma_f32_16x16x32_bf16 v[156:159], a[28:31], v[124:127], v[156:159]\n"
  "s_nop 7\n"
  "s_nop 7\n"
  "v_max3_f32 v26, v26, v128, v129\n"
  "v_max3_f32 v26, v26, v130, v131\n"
  "v_max3_f32 v26, v26, v144, v145\n"
  "v_max3_f32 v26, v26, v146, v147\n"
  "v_max3_f32 v27, v27, v132, v133\n"
  "v_max3_f32 v27, v27, v134, v135\n"
  "v_max3_f32 v27, v27, v148, v149\n"
  "v_max3_f32 v27, v27, v150, v151\n"
  "v_max3_f32 v28, v28, v136, v137\n"
  "v_max3_f32 v28, v28, v138, v139\n"
  "v_max3_f32 v28, v28, v152, v153\n"
  "v_max3_f32 v28, v28, v154, v155\n"
  "v_max3_f32 v29, v29, v140, v141\n"
  "v_max3_f32 v29, v29, v142, v143\n"
  "v_max3_f32 v29, v29, v156, v157\n"
  "v_max3_f32 v29, v29, v158, v159\n"
  "ds_read_b128 a[0:3], v16 offset:8192\n"
  "ds_read_b128 a[4:7], v16 offset:9216\n"
  "ds_read_b128 a[8:11], v16 offset:10240\n"
  "ds_read_b128 a[12:15], v16 offset:11264\n"
  "ds_read_b128 a[16:19], v16 offset:12288\n"
  "ds_read_b128 a[20:23], v16 offset:13312\n"
  "ds_read_b128 a[24:27], v16 offset:14336\n"
  "ds_read_b128 a[28:31], v16 offset:15360\n"
  "s_waitcnt lgkmcnt(0)\n"
  "v_mfma_f32_16x16x32_bf16 v[160:163], a[0:3], v[64:67], 0\n"
  "v_mfma_f32_16x16x32_bf16 v[164:167], a[0:3], v[80:83], 0\n"
  "v_mfma_f32_16x16x32_bf16 v[168:171], a[0:3], v[96:99], 0\n"
  "v_mfma_f32_16x16x32_bf16 v[172:175], a[0:3], v[112:115], 0\n"
  "v_mfma_f32_16x16x32_bf16 v[160:163], a[4:7], v[68:71], v[160:163]\n"
  "v_mfma_f32_16x16x32_bf16 v[164:167], a[4:7], v[84:87], v[164:167]\n"
  "v_mfma_f32_16x16x32_bf16 v[168:171], a[4:7], v[100:103], v[168:171]\n"
  "v_mfma_f32_16x16x32_bf16 v[172:175], a[4:7], v[116:119], v[172:175]\n"
  "v_mfma_f32_16x16x32_bf16 v[160:163], a[8:11], v[72:75], v[160:163]\n"
  "v_mfma_f32_16x16x32_bf16 v[164:167], a[8:11], v[88:91], v[164:167]\n"
  "v_mfma_f32_16x16x32_bf16 v[168:171], a[8:11], v[104:107], v[168:171]\n"
  "v_mfma_f32_16x16x32_bf16 v[172:175], a[8:11], v[120:123], v[172:175]\n"
  "v_mfma_f32_16x16x32_bf16 v[160:163], a[12:15], v[76:79], v[160:163]\n"
  "v_mfma_f32_16x16x32_bf16 v[164:167], a[12:15], v[92:95], v[164:167]\n"
  "v_mfma_f32_16x16x32_bf16 v[168:171], a[12:15], v[108:111], v[168:171]\n"
  "v_mfma_f32_16x16x32_bf16 v[172:175], a[12:15], v[124:127], v[172:175]\n"
  "v_mfma_f32_16x16x32_bf16 v[176:179], a[16:19], v[64:67], 0\n"
  "v_mfma_f32_16x16x32_bf16 v[180:183], a[16:19], v[80:83], 0\n"
  "v_mfma_f32_16x16x32_bf16 v[184:187], a[16:19], v[96:99], 0\n"
  "v_mfma_f32_16x16x32_bf16 v[188:191], a[16:19], v[112:115], 0\n"
  "v_mfma_f32_16x16x32_bf16 v[176:179], a[20:23], v[68:71], v[176:179]\n"
  "v_mfma_f32_16x16x32_bf16 v[180:183], a[20:23], v[84:87], v[180:183]\n"
  "v_mfma_f32_16x16x32_bf16 v[184:187], a[20:23], v[100:103], v[184:187]\n"
  "v_mfma_f32_16x16x32_bf16 v[188:191], a[20:23], v[116:119], v[188:191]\n"
  "v_mfma_f32_16x16x32_bf16 v[176:179], a[24:27], v[72:75], v[176:179]\n"
  "v_mfma_f32_16x16x32_bf16 v[180:183], a[24:27], v[88:91], v[180:183]\n"
  "v_mfma_f32_16x16x32_bf16 v[184:187], a[24:27], v[104:107], v[184:187]\n"
  "v_mfma_f32_16x16x32_bf16 v[188:191], a[24:27], v[120:123], v[188:191]\n"
  "v_mfma_f32_16x16x32_bf16 v[176:179], a[28:31], v[76:79], v[176:179]\n"
  "v_mfma_f32_16x16x32_bf16 v[180:183], a[28:31], v[92:95], v[180:183]\n"
  "v_mfma_f32_16x16x32_bf16 v[184:187], a[28:31], v[108:111], v[184:187]\n"
  "v_mfma_f32_16x16x32_bf16 v[188:191], a[28:31], v[124:127], v[188:191]\n"
  "s_nop 7\n"
  "s_nop 7\n"
  "v_max3_f32 v26, v26, v160, v161\n"
  "v_max3_f32 v26, v26, v162, v163\n"
  "v_max3_f32 v26, v26, v176, v177\n"
  "v_max3_f32 v26, v26, v178, v179\n"
  "v_max3_f32 v27, v27, v164, v165\n"
  "v_max3_f32 v27, v27, v166, v167\n"
  "v_max3_f32 v27, v27, v180, v181\n"
  "v_max3_f32 v27, v27, v182, v183\n"
  "v_max3_f32 v28, v28, v168, v169\n"
  "v_max3_f32 v28, v28, v170, v171\n"
  "v_max3_f32 v28, v28, v184, v185\n"
  "v_max3_f32 v28, v28, v186, v187\n"
  "v_max3_f32 v29, v29, v172, v173\n"
  "v_max3_f32 v29, v29, v174, v175\n"
  "v_max3_f32 v29, v29, v188, v189\n"
  "v_max3_f32 v29, v29, v190, v191\n"
  "s_barrier\n"
  "s_sub_u32 s20, s20, 1\n"
  "s_cmp_lg_u32 s20, 0\n"
  "s_cbranch_scc1 ATT_MAXLOOP_%=\n"
  "v_mov_b32 v36, v26\n"
  "s_nop 1\n"
  "v_permlane32_swap_b32 v26, v36\n"
  "v_max_f32 v26, v26, v36\n"
  "v_mov_b32 v36, v26\n"
  "s_nop 1\n"
  "v_permlane16_swap_b32 v26, v36\n"
  "v_max_f32 v26, v26, v36\n"
  "v_mov_b32 v36, v27\n"
  "s_nop 1\n"
  "v_permlane32_swap_b32 v27, v36\n"
  "v_max_f32 v27, v27, v36\n"
  "v_mov_b32 v36, v27\n"
  "s_nop 1\n"
  "v_permlane16_swap_b32 v27, v36\n"
  "v_max_f32 v27, v27, v36\n"
  "v_mov_b32 v36, v28\n"
  "s_nop 1\n"
  "v_permlane32_swap_b32 v28, v36\n"
  "v_max_f32 v28, v28, v36\n"
  "v_mov_b32 v36, v28\n"
  "s_nop 1\n"
  "v_permlane16_swap_b32 v28, v36\n"
  "v_max_f32 v28, v28, v36\n"
  "v_mov_b32 v36, v29\n"
  "s_nop 1\n"
  "v_permlane32_swap_b32 v29, v36\n"
  "v_max_f32 v29, v29, v36\n"
  "v_mov_b32 v36, v29\n"
  "s_nop 1\n"
  "v_permlane16_swap_b32 v29, v36\n"
  "v_max_f32 v29, v29, v36\n"
  "s_add_u32 m0, s24, 0x10000\n"
  "s_nop 0\n"
  "buffer_load_dwordx4 v15, s[16:19], s24 offen sc1 lds\n"
  "buffer_load_dwordx4 v15, s[16:19], s24 offen offset:1024 sc1 lds\n"
  "buffer_load_dwordx4 v15, s[16:19], s24 offen offset:2048 sc1 lds\n"
  "buffer_load_dwordx4 v15, s[16:19], s24 offen offset:3072 sc1 lds\n"
  "s_add_u32 m0, s24, 0x1c000\n"
  "s_nop 0\n"
  "buffer_load_dwordx4 v15, s[56:59], s24 offen sc1 lds\n"
  "buffer_load_dwordx4 v15, s[56:59], s24 offen offset:1024 sc1 lds\n"
  "buffer_load_dwordx4 v15, s[56:59], s24 offen offset:2048 sc1 lds\n"
  "buffer_load_dwordx4 v15, s[56:59], s24 offen offset:3072 sc1 lds\n"
  "s_add_u32 s29, s24, 0x4000\n"
  "s_add_u32 m0, s24, 0x14000\n"
  "s_nop 0\n"
  "buffer_load_dwordx4 v15, s[16:19], s29 offen sc1 lds\n"
  "buffer_load_dwordx4 v15, s[16:19], s29 offen offset:1024 sc1 lds\n"
  "buffer_load_dwordx4 v15, s[16:19], s29 offen offset:2048 sc1 lds\n"
  "buffer_load_dwordx4 v15, s[16:19], s29 offen offset:3072 sc1 lds\n"
  "s_waitcnt vmcnt(0)\n"
  "s_barrier\n"
  "s_mov_b32 s21, 0\n"
  "s_mul_i32 s22, s25, 0x1c000\n"
  "s_branch ATT_START_%=\n"
  "ATT_DONE_%=:\n"
  "s_nop 7\n"
  "s_cmp_ge_u32 s27, 28\n"
  "s_cbranch_scc1 F1_SKIP_%=\n"
  "s_lshl_b32 s29, s38, 21\n"
  "s_add_u32 s12, s80, 0x600000\n"
  "s_addc_u32 s13, s81, 0\n"
  "s_add_u32 s12, s12, s29\n"
  "s_addc_u32 s13, s13, 0\n"
  "s_and_b32 s13, s13, 0xffff\n"
  "s_mov_b32 s14, 0x200000\n"
  "s_mov_b32 s15, 0x20000\n"
  "s_lshl_b32 s91, s39, 16\n"
  "s_mov_b32 s88, -1\n"
  "v_rcp_f32 v26, v240\n"
  "v_rcp_f32 v27, v244\n"
  "v_rcp_f32 v28, v248\n"
  "v_rcp_f32 v29, v252\n"
  "v_accvgpr_read_b32 v136, a128\n"
  "v_accvgpr_read_b32 v137, a129\n"
  "v_accvgpr_read_b32 v138, a130\n"
  "v_accvgpr_read_b32 v139, a131\n"
  "v_accvgpr_read_b32 v140, a144\n"
  "v_accvgpr_read_b32 v141, a145\n"
  "v_accvgpr_read_b32 v142, a146\n"
  "v_accvgpr_read_b32 v143, a147\n"
  "v_mul_f32 v136, v136, v26\n"
  "v_mul_f32 v137, v137, v26\n"
  "v_mul_f32 v138, v138, v26\n"
  "v_mul_f32 v139, v139, v26\n"
  "v_mul_f32 v140, v140, v26\n"
  "v_mul_f32 v141, v141, v26\n"
  "v_mul_f32 v142, v142, v26\n"
  "v_mul_f32 v143, v143, v26\n"
  "v_cvt_pk_bf16_f32 v64, v136, v137\n"
  "v_cvt_pk_bf16_f32 v65, v138, v139\n"
  "v_cvt_pk_bf16_f32 v66, v140, v141\n"
  "v_cvt_pk_bf16_f32 v67, v142, v143\n"
  "v_accvgpr_read_b32 v144, a132\n"
  "v_accvgpr_read_b32 v145, a133\n"
  "v_accvgpr_read_b32 v146, a134\n"
  "v_accvgpr_read_b32 v147, a135\n"
  "v_accvgpr_read_b32 v148, a148\n"
  "v_accvgpr_read_b32 v149, a149\n"
  "v_accvgpr_read_b32 v150, a150\n"
  "v_accvgpr_read_b32 v151, a151\n"
  "v_mul_f32 v144, v144, v27\n"
  "v_mul_f32 v145, v145, v27\n"
  "v_mul_f32 v146, v146, v27\n"
  "v_mul_f32 v147, v147, v27\n"
  "v_mul_f32 v148, v148, v27\n"
  "v_mul_f32 v149, v149, v27\n"
  "v_mul_f32 v150, v150, v27\n"
  "v_mul_f32 v151, v151, v27\n"
  "v_cvt_pk_bf16_f32 v68, v144, v145\n"
  "v_cvt_pk_bf16_f32 v69, v146, v147\n"
  "v_cvt_pk_bf16_f32 v70, v148, v149\n"
  "v_cvt_pk_bf16_f32 v71, v150, v151\n"
  "v_accvgpr_read_b32 v152, a136\n"
  "v_accvgpr_read_b32 v153, a137\n"
  "v_accvgpr_read_b32 v154, a138\n"
  "v_accvgpr_read_b32 v155, a139\n"
  "v_accvgpr_read_b32 v156, a152\n"
  "v_accvgpr_read_b32 v157, a153\n"
  "v_accvgpr_read_b32 v158, a154\n"
  "v_accvgpr_read_b32 v159, a155\n"
  "v_mul_f32 v152, v152, v28\n"
  "v_mul_f32 v153, v153, v28\n"
  "v_mul_f32 v154, v154, v28\n"
  "v_mul_f32 v155, v155, v28\n"
  "v_mul_f32 v156, v156, v28\n"
  "v_mul_f32 v157, v157, v28\n"
  "v_mul_f32 v158, v158, v28\n"
  "v_mul_f32 v159, v159, v28\n"
  "v_cvt_pk_bf16_f32 v72, v152, v153\n"
  "v_cvt_pk_bf16_f32 v73, v154, v155\n"
  "v_cvt_pk_bf16_f32 v74, v156, v157\n"
  "v_cvt_pk_bf16_f32 v75, v158, v159\n"
  "v_accvgpr_read_b32 v128, a140\n"
  "v_accvgpr_read_b32 v129, a141\n"
  "v_accvgpr_read_b32 v130, a142\n"
  "v_accvgpr_read_b32 v131, a143\n"
  "v_accvgpr_read_b32 v132, a156\n"
  "v_accvgpr_read_b32 v133, a157\n"
  "v_accvgpr_read_b32 v134, a158\n"
  "v_accvgpr_read_b32 v135, a159\n"
  "v_mul_f32 v128, v128, v29\n"
  "v_mul_f32 v129, v129, v29\n"
  "v_mul_f32 v130, v130, v29\n"
  "v_mul_f32 v131, v131, v29\n"
  "v_mul_f32 v132, v132, v29\n"
  "v_mul_f32 v133, v133, v29\n"
  "v_mul_f32 v134, v134, v29\n"
  "v_mul_f32 v135, v135, v29\n"
  "v_cvt_pk_bf16_f32 v76, v128, v129\n"
  "v_cvt_pk_bf16_f32 v77, v130, v131\n"
  "v_cvt_pk_bf16_f32 v78, v132, v133\n"
  "v_cvt_pk_bf16_f32 v79, v134, v135\n"
  "buffer_load_dwordx4 a[128:131], v2, s[4:7], s21 offen sc1\n"
  "buffer_load_dwordx4 a[132:135], v2, s[4:7], s21 offen offset:256 sc1\n"
  "buffer_load_dwordx4 a[136:139], v2, s[4:7], s21 offen offset:512 sc1\n"
  "buffer_load_dwordx4 a[140:143], v2, s[4:7], s21 offen offset:768 sc1\n"
  "s_add_u32 s21, s21, 0x4000\n"
  "buffer_load_dwordx4 a[144:147], v2, s[4:7], s21 offen sc1\n"
  "buffer_load_dwordx4 a[148:151], v2, s[4:7], s21 offen offset:256 sc1\n"
  "buffer_load_dwordx4 a[152:155], v2, s[4:7], s21 offen offset:512 sc1\n"
  "buffer_load_dwordx4 a[156:159], v2, s[4:7], s21 offen offset:768 sc1\n"
  "s_add_u32 s21, s21, 0x4000\n"
  "v_accvgpr_read_b32 v136, a160\n"
  "v_accvgpr_read_b32 v137, a161\n"
  "v_accvgpr_read_b32 v138, a162\n"
  "v_accvgpr_read_b32 v139, a163\n"
  "v_accvgpr_read_b32 v140, a176\n"
  "v_accvgpr_read_b32 v141, a177\n"
  "v_accvgpr_read_b32 v142, a178\n"
  "v_accvgpr_read_b32 v143, a179\n"
  "v_mul_f32 v136, v136, v26\n"
  "v_mul_f32 v137, v137, v26\n"
  "v_mul_f32 v138, v138, v26\n"
  "v_mul_f32 v139, v139, v26\n"
  "v_mul_f32 v140, v140, v26\n"
  "v_mul_f32 v141, v141, v26\n"
  "v_mul_f32 v142, v142, v26\n"
  "v_mul_f32 v143, v143, v26\n"
  "v_cvt_pk_bf16_f32 v80, v136, v137\n"
  "v_cvt_pk_bf16_f32 v81, v138, v139\n"
  "v_cvt_pk_bf16_f32 v82, v140, v141\n"
  "v_cvt_pk_bf16_f32 v83, v142, v143\n"
  "v_accvgpr_read_b32 v144, a164\n"
  "v_accvgpr_read_b32 v145, a165\n"
  "v_accvgpr_read_b32 v146, a166\n"
  "v_accvgpr_read_b32 v147, a167\n"
  "v_accvgpr_read_b32 v148, a180\n"
  "v_accvgpr_read_b32 v149, a181\n"
  "v_accvgpr_read_b32 v150, a182\n"
  "v_accvgpr_read_b32 v151, a183\n"
  "v_mul_f32 v144, v144, v27\n"
  "v_mul_f32 v145, v145, v27\n"
  "v_mul_f32 v146, v146, v27\n"
  "v_mul_f32 v147, v147, v27\n"
  "v_mul_f32 v148, v148, v27\n"
  "v_mul_f32 v149, v149, v27\n"
  "v_mul_f32 v150, v150, v27\n"
  "v_mul_f32 v151, v151, v27\n"
  "v_cvt_pk_bf16_f32 v84, v144, v145\n"
  "v_cvt_pk_bf16_f32 v85, v146, v147\n"
  "v_cvt_pk_bf16_f32 v86, v148, v149\n"
  "v_cvt_pk_bf16_f32 v87, v150, v151\n"
  "v_accvgpr_read_b32 v152, a168\n"
  "v_accvgpr_read_b32 v153, a169\n"
  "v_accvgpr_read_b32 v154, a170\n"
  "v_accvgpr_read_b32 v155, a171\n"
  "v_accvgpr_read_b32 v156, a184\n"
  "v_accvgpr_read_b32 v157, a185\n"
  "v_accvgpr_read_b32 v158, a186\n"
  "v_accvgpr_read_b32 v159, a187\n"
  "v_mul_f32 v152, v152, v28\n"
  "v_mul_f32 v153, v153, v28\n"
  "v_mul_f32 v154, v154, v28\n"
  "v_mul_f32 v155, v155, v28\n"
  "v_mul_f32 v156, v156, v28\n"
  "v_mul_f32 v157, v157, v28\n"
  "v_mul_f32 v158, v158, v28\n"
  "v_mul_f32 v159, v159, v28\n"
  "v_cvt_pk_bf16_f32 v88, v152, v153\n"
  "v_cvt_pk_bf16_f32 v89, v154, v155\n"
  "v_cvt_pk_bf16_f32 v90, v156, v157\n"
  "v_cvt_pk_bf16_f32 v91, v158, v159\n"
  "v_accvgpr_read_b32 v128, a172\n"
  "v_accvgpr_read_b32 v129, a173\n"
  "v_accvgpr_read_b32 v130, a174\n"
  "v_accvgpr_read_b32 v131, a175\n"
  "v_accvgpr_read_b32 v132, a188\n"
  "v_accvgpr_read_b32 v133, a189\n"
  "v_accvgpr_read_b32 v134, a190\n"
  "v_accvgpr_read_b32 v135, a191\n"
  "v_mul_f32 v128, v128, v29\n"
  "v_mul_f32 v129, v129, v29\n"
  "v_mul_f32 v130, v130, v29\n"
  "v_mul_f32 v131, v131, v29\n"
  "v_mul_f32 v132, v132, v29\n"
  "v_mul_f32 v133, v133, v29\n"
  "v_mul_f32 v134, v134, v29\n"
  "v_mul_f32 v135, v135, v29\n"
  "v_cvt_pk_bf16_f32 v92, v128, v129\n"
  "v_cvt_pk_bf16_f32 v93, v130, v131\n"
  "v_cvt_pk_bf16_f32 v94, v132, v133\n"
  "v_cvt_pk_bf16_f32 v95, v134, v135\n"
  "buffer_load_dwordx4 a[160:163], v2, s[4:7], s21 offen sc1\n"
  "buffer_load_dwordx4 a[164:167], v2, s[4:7], s21 offen offset:256 sc1\n"
  "buffer_load_dwordx4 a[168:171], v2, s[4:7], s21 offen offset:512 sc1\n"
  "buffer_load_dwordx4 a[172:175], v2, s[4:7], s21 offen offset:768 sc1\n"
  "s_add_u32 s21, s21, 0x4000\n"
  "v_accvgpr_read_b32 v136, a192\n"
  "v_accvgpr_read_b32 v137, a193\n"
  "v_accvgpr_read_b32 v138, a194\n"
  "v_accvgpr_read_b32 v139, a195\n"
  "v_accvgpr_read_b32 v140, a208\n"
  "v_accvgpr_read_b32 v141, a209\n"
  "v_accvgpr_read_b32 v142, a210\n"
  "v_accvgpr_read_b32 v143, a211\n"
  "v_mul_f32 v136, v136, v26\n"
  "v_mul_f32 v137, v137, v26\n"
  "v_mul_f32 v138, v138, v26\n"
  "v_mul_f32 v139, v139, v26\n"
  "v_mul_f32 v140, v140, v26\n"
  "v_mul_f32 v141, v141, v26\n"
  "v_mul_f32 v142, v142, v26\n"
  "v_mul_f32 v143, v143, v26\n"
  "v_cvt_pk_bf16_f32 v96, v136, v137\n"
  "v_cvt_pk_bf16_f32 v97, v138, v139\n"
  "v_cvt_pk_bf16_f32 v98, v140, v141\n"
  "v_cvt_pk_bf16_f32 v99, v142, v143\n"
  "v_accvgpr_read_b32 v144, a196\n"
  "v_accvgpr_read_b32 v145, a197\n"
  "v_accvgpr_read_b32 v146, a198\n"
  "v_accvgpr_read_b32 v147, a199\n"
  "v_accvgpr_read_b32 v148, a212\n"
  "v_accvgpr_read_b32 v149, a213\n"
  "v_accvgpr_read_b32 v150, a214\n"
  "v_accvgpr_read_b32 v151, a215\n"
  "v_mul_f32 v144, v144, v27\n"
  "v_mul_f32 v145, v145, v27\n"
  "v_mul_f32 v146, v146, v27\n"
  "v_mul_f32 v147, v147, v27\n"
  "v_mul_f32 v148, v148, v27\n"
  "v_mul_f32 v149, v149, v27\n"
  "v_mul_f32 v150, v150, v27\n"
  "v_mul_f32 v151, v151, v27\n"
  "v_cvt_pk_bf16_f32 v100, v144, v145\n"
  "v_cvt_pk_bf16_f32 v101, v146, v147\n"
  "v_cvt_pk_bf16_f32 v102, v148, v149\n"
  "v_cvt_pk_bf16_f32 v103, v150, v151\n"
  "v_accvgpr_read_b32 v152, a200\n"
  "v_accvgpr_read_b32 v153, a201\n"
  "v_accvgpr_read_b32 v154, a202\n"
  "v_accvgpr_read_b32 v155, a203\n"
  "v_accvgpr_read_b32 v156, a216\n"
  "v_accvgpr_read_b32 v157, a217\n"
  "v_accvgpr_read_b32 v158, a218\n"
  "v_accvgpr_read_b32 v159, a219\n"
  "v_mul_f32 v152, v152, v28\n"
  "v_mul_f32 v153, v153, v28\n"
  "v_mul_f32 v154, v154, v28\n"
  "v_mul_f32 v155, v155, v28\n"
  "v_mul_f32 v156, v156, v28\n"
  "v_mul_f32 v157, v157, v28\n"
  "v_mul_f32 v158, v158, v28\n"
  "v_mul_f32 v159, v159, v28\n"
  "v_cvt_pk_bf16_f32 v104, v152, v153\n"
  "v_cvt_pk_bf16_f32 v105, v154, v155\n"
  "v_cvt_pk_bf16_f32 v106, v156, v157\n"
  "v_cvt_pk_bf16_f32 v107, v158, v159\n"
  "v_accvgpr_read_b32 v128, a204\n"
  "v_accvgpr_read_b32 v129, a205\n"
  "v_accvgpr_read_b32 v130, a206\n"
  "v_accvgpr_read_b32 v131, a207\n"
  "v_accvgpr_read_b32 v132, a220\n"
  "v_accvgpr_read_b32 v133, a221\n"
  "v_accvgpr_read_b32 v134, a222\n"
  "v_accvgpr_read_b32 v135, a223\n"
  "v_mul_f32 v128, v128, v29\n"
  "v_mul_f32 v129, v129, v29\n"
  "v_mul_f32 v130, v130, v29\n"
  "v_mul_f32 v131, v131, v29\n"
  "v_mul_f32 v132, v132, v29\n"
  "v_mul_f32 v133, v133, v29\n"
  "v_mul_f32 v134, v134, v29\n"
  "v_mul_f32 v135, v135, v29\n"
  "v_cvt_pk_bf16_f32 v108, v128, v129\n"
  "v_cvt_pk_bf16_f32 v109, v130, v131\n"
  "v_cvt_pk_bf16_f32 v110, v132, v133\n"
  "v_cvt_pk_bf16_f32 v111, v134, v135\n"
  "s_waitcnt vmcnt(24)\n"
  "s_barrier\n"
  "ds_read_b64_tr_b16 a[0:1], v7\n"
  "ds_read_b64_tr_b16 a[2:3], v7 offset:4096\n"
  "ds_read_b64_tr_b16 a[4:5], v8\n"
  "ds_read_b64_tr_b16 a[6:7], v8 offset:4096\n"
  "ds_read_b64_tr_b16 a[8:9], v9\n"
  "ds_read_b64_tr_b16 a[10:11], v9 offset:4096\n"
  "ds_read_b64_tr_b16 a[12:13], v10\n"
  "ds_read_b64_tr_b16 a[14:15], v10 offset:4096\n"
  "ds_read_b64_tr_b16 a[16:17], v11\n"
  "ds_read_b64_tr_b16 a[18:19], v11 offset:4096\n"
  "ds_read_b64_tr_b16 a[20:21], v12\n"
  "ds_read_b64_tr_b16 a[22:23], v12 offset:4096\n"
  "ds_read_b64_tr_b16 a[24:25], v13\n"
  "ds_read_b64_tr_b16 a[26:27], v13 offset:4096\n"
  "ds_read_b64_tr_b16 a[28:29], v14\n"
  "ds_read_b64_tr_b16 a[30:31], v14 offset:4096\n"
  "v_accvgpr_read_b32 v136, a224\n"
  "v_accvgpr_read_b32 v137, a225\n"
  "v_accvgpr_read_b32 v138, a226\n"
  "v_accvgpr_read_b32 v139, a227\n"
  "v_accvgpr_read_b32 v140, a240\n"
  "v_accvgpr_read_b32 v141, a241\n"
  "v_accvgpr_read_b32 v142, a242\n"
  "v_accvgpr_read_b32 v143, a243\n"
  "v_mul_f32 v136, v136, v26\n"
  "v_mul_f32 v137, v137, v26\n"
  "v_mul_f32 v138, v138, v26\n"
  "v_mul_f32 v139, v139, v26\n"
  "v_mul_f32 v140, v140, v26\n"
  "v_mul_f32 v141, v141, v26\n"
  "v_mul_f32 v142, v142, v26\n"
  "v_mul_f32 v143, v143, v26\n"
  "v_cvt_pk_bf16_f32 v112, v136, v137\n"
  "v_cvt_pk_bf16_f32 v113, v138, v139\n"
  "v_cvt_pk_bf16_f32 v114, v140, v141\n"
  "v_cvt_pk_bf16_f32 v115, v142, v143\n"
  "v_accvgpr_read_b32 v144, a228\n"
  "v_accvgpr_read_b32 v145, a229\n"
  "v_accvgpr_read_b32 v146, a230\n"
  "v_accvgpr_read_b32 v147, a231\n"
  "v_accvgpr_read_b32 v148, a244\n"
  "v_accvgpr_read_b32 v149, a245\n"
  "v_accvgpr_read_b32 v150, a246\n"
  "v_accvgpr_read_b32 v151, a247\n"
  "v_mul_f32 v144, v144, v27\n"
  "v_mul_f32 v145, v145, v27\n"
  "v_mul_f32 v146, v146, v27\n"
  "v_mul_f32 v147, v147, v27\n"
  "v_mul_f32 v148, v148, v27\n"
  "v_mul_f32 v149, v149, v27\n"
  "v_mul_f32 v150, v150, v27\n"
  "v_mul_f32 v151, v151, v27\n"
  "v_cvt_pk_bf16_f32 v116, v144, v145\n"
  "v_cvt_pk_bf16_f32 v117, v146, v147\n"
  "v_cvt_pk_bf16_f32 v118, v148, v149\n"
  "v_cvt_pk_bf16_f32 v119, v150, v151\n"
  "v_accvgpr_read_b32 v152, a232\n"
  "v_accvgpr_read_b32 v153, a233\n"
  "v_accvgpr_read_b32 v154, a234\n"
  "v_accvgpr_read_b32 v155, a235\n"
  "v_accvgpr_read_b32 v156, a248\n"
  "v_accvgpr_read_b32 v157, a249\n"
  "v_accvgpr_read_b32 v158, a250\n"
  "v_accvgpr_read_b32 v159, a251\n"
  "v_mul_f32 v152, v152, v28\n"
  "v_mul_f32 v153, v153, v28\n"
  "v_mul_f32 v154, v154, v28\n"
  "v_mul_f32 v155, v155, v28\n"
  "v_mul_f32 v156, v156, v28\n"
  "v_mul_f32 v157, v157, v28\n"
  "v_mul_f32 v158, v158, v28\n"
  "v_mul_f32 v159, v159, v28\n"
  "v_cvt_pk_bf16_f32 v120, v152, v153\n"
  "v_cvt_pk_bf16_f32 v121, v154, v155\n"
  "v_cvt_pk_bf16_f32 v122, v156, v157\n"
  "v_cvt_pk_bf16_f32 v123, v158, v159\n"
  "v_accvgpr_read_b32 v128, a236\n"
  "v_accvgpr_read_b32 v129, a237\n"
  "v_accvgpr_read_b32 v130, a238\n"
  "v_accvgpr_read_b32 v131, a239\n"
  "v_accvgpr_read_b32 v132, a252\n"
  "v_accvgpr_read_b32 v133, a253\n"
  "v_accvgpr_read_b32 v134, a254\n"
  "v_accvgpr_read_b32 v135, a255\n"
  "v_mul_f32 v128, v128, v29\n"
  "v_mul_f32 v129, v129, v29\n"
  "v_mul_f32 v130, v130, v29\n"
  "v_mul_f32 v131, v131, v29\n"
  "v_mul_f32 v132, v132, v29\n"
  "v_mul_f32 v133, v133, v29\n"
  "v_mul_f32 v134, v134, v29\n"
  "v_mul_f32 v135, v135, v29\n"
  "v_cvt_pk_bf16_f32 v124, v128, v129\n"
  "v_cvt_pk_bf16_f32 v125, v130, v131\n"
  "v_cvt_pk_bf16_f32 v126, v132, v133\n"
  "v_cvt_pk_bf16_f32 v127, v134, v135\n"
  "s_mov_b32 s16, s72\n"
  "s_and_b32 s17, s73, 0xffff\n"
  "s_mov_b32 s18, 0x1c00\n"
  "s_mov_b32 s19, 0x20000\n"
  "v_lshrrev_b32 v18, 4, v1\n"
  "v_lshlrev_b32 v18, 3, v18\n"
  "s_lshl_b32 s30, s40, 8\n"
  "buffer_load_dwordx2 a[192:193], v18, s[16:19], s30 offen\n"
  "buffer_load_dwordx2 a[194:195], v18, s[16:19], s30 offen offset:32\n"
  "buffer_load_dwordx2 a[196:197], v18, s[16:19], s30 offen offset:64\n"
  "buffer_load_dwordx2 a[198:199], v18, s[16:19], s30 offen offset:96\n"
  "buffer_load_dwordx2 a[200:201], v18, s[16:19], s30 offen offset:128\n"
  "buffer_load_dwordx2 a[202:203], v18, s[16:19], s30 offen offset:160\n"
  "buffer_load_dwordx2 a[204:205], v18, s[16:19], s30 offen offset:192\n"
  "buffer_load_dwordx2 a[206:207], v18, s[16:19], s30 offen offset:224\n"
  "s_mov_b32 s20, 3\n"
  "s_waitcnt lgkmcnt(0)\n"
  "s_swappc_b64 s[48:49], s[52:53]\n"
  "s_nop 0\n"
  "s_waitcnt vmcnt(8)\n"
  "s_barrier\n"
  "s_lshl_b32 s29, s38, 7\n"
  "s_lshl_b32 s30, s39, 2\n"
  "s_add_u32 s29, s29, s30\n"
  "s_add_u32 s29, s29, 0x2000\n"
  "s_cmp_lg_u32 s25, 0\n"
  "s_cbranch_scc1 PUBSKIP_7_%=\n"
  "v_mov_b32 v56, s29\n"
  "v_mov_b32 v57, s90\n"
  "s_mov_b64 exec, 1\n"
  "global_store_dword v56, v57, s[80:81] sc1\n"
  "s_mov_b64 exec, -1\n"
  "PUBSKIP_7_%=:\n"
  "s_mul_i32 s29, s99, 0x1c0000\n"
  "s_add_u32 s12, s80, 0x1600000\n"
  "s_addc_u32 s13, s81, 0\n"
  "s_add_u32 s12, s12, s29\n"
  "s_addc_u32 s13, s13, 0\n"
  "s_and_b32 s13, s13, 0xffff\n"
  "s_mov_b32 s14, 0x1c0000\n"
  "s_mov_b32 s15, 0x20000\n"
  "s_lshl_b32 s35, s40, 16\n"
  "s_lshl_b32 s29, s99, 7\n"
  "s_lshl_b32 s30, s40, 2\n"
  "s_add_u32 s29, s29, s30\n"
  "s_add_u32 s46, s29, 0x2400\n"
  "s_cmp_ge_u32 s42, 2\n"
  "s_cbranch_scc1 P3G_8_%=\n"
  "s_lshl_b32 s31, s42, 5\n"
  "s_lshl_b32 s29, s89, 21\n"
  "s_lshl_b32 s30, s31, 15\n"
  "s_add_u32 s29, s29, s30\n"
  "s_lshl_b32 s30, s31, 17\n"
  "s_lshl_b32 s34, s43, 8\n"
  "s_add_u32 s30, s30, s34\n"
  "s_add_u32 s4, s80, 0x600000\n"
  "s_addc_u32 s5, s81, 0\n"
  "s_add_u32 s4, s4, s29\n"
  "s_addc_u32 s5, s5, 0\n"
  "s_and_b32 s5, s5, 0xffff\n"
  "s_mov_b32 s6, 0x100000\n"
  "s_mov_b32 s7, 0x20000\n"
  "s_add_u32 s8, s68, s30\n"
  "s_addc_u32 s9, s69, 0\n"
  "s_and_b32 s9, s9, 0xffff\n"
  "s_mov_b32 s10, 0x3ff900\n"
  "s_mov_b32 s11, 0x20000\n"
  "s_branch P3GO_9_%=\n"
  "P3G_8_%=:\n"
  "s_and_b32 s31, s42, 1\n"
  "s_mul_i32 s31, s31, 28\n"
  "s_mul_i32 s29, s89, 0x1c0000\n"
  "s_lshl_b32 s30, s31, 15\n"
  "s_add_u32 s29, s29, s30\n"
  "s_lshl_b32 s30, s31, 17\n"
  "s_lshl_b32 s34, s43, 8\n"
  "s_add_u32 s30, s30, s34\n"
  "s_add_u32 s4, s80, 0x1600000\n"
  "s_addc_u32 s5, s81, 0\n"
  "s_add_u32 s4, s4, s29\n"
  "s_addc_u32 s5, s5, 0\n"
  "s_and_b32 s5, s5, 0xffff\n"
  "s_mov_b32 s6, 0xe0000\n"
  "s_mov_b32 s7, 0x20000\n"
  "s_add_u32 s8, s74, s30\n"
  "s_addc_u32 s9, s75, 0\n"
  "s_and_b32 s9, s9, 0xffff\n"
  "s_mov_b32 s10, 0x37f900\n"
  "s_mov_b32 s11, 0x20000\n"
  "P3GO_9_%=:\n"
  "s_mov_b32 s21, 0\n"
  "s_mul_i32 s22, s25, 0x8000\n"
  "s_mov_b32 s23, 0x20000\n"
  "v_lshrrev_b32 v18, 4, v1\n"
  "v_bfe_u32 v19, v1, 1, 3\n"
  "v_and_b32 v20, 1, v1\n"
  "v_lshlrev_b32 v20, 4, v20\n"
  "s_mov_b32 s36, 0x800\n"
  "v_add_u32 v21, 0, v18\n"
  "v_xor_b32 v21, v21, v19\n"
  "v_lshl_add_u32 v21, v21, 5, v20\n"
  "v_add_u32 v22, 0, v18\n"
  "v_mul_lo_u32 v22, v22, s36\n"
  "v_add_u32 v3, v22, v21\n"
  "v_add_u32 v21, 4, v18\n"
  "v_xor_b32 v21, v21, v19\n"
  "v_lshl_add_u32 v21, v21, 5, v20\n"
  "v_add_u32 v22, 4, v18\n"
  "v_mul_lo_u32 v22, v22, s36\n"
  "v_add_u32 v4, v22, v21\n"
  "v_subrev_u32 v4, 0x400, v4\n"
  "v_add_u32 v21, 0, v18\n"
  "v_xor_b32 v21, v21, v19\n"
  "v_lshl_add_u32 v21, v21, 5, v20\n"
  "v_add_u32 v22, 8, v18\n"
  "v_mul_lo_u32 v22, v22, s36\n"
  "v_add_u32 v5, v22, v21\n"
  "v_subrev_u32 v5, 0x800, v5\n"
  "v_add_u32 v21, 4, v18\n"
  "v_xor_b32 v21, v21, v19\n"
  "v_lshl_add_u32 v21, v21, 5, v20\n"
  "v_add_u32 v22, 12, v18\n"
  "v_mul_lo_u32 v22, v22, s36\n"
  "v_add_u32 v6, v22, v21\n"
  "v_subrev_u32 v6, 0xc00, v6\n"
  "s_add_u32 m0, s24, 0\n"
  "s_nop 0\n"
  "buffer_load_dwordx4 v3, s[8:11], s22 offen lds\n"
  "buffer_load_dwordx4 v4, s[8:11], s22 offen offset:1024 lds\n"
  "buffer_load_dwordx4 v5, s[8:11], s22 offen offset:2048 lds\n"
  "buffer_load_dwordx4 v6, s[8:11], s22 offen offset:3072 lds\n"
  "s_add_u32 s22, s22, s23\n"
  "s_add_u32 m0, s24, 0x4000\n"
  "s_nop 0\n"
  "buffer_load_dwordx4 v3, s[8:11], s22 offen lds\n"
  "buffer_load_dwordx4 v4, s[8:11], s22 offen offset:1024 lds\n"
  "buffer_load_dwordx4 v5, s[8:11], s22 offen offset:2048 lds\n"
  "buffer_load_dwordx4 v6, s[8:11], s22 offen offset:3072 lds\n"
  "s_add_u32 s22, s22, s23\n"
  "s_add_u32 m0, s24, 0x8000\n"
  "s_nop 0\n"
  "buffer_load_dwordx4 v3, s[8:11], s22 offen lds\n"
  "buffer_load_dwordx4 v4, s[8:11], s22 offen offset:1024 lds\n"
  "buffer_load_dwordx4 v5, s[8:11], s22 offen offset:2048 lds\n"
  "buffer_load_dwordx4 v6, s[8:11], s22 offen offset:3072 lds\n"
  "s_add_u32 s22, s22, s23\n"
  "s_add_u32 m0, s24, 0xc000\n"
  "s_nop 0\n"
  "buffer_load_dwordx4 v3, s[8:11], s22 offen lds\n"
  "buffer_load_dwordx4 v4, s[8:11], s22 offen offset:1024 lds\n"
  "buffer_load_dwordx4 v5, s[8:11], s22 offen offset:2048 lds\n"
  "buffer_load_dwordx4 v6, s[8:11], s22 offen offset:3072 lds\n"
  "s_add_u32 s22, s22, s23\n"
  "v_accvgpr_read_b32 v64, a192\n"
  "v_accvgpr_read_b32 v65, a193\n"
  "v_accvgpr_read_b32 v66, a194\n"
  "v_accvgpr_read_b32 v67, a195\n"
  "v_accvgpr_read_b32 v68, a196\n"
  "v_accvgpr_read_b32 v69, a197\n"
  "v_accvgpr_read_b32 v70, a198\n"
  "v_accvgpr_read_b32 v71, a199\n"
  "v_accvgpr_read_b32 v72, a200\n"
  "v_accvgpr_read_b32 v73, a201\n"
  "v_accvgpr_read_b32 v74, a202\n"
  "v_accvgpr_read_b32 v75, a203\n"
  "v_accvgpr_read_b32 v76, a204\n"
  "v_accvgpr_read_b32 v77, a205\n"
  "v_accvgpr_read_b32 v78, a206\n"
  "v_accvgpr_read_b32 v79, a207\n"
  "s_cmp_ge_u32 s42, 2\n"
  "s_cbranch_scc1 F1_G_%=\n"
  "s_cmp_eq_u32 s25, 0\n"
  "s_cselect_b32 s88, s46, -1\n"
  "v_lshlrev_b32 v80, 16, v64\n"
  "v_and_b32 v81, 0xffff0000, v64\n"
  "v_lshlrev_b32 v82, 16, v65\n"
  "v_and_b32 v83, 0xffff0000, v65\n"
  "v_lshlrev_b32 v84, 16, v66\n"
  "v_and_b32 v85, 0xffff0000, v66\n"
  "v_lshlrev_b32 v86, 16, v67\n"
  "v_and_b32 v87, 0xffff0000, v67\n"
  "v_lshlrev_b32 v88, 16, v68\n"
  "v_and_b32 v89, 0xffff0000, v68\n"
  "v_lshlrev_b32 v90, 16, v69\n"
  "v_and_b32 v91, 0xffff0000, v69\n"
  "v_lshlrev_b32 v92, 16, v70\n"
  "v_and_b32 v93, 0xffff0000, v70\n"
  "v_lshlrev_b32 v94, 16, v71\n"
  "v_and_b32 v95, 0xffff0000, v71\n"
  "v_lshlrev_b32 v96, 16, v72\n"
  "v_and_b32 v97, 0xffff0000, v72\n"
  "v_lshlrev_b32 v98, 16, v73\n"
  "v_and_b32 v99, 0xffff0000, v73\n"
  "v_lshlrev_b32 v100, 16, v74\n"
  "v_and_b32 v101, 0xffff0000, v74\n"
  "v_lshlrev_b32 v102, 16, v75\n"
  "v_and_b32 v103, 0xffff0000, v75\n"
  "v_lshlrev_b32 v104, 16, v76\n"
  "v_and_b32 v105, 0xffff0000, v76\n"
  "v_lshlrev_b32 v106, 16, v77\n"
  "v_and_b32 v107, 0xffff0000, v77\n"
  "v_lshlrev_b32 v108, 16, v78\n"
  "v_and_b32 v109, 0xffff0000, v78\n"
  "v_lshlrev_b32 v110, 16, v79\n"
  "v_and_b32 v111, 0xffff0000, v79\n"
  "s_mov_b32 s84, 0xbdd2d3e8\n"
  "s_mov_b32 s85, 0xbdd2d3e8\n"
  "s_mov_b32 s86, 0x3f800000\n"
  "s_mov_b32 s87, 0x3f800000\n"
  "v_mov_b32 v24, 0xc0135761\n"
  "v_mov_b32 v25, 0xc0135761\n"
  "v_pk_add_f32 v[128:129], v[128:129], v[80:81]\n"
  "v_pk_add_f32 v[130:131], v[130:131], v[82:83]\n"
  "v_pk_add_f32 v[144:145], v[144:145], v[84:85]\n"
  "v_pk_add_f32 v[146:147], v[146:147], v[86:87]\n"
  "v_pk_mul_f32 v[64:65], v[128:129], v[128:129]\n"
  "v_pk_mul_f32 v[66:67], v[130:131], v[130:131]\n"
  "v_pk_mul_f32 v[68:69], v[144:145], v[144:145]\n"
  "v_pk_mul_f32 v[70:71], v[146:147], v[146:147]\n"
  "v_pk_fma_f32 v[64:65], v[64:65], s[84:85], v[24:25]\n"
  "v_pk_fma_f32 v[66:67], v[66:67], s[84:85], v[24:25]\n"
  "v_pk_fma_f32 v[68:69], v[68:69], s[84:85], v[24:25]\n"
  "v_pk_fma_f32 v[70:71], v[70:71], s[84:85], v[24:25]\n"
  "v_pk_mul_f32 v[64:65], v[64:65], v[128:129]\n"
  "v_pk_mul_f32 v[66:67], v[66:67], v[130:131]\n"
  "v_pk_mul_f32 v[68:69], v[68:69], v[144:145]\n"
  "v_pk_mul_f32 v[70:71], v[70:71], v[146:147]\n"
  "v_exp_f32 v64, v64\n"
  "v_exp_f32 v65, v65\n"
  "v_exp_f32 v66, v66\n"
  "v_exp_f32 v67, v67\n"
  "v_exp_f32 v68, v68\n"
  "v_exp_f32 v69, v69\n"
  "v_exp_f32 v70, v70\n"
  "v_exp_f32 v71, v71\n"
  "v_pk_add_f32 v[64:65], v[64:65], s[86:87]\n"
  "v_pk_add_f32 v[66:67], v[66:67], s[86:87]\n"
  "v_pk_add_f32 v[68:69], v[68:69], s[86:87]\n"
  "v_pk_add_f32 v[70:71], v[70:71], s[86:87]\n"
  "v_rcp_f32 v64, v64\n"
  "v_rcp_f32 v65, v65\n"
  "v_rcp_f32 v66, v66\n"
  "v_rcp_f32 v67, v67\n"
  "v_rcp_f32 v68, v68\n"
  "v_rcp_f32 v69, v69\n"
  "v_rcp_f32 v70, v70\n"
  "v_rcp_f32 v71, v71\n"
  "v_pk_mul_f32 v[64:65], v[64:65], v[128:129]\n"
  "v_pk_mul_f32 v[66:67], v[66:67], v[130:131]\n"
  "v_pk_mul_f32 v[68:69], v[68:69], v[144:145]\n"
  "v_pk_mul_f32 v[70:71], v[70:71], v[146:147]\n"
  "v_cvt_pk_bf16_f32 v112, v64, v65\n"
  "v_cvt_pk_bf16_f32 v113, v66, v67\n"
  "v_cvt_pk_bf16_f32 v114, v68, v69\n"
  "v_cvt_pk_bf16_f32 v115, v70, v71\n"
  "buffer_store_dwordx4 v[112:115], v2, s[12:15], s35 offen sc1\n"
  "v_pk_add_f32 v[132:133], v[132:133], v[80:81]\n"
  "v_pk_add_f32 v[134:135], v[134:135], v[82:83]\n"
  "v_pk_add_f32 v[148:149], v[148:149], v[84:85]\n"
  "v_pk_add_f32 v[150:151], v[150:151], v[86:87]\n"
  "v_pk_mul_f32 v[64:65], v[132:133], v[132:133]\n"
  "v_pk_mul_f32 v[66:67], v[134:135], v[134:135]\n"
  "v_pk_mul_f32 v[68:69], v[148:149], v[148:149]\n"
  "v_pk_mul_f32 v[70:71], v[150:151], v[150:151]\n"
  "v_pk_fma_f32 v[64:65], v[64:65], s[84:85], v[24:25]\n"
  "v_pk_fma_f32 v[66:67], v[66:67], s[84:85], v[24:25]\n"
  "v_pk_fma_f32 v[68:69], v[68:69], s[84:85], v[24:25]\n"
  "v_pk_fma_f32 v[70:71], v[70:71], s[84:85], v[24:25]\n"
  "v_pk_mul_f32 v[64:65], v[64:65], v[132:133]\n"
  "v_pk_mul_f32 v[66:67], v[66:67], v[134:135]\n"
  "v_pk_mul_f32 v[68:69], v[68:69], v[148:149]\n"
  "v_pk_mul_f32 v[70:71], v[70:71], v[150:151]\n"
  "v_exp_f32 v64, v64\n"
  "v_exp_f32 v65, v65\n"
  "v_exp_f32 v66, v66\n"
  "v_exp_f32 v67, v67\n"
  "v_exp_f32 v68, v68\n"
  "v_exp_f32 v69, v69\n"
  "v_exp_f32 v70, v70\n"
  "v_exp_f32 v71, v71\n"
  "v_pk_add_f32 v[64:65], v[64:65], s[86:87]\n"
  "v_pk_add_f32 v[66:67], v[66:67], s[86:87]\n"
  "v_pk_add_f32 v[68:69], v[68:69], s[86:87]\n"
  "v_pk_add_f32 v[70:71], v[70:71], s[86:87]\n"
  "v_rcp_f32 v64, v64\n"
  "v_rcp_f32 v65, v65\n"
  "v_rcp_f32 v66, v66\n"
  "v_rcp_f32 v67, v67\n"
  "v_rcp_f32 v68, v68\n"
  "v_rcp_f32 v69, v69\n"
  "v_rcp_f32 v70, v70\n"
  "v_rcp_f32 v71, v71\n"
  "v_pk_mul_f32 v[64:65], v[64:65], v[132:133]\n"
  "v_pk_mul_f32 v[66:67], v[66:67], v[134:135]\n"
  "v_pk_mul_f32 v[68:69], v[68:69], v[148:149]\n"
  "v_pk_mul_f32 v[70:71], v[70:71], v[150:151]\n"
  "v_cvt_pk_bf16_f32 v116, v64, v65\n"
  "v_cvt_pk_bf16_f32 v117, v66, v67\n"
  "v_cvt_pk_bf16_f32 v118, v68, v69\n"
  "v_cvt_pk_bf16_f32 v119, v70, v71\n"
  "buffer_store_dwordx4 v[116:119], v2, s[12:15], s35 offen offset:256 sc1\n"
  "v_pk_add_f32 v[136:137], v[136:137], v[80:81]\n"
  "v_pk_add_f32 v[138:139], v[138:139], v[82:83]\n"
  "v_pk_add_f32 v[152:153], v[152:153], v[84:85]\n"
  "v_pk_add_f32 v[154:155], v[154:155], v[86:87]\n"
  "v_pk_mul_f32 v[64:65], v[136:137], v[136:137]\n"
  "v_pk_mul_f32 v[66:67], v[138:139], v[138:139]\n"
  "v_pk_mul_f32 v[68:69], v[152:153], v[152:153]\n"
  "v_pk_mul_f32 v[70:71], v[154:155], v[154:155]\n"
  "v_pk_fma_f32 v[64:65], v[64:65], s[84:85], v[24:25]\n"
  "v_pk_fma_f32 v[66:67], v[66:67], s[84:85], v[24:25]\n"
  "v_pk_fma_f32 v[68:69], v[68:69], s[84:85], v[24:25]\n"
  "v_pk_fma_f32 v[70:71], v[70:71], s[84:85], v[24:25]\n"
  "v_pk_mul_f32 v[64:65], v[64:65], v[136:137]\n"
  "v_pk_mul_f32 v[66:67], v[66:67], v[138:139]\n"
  "v_pk_mul_f32 v[68:69], v[68:69], v[152:153]\n"
  "v_pk_mul_f32 v[70:71], v[70:71], v[154:155]\n"
  "v_exp_f32 v64, v64\n"
  "v_exp_f32 v65, v65\n"
  "v_exp_f32 v66, v66\n"
  "v_exp_f32 v67, v67\n"
  "v_exp_f32 v68, v68\n"
  "v_exp_f32 v69, v69\n"
  "v_exp_f32 v70, v70\n"
  "v_exp_f32 v71, v71\n"
  "v_pk_add_f32 v[64:65], v[64:65], s[86:87]\n"
  "v_pk_add_f32 v[66:67], v[66:67], s[86:87]\n"
  "v_pk_add_f32 v[68:69], v[68:69], s[86:87]\n"
  "v_pk_add_f32 v[70:71], v[70:71], s[86:87]\n"
  "v_rcp_f32 v64, v64\n"
  "v_rcp_f32 v65, v65\n"
  "v_rcp_f32 v66, v66\n"
  "v_rcp_f32 v67, v67\n"
  "v_rcp_f32 v68, v68\n"
  "v_rcp_f32 v69, v69\n"
  "v_rcp_f32 v70, v70\n"
  "v_rcp_f32 v71, v71\n"
  "v_pk_mul_f32 v[64:65], v[64:65], v[136:137]\n"
  "v_pk_mul_f32 v[66:67], v[66:67], v[138:139]\n"
  "v_pk_mul_f32 v[68:69], v[68:69], v[152:153]\n"
  "v_pk_mul_f32 v[70:71], v[70:71], v[154:155]\n"
  "v_cvt_pk_bf16_f32 v120, v64, v65\n"
  "v_cvt_pk_bf16_f32 v121, v66, v67\n"
  "v_cvt_pk_bf16_f32 v122, v68, v69\n"
  "v_cvt_pk_bf16_f32 v123, v70, v71\n"
  "buffer_store_dwordx4 v[120:123], v2, s[12:15], s35 offen offset:512 sc1\n"
  "v_pk_add_f32 v[140:141], v[140:141], v[80:81]\n"
  "v_pk_add_f32 v[142:143], v[142:143], v[82:83]\n"
  "v_pk_add_f32 v[156:157], v[156:157], v[84:85]\n"
  "v_pk_add_f32 v[158:159], v[158:159], v[86:87]\n"
  "v_pk_mul_f32 v[64:65], v[140:141], v[140:141]\n"
  "v_pk_mul_f32 v[66:67], v[142:143], v[142:143]\n"
  "v_pk_mul_f32 v[68:69], v[156:157], v[156:157]\n"
  "v_pk_mul_f32 v[70:71], v[158:159], v[158:159]\n"
  "v_pk_fma_f32 v[64:65], v[64:65], s[84:85], v[24:25]\n"
  "v_pk_fma_f32 v[66:67], v[66:67], s[84:85], v[24:25]\n"
  "v_pk_fma_f32 v[68:69], v[68:69], s[84:85], v[24:25]\n"
  "v_pk_fma_f32 v[70:71], v[70:71], s[84:85], v[24:25]\n"
  "v_pk_mul_f32 v[64:65], v[64:65], v[140:141]\n"
  "v_pk_mul_f32 v[66:67], v[66:67], v[142:143]\n"
  "v_pk_mul_f32 v[68:69], v[68:69], v[156:157]\n"
  "v_pk_mul_f32 v[70:71], v[70:71], v[158:159]\n"
  "v_exp_f32 v64, v64\n"
  "v_exp_f32 v65, v65\n"
  "v_exp_f32 v66, v66\n"
  "v_exp_f32 v67, v67\n"
  "v_exp_f32 v68, v68\n"
  "v_exp_f32 v69, v69\n"
  "v_exp_f32 v70, v70\n"
  "v_exp_f32 v71, v71\n"
  "v_pk_add_f32 v[64:65], v[64:65], s[86:87]\n"
  "v_pk_add_f32 v[66:67], v[66:67], s[86:87]\n"
  "v_pk_add_f32 v[68:69], v[68:69], s[86:87]\n"
  "v_pk_add_f32 v[70:71], v[70:71], s[86:87]\n"
  "v_rcp_f32 v64, v64\n"
  "v_rcp_f32 v65, v65\n"
  "v_rcp_f32 v66, v66\n"
  "v_rcp_f32 v67, v67\n"
  "v_rcp_f32 v68, v68\n"
  "v_rcp_f32 v69, v69\n"
  "v_rcp_f32 v70, v70\n"
  "v_rcp_f32 v71, v71\n"
  "v_pk_mul_f32 v[64:65], v[64:65], v[140:141]\n"
  "v_pk_mul_f32 v[66:67], v[66:67], v[142:143]\n"
  "v_pk_mul_f32 v[68:69], v[68:69], v[156:157]\n"
  "v_pk_mul_f32 v[70:71], v[70:71], v[158:159]\n"
  "v_cvt_pk_bf16_f32 v124, v64, v65\n"
  "v_cvt_pk_bf16_f32 v125, v66, v67\n"
  "v_cvt_pk_bf16_f32 v126, v68, v69\n"
  "v_cvt_pk_bf16_f32 v127, v70, v71\n"
  "buffer_store_dwordx4 v[124:127], v2, s[12:15], s35 offen offset:768 sc1\n"
  "s_add_u32 s35, s35, 0x4000\n"
  "s_lshl_b32 s29, s89, 7\n"
  "s_add_u32 s29, s29, 0x2000\n"
  "v_min_u32 v54, 31, v1\n"
  "v_lshlrev_b32 v54, 2, v54\n"
  "v_add_u32 v54, s29, v54\n"
  "global_load_dword v59, v54, s[80:81] sc1\n"
  "v_pk_add_f32 v[160:161], v[160:161], v[88:89]\n"
  "v_pk_add_f32 v[162:163], v[162:163], v[90:91]\n"
  "v_pk_add_f32 v[176:177], v[176:177], v[92:93]\n"
  "v_pk_add_f32 v[178:179], v[178:179], v[94:95]\n"
  "v_pk_mul_f32 v[64:65], v[160:161], v[160:161]\n"
  "v_pk_mul_f32 v[66:67], v[162:163], v[162:163]\n"
  "v_pk_mul_f32 v[68:69], v[176:177], v[176:177]\n"
  "v_pk_mul_f32 v[70:71], v[178:179], v[178:179]\n"
  "v_pk_fma_f32 v[64:65], v[64:65], s[84:85], v[24:25]\n"
  "v_pk_fma_f32 v[66:67], v[66:67], s[84:85], v[24:25]\n"
  "v_pk_fma_f32 v[68:69], v[68:69], s[84:85], v[24:25]\n"
  "v_pk_fma_f32 v[70:71], v[70:71], s[84:85], v[24:25]\n"
  "v_pk_mul_f32 v[64:65], v[64:65], v[160:161]\n"
  "v_pk_mul_f32 v[66:67], v[66:67], v[162:163]\n"
  "v_pk_mul_f32 v[68:69], v[68:69], v[176:177]\n"
  "v_pk_mul_f32 v[70:71], v[70:71], v[178:179]\n"
  "v_exp_f32 v64, v64\n"
  "v_exp_f32 v65, v65\n"
  "v_exp_f32 v66, v66\n"
  "v_exp_f32 v67, v67\n"
  "v_exp_f32 v68, v68\n"
  "v_exp_f32 v69, v69\n"
  "v_exp_f32 v70, v70\n"
  "v_exp_f32 v71, v71\n"
  "v_pk_add_f32 v[64:65], v[64:65], s[86:87]\n"
  "v_pk_add_f32 v[66:67], v[66:67], s[86:87]\n"
  "v_pk_add_f32 v[68:69], v[68:69], s[86:87]\n"
  "v_pk_add_f32 v[70:71], v[70:71], s[86:87]\n"
  "v_rcp_f32 v64, v64\n"
  "v_rcp_f32 v65, v65\n"
  "v_rcp_f32 v66, v66\n"
  "v_rcp_f32 v67, v67\n"
  "v_rcp_f32 v68, v68\n"
  "v_rcp_f32 v69, v69\n"
  "v_rcp_f32 v70, v70\n"
  "v_rcp_f32 v71, v71\n"
  "v_pk_mul_f32 v[64:65], v[64:65], v[160:161]\n"
  "v_pk_mul_f32 v[66:67], v[66:67], v[162:163]\n"
  "v_pk_mul_f32 v[68:69], v[68:69], v[176:177]\n"
  "v_pk_mul_f32 v[70:71], v[70:71], v[178:179]\n"
  "v_cvt_pk_bf16_f32 v112, v64, v65\n"
  "v_cvt_pk_bf16_f32 v113, v66, v67\n"
  "v_cvt_pk_bf16_f32 v114, v68, v69\n"
  "v_cvt_pk_bf16_f32 v115, v70, v71\n"
  "buffer_store_dwordx4 v[112:115], v2, s[12:15], s35 offen sc1\n"
  "v_pk_add_f32 v[164:165], v[164:165], v[88:89]\n"
  "v_pk_add_f32 v[166:167], v[166:167], v[90:91]\n"
  "v_pk_add_f32 v[180:181], v[180:181], v[92:93]\n"
  "v_pk_add_f32 v[182:183], v[182:183], v[94:95]\n"
  "v_pk_mul_f32 v[64:65], v[164:165], v[164:165]\n"
  "v_pk_mul_f32 v[66:67], v[166:167], v[166:167]\n"
  "v_pk_mul_f32 v[68:69], v[180:181], v[180:181]\n"
  "v_pk_mul_f32 v[70:71], v[182:183], v[182:183]\n"
  "v_pk_fma_f32 v[64:65], v[64:65], s[84:85], v[24:25]\n"
  "v_pk_fma_f32 v[66:67], v[66:67], s[84:85], v[24:25]\n"
  "v_pk_fma_f32 v[68:69], v[68:69], s[84:85], v[24:25]\n"
  "v_pk_fma_f32 v[70:71], v[70:71], s[84:85], v[24:25]\n"
  "v_pk_mul_f32 v[64:65], v[64:65], v[164:165]\n"
  "v_pk_mul_f32 v[66:67], v[66:67], v[166:167]\n"
  "v_pk_mul_f32 v[68:69], v[68:69], v[180:181]\n"
  "v_pk_mul_f32 v[70:71], v[70:71], v[182:183]\n"
  "v_exp_f32 v64, v64\n"
  "v_exp_f32 v65, v65\n"
  "v_exp_f32 v66, v66\n"
  "v_exp_f32 v67, v67\n"
  "v_exp_f32 v68, v68\n"
  "v_exp_f32 v69, v69\n"
  "v_exp_f32 v70, v70\n"
  "v_exp_f32 v71, v71\n"
  "v_pk_add_f32 v[64:65], v[64:65], s[86:87]\n"
  "v_pk_add_f32 v[66:67], v[66:67], s[86:87]\n"
  "v_pk_add_f32 v[68:69], v[68:69], s[86:87]\n"
  "v_pk_add_f32 v[70:71], v[70:71], s[86:87]\n"
  "v_rcp_f32 v64, v64\n"
  "v_rcp_f32 v65, v65\n"
  "v_rcp_f32 v66, v66\n"
  "v_rcp_f32 v67, v67\n"
  "v_rcp_f32 v68, v68\n"
  "v_rcp_f32 v69, v69\n"
  "v_rcp_f32 v70, v70\n"
  "v_rcp_f32 v71, v71\n"
  "v_pk_mul_f32 v[64:65], v[64:65], v[164:165]\n"
  "v_pk_mul_f32 v[66:67], v[66:67], v[166:167]\n"
  "v_pk_mul_f32 v[68:69], v[68:69], v[180:181]\n"
  "v_pk_mul_f32 v[70:71], v[70:71], v[182:183]\n"
  "v_cvt_pk_bf16_f32 v116, v64, v65\n"
  "v_cvt_pk_bf16_f32 v117, v66, v67\n"
  "v_cvt_pk_bf16_f32 v118, v68, v69\n"
  "v_cvt_pk_bf16_f32 v119, v70, v71\n"
  "buffer_store_dwordx4 v[116:119], v2, s[12:15], s35 offen offset:256 sc1\n"
  "v_pk_add_f32 v[168:169], v[168:169], v[88:89]\n"
  "v_pk_add_f32 v[170:171], v[170:171], v[90:91]\n"
  "v_pk_add_f32 v[184:185], v[184:185], v[92:93]\n"
  "v_pk_add_f32 v[186:187], v[186:187], v[94:95]\n"
  "v_pk_mul_f32 v[64:65], v[168:169], v[168:169]\n"
  "v_pk_mul_f32 v[66:67], v[170:171], v[170:171]\n"
  "v_pk_mul_f32 v[68:69], v[184:185], v[184:185]\n"
  "v_pk_mul_f32 v[70:71], v[186:187], v[186:187]\n"
  "v_pk_fma_f32 v[64:65], v[64:65], s[84:85], v[24:25]\n"
  "v_pk_fma_f32 v[66:67], v[66:67], s[84:85], v[24:25]\n"
  "v_pk_fma_f32 v[68:69], v[68:69], s[84:85], v[24:25]\n"
  "v_pk_fma_f32 v[70:71], v[70:71], s[84:85], v[24:25]\n"
  "v_pk_mul_f32 v[64:65], v[64:65], v[168:169]\n"
  "v_pk_mul_f32 v[66:67], v[66:67], v[170:171]\n"
  "v_pk_mul_f32 v[68:69], v[68:69], v[184:185]\n"
  "v_pk_mul_f32 v[70:71], v[70:71], v[186:187]\n"
  "v_exp_f32 v64, v64\n"
  "v_exp_f32 v65, v65\n"
  "v_exp_f32 v66, v66\n"
  "v_exp_f32 v67, v67\n"
  "v_exp_f32 v68, v68\n"
  "v_exp_f32 v69, v69\n"
  "v_exp_f32 v70, v70\n"
  "v_exp_f32 v71, v71\n"
  "v_pk_add_f32 v[64:65], v[64:65], s[86:87]\n"
  "v_pk_add_f32 v[66:67], v[66:67], s[86:87]\n"
  "v_pk_add_f32 v[68:69], v[68:69], s[86:87]\n"
  "v_pk_add_f32 v[70:71], v[70:71], s[86:87]\n"
  "v_rcp_f32 v64, v64\n"
  "v_rcp_f32 v65, v65\n"
  "v_rcp_f32 v66, v66\n"
  "v_rcp_f32 v67, v67\n"
  "v_rcp_f32 v68, v68\n"
  "v_rcp_f32 v69, v69\n"
  "v_rcp_f32 v70, v70\n"
  "v_rcp_f32 v71, v71\n"
  "v_pk_mul_f32 v[64:65], v[64:65], v[168:169]\n"
  "v_pk_mul_f32 v[66:67], v[66:67], v[170:171]\n"
  "v_pk_mul_f32 v[68:69], v[68:69], v[184:185]\n"
  "v_pk_mul_f32 v[70:71], v[70:71], v[186:187]\n"
  "v_cvt_pk_bf16_f32 v120, v64, v65\n"
  "v_cvt_pk_bf16_f32 v121, v66, v67\n"
  "v_cvt_pk_bf16_f32 v122, v68, v69\n"
  "v_cvt_pk_bf16_f32 v123, v70, v71\n"
  "buffer_store_dwordx4 v[120:123], v2, s[12:15], s35 offen offset:512 sc1\n"
  "v_pk_add_f32 v[172:173], v[172:173], v[88:89]\n"
  "v_pk_add_f32 v[174:175], v[174:175], v[90:91]\n"
  "v_pk_add_f32 v[188:189], v[188:189], v[92:93]\n"
  "v_pk_add_f32 v[190:191], v[190:191], v[94:95]\n"
  "v_pk_mul_f32 v[64:65], v[172:173], v[172:173]\n"
  "v_pk_mul_f32 v[66:67], v[174:175], v[174:175]\n"
  "v_pk_mul_f32 v[68:69], v[188:189], v[188:189]\n"
  "v_pk_mul_f32 v[70:71], v[190:191], v[190:191]\n"
  "v_pk_fma_f32 v[64:65], v[64:65], s[84:85], v[24:25]\n"
  "v_pk_fma_f32 v[66:67], v[66:67], s[84:85], v[24:25]\n"
  "v_pk_fma_f32 v[68:69], v[68:69], s[84:85], v[24:25]\n"
  "v_pk_fma_f32 v[70:71], v[70:71], s[84:85], v[24:25]\n"
  "v_pk_mul_f32 v[64:65], v[64:65], v[172:173]\n"
  "v_pk_mul_f32 v[66:67], v[66:67], v[174:175]\n"
  "v_pk_mul_f32 v[68:69], v[68:69], v[188:189]\n"
  "v_pk_mul_f32 v[70:71], v[70:71], v[190:191]\n"
  "v_exp_f32 v64, v64\n"
  "v_exp_f32 v65, v65\n"
  "v_exp_f32 v66, v66\n"
  "v_exp_f32 v67, v67\n"
  "v_exp_f32 v68, v68\n"
  "v_exp_f32 v69, v69\n"
  "v_exp_f32 v70, v70\n"
  "v_exp_f32 v71, v71\n"
  "v_pk_add_f32 v[64:65], v[64:65], s[86:87]\n"
  "v_pk_add_f32 v[66:67], v[66:67], s[86:87]\n"
  "v_pk_add_f32 v[68:69], v[68:69], s[86:87]\n"
  "v_pk_add_f32 v[70:71], v[70:71], s[86:87]\n"
  "v_rcp_f32 v64, v64\n"
  "v_rcp_f32 v65, v65\n"
  "v_rcp_f32 v66, v66\n"
  "v_rcp_f32 v67, v67\n"
  "v_rcp_f32 v68, v68\n"
  "v_rcp_f32 v69, v69\n"
  "v_rcp_f32 v70, v70\n"
  "v_rcp_f32 v71, v71\n"
  "v_pk_mul_f32 v[64:65], v[64:65], v[172:173]\n"
  "v_pk_mul_f32 v[66:67], v[66:67], v[174:175]\n"
  "v_pk_mul_f32 v[68:69], v[68:69], v[188:189]\n"
  "v_pk_mul_f32 v[70:71], v[70:71], v[190:191]\n"
  "v_cvt_pk_bf16_f32 v124, v64, v65\n"
  "v_cvt_pk_bf16_f32 v125, v66, v67\n"
  "v_cvt_pk_bf16_f32 v126, v68, v69\n"
  "v_cvt_pk_bf16_f32 v127, v70, v71\n"
  "buffer_store_dwordx4 v[124:127], v2, s[12:15], s35 offen offset:768 sc1\n"
  "s_add_u32 s35, s35, 0x4000\n"
  "s_waitcnt vmcnt(4)\n"
  "v_cmp_ne_u32 vcc, s90, v59\n"
  "s_nop 1\n"
  "s_cmp_eq_u64 vcc, 0\n"
  "s_cbranch_scc1 FLAGOK_10_%=\n"
  "s_lshl_b32 s29, s89, 7\n"
  "s_add_u32 s29, s29, 0x2000\n"
  "v_min_u32 v56, 31, v1\n"
  "v_lshlrev_b32 v56, 2, v56\n"
  "v_add_u32 v56, s29, v56\n"
  "s_mov_b32 s63, 0x30d40\n"
  "POLL_11_%=:\n"
  "global_load_dword v57, v56, s[80:81] sc1\n"
  "s_waitcnt vmcnt(0)\n"
  "v_cmp_ne_u32 vcc, s90, v57\n"
  "s_nop 1\n"
  "s_cmp_eq_u64 vcc, 0\n"
  "s_cbranch_scc1 POLLOK_12_%=\n"
  "s_sleep 1\n"
  "s_sub_u32 s63, s63, 1\n"
  "s_cmp_lg_u32 s63, 0\n"
  "s_cbranch_scc1 POLL_11_%=\n"
  "POLLOK_12_%=:\n"
  "FLAGOK_10_%=:\n"
  "buffer_load_dwordx4 a[64:67], v2, s[4:7], s21 offen sc1\n"
  "buffer_load_dwordx4 a[68:71], v2, s[4:7], s21 offen offset:256 sc1\n"
  "buffer_load_dwordx4 a[72:75], v2, s[4:7], s21 offen offset:512 sc1\n"
  "buffer_load_dwordx4 a[76:79], v2, s[4:7], s21 offen offset:768 sc1\n"
  "s_add_u32 s21, s21, 0x4000\n"
  "buffer_load_dwordx4 a[80:83], v2, s[4:7], s21 offen sc1\n"
  "buffer_load_dwordx4 a[84:87], v2, s[4:7], s21 offen offset:256 sc1\n"
  "buffer_load_dwordx4 a[88:91], v2, s[4:7], s21 offen offset:512 sc1\n"
  "buffer_load_dwordx4 a[92:95], v2, s[4:7], s21 offen offset:768 sc1\n"
  "s_add_u32 s21, s21, 0x4000\n"
  "buffer_load_dwordx4 a[96:99], v2, s[4:7], s21 offen sc1\n"
  "buffer_load_dwordx4 a[100:103], v2, s[4:7], s21 offen offset:256 sc1\n"
  "buffer_load_dwordx4 a[104:107], v2, s[4:7], s21 offen offset:512 sc1\n"
  "buffer_load_dwordx4 a[108:111], v2, s[4:7], s21 offen offset:768 sc1\n"
  "s_add_u32 s21, s21, 0x4000\n"
  "buffer_load_dwordx4 a[112:115], v2, s[4:7], s21 offen sc1\n"
  "buffer_load_dwordx4 a[116:119], v2, s[4:7], s21 offen offset:256 sc1\n"
  "buffer_load_dwordx4 a[120:123], v2, s[4:7], s21 offen offset:512 sc1\n"
  "buffer_load_dwordx4 a[124:127], v2, s[4:7], s21 offen offset:768 sc1\n"
  "s_add_u32 s21, s21, 0x4000\n"
  "buffer_load_dwordx4 a[128:131], v2, s[4:7], s21 offen sc1\n"
  "buffer_load_dwordx4 a[132:135], v2, s[4:7], s21 offen offset:256 sc1\n"
  "buffer_load_dwordx4 a[136:139], v2, s[4:7], s21 offen offset:512 sc1\n"
  "buffer_load_dwordx4 a[140:143], v2, s[4:7], s21 offen offset:768 sc1\n"
  "s_add_u32 s21, s21, 0x4000\n"
  "buffer_load_dwordx4 a[144:147], v2, s[4:7], s21 offen sc1\n"
  "buffer_load_dwordx4 a[148:151], v2, s[4:7], s21 offen offset:256 sc1\n"
  "buffer_load_dwordx4 a[152:155], v2, s[4:7], s21 offen offset:512 sc1\n"
  "buffer_load_dwordx4 a[156:159], v2, s[4:7], s21 offen offset:768 sc1\n"
  "s_add_u32 s21, s21, 0x4000\n"
  "buffer_load_dwordx4 a[160:163], v2, s[4:7], s21 offen sc1\n"
  "buffer_load_dwordx4 a[164:167], v2, s[4:7], s21 offen offset:256 sc1\n"
  "buffer_load_dwordx4 a[168:171], v2, s[4:7], s21 offen offset:512 sc1\n"
  "buffer_load_dwordx4 a[172:175], v2, s[4:7], s21 offen offset:768 sc1\n"
  "s_add_u32 s21, s21, 0x4000\n"
  "v_pk_add_f32 v[192:193], v[192:193], v[96:97]\n"
  "v_pk_add_f32 v[194:195], v[194:195], v[98:99]\n"
  "v_pk_add_f32 v[208:209], v[208:209], v[100:101]\n"
  "v_pk_add_f32 v[210:211], v[210:211], v[102:103]\n"
  "v_pk_mul_f32 v[64:65], v[192:193], v[192:193]\n"
  "v_pk_mul_f32 v[66:67], v[194:195], v[194:195]\n"
  "v_pk_mul_f32 v[68:69], v[208:209], v[208:209]\n"
  "v_pk_mul_f32 v[70:71], v[210:211], v[210:211]\n"
  "v_pk_fma_f32 v[64:65], v[64:65], s[84:85], v[24:25]\n"
  "v_pk_fma_f32 v[66:67], v[66:67], s[84:85], v[24:25]\n"
  "v_pk_fma_f32 v[68:69], v[68:69], s[84:85], v[24:25]\n"
  "v_pk_fma_f32 v[70:71], v[70:71], s[84:85], v[24:25]\n"
  "v_pk_mul_f32 v[64:65], v[64:65], v[192:193]\n"
  "v_pk_mul_f32 v[66:67], v[66:67], v[194:195]\n"
  "v_pk_mul_f32 v[68:69], v[68:69], v[208:209]\n"
  "v_pk_mul_f32 v[70:71], v[70:71], v[210:211]\n"
  "v_exp_f32 v64, v64\n"
  "v_exp_f32 v65, v65\n"
  "v_exp_f32 v66, v66\n"
  "v_exp_f32 v67, v67\n"
  "v_exp_f32 v68, v68\n"
  "v_exp_f32 v69, v69\n"
  "v_exp_f32 v70, v70\n"
  "v_exp_f32 v71, v71\n"
  "v_pk_add_f32 v[64:65], v[64:65], s[86:87]\n"
  "v_pk_add_f32 v[66:67], v[66:67], s[86:87]\n"
  "v_pk_add_f32 v[68:69], v[68:69], s[86:87]\n"
  "v_pk_add_f32 v[70:71], v[70:71], s[86:87]\n"
  "v_rcp_f32 v64, v64\n"
  "v_rcp_f32 v65, v65\n"
  "v_rcp_f32 v66, v66\n"
  "v_rcp_f32 v67, v67\n"
  "v_rcp_f32 v68, v68\n"
  "v_rcp_f32 v69, v69\n"
  "v_rcp_f32 v70, v70\n"
  "v_rcp_f32 v71, v71\n"
  "v_pk_mul_f32 v[64:65], v[64:65], v[192:193]\n"
  "v_pk_mul_f32 v[66:67], v[66:67], v[194:195]\n"
  "v_pk_mul_f32 v[68:69], v[68:69], v[208:209]\n"
  "v_pk_mul_f32 v[70:71], v[70:71], v[210:211]\n"
  "v_cvt_pk_bf16_f32 v112, v64, v65\n"
  "v_cvt_pk_bf16_f32 v113, v66, v67\n"
  "v_cvt_pk_bf16_f32 v114, v68, v69\n"
  "v_cvt_pk_bf16_f32 v115, v70, v71\n"
  "buffer_store_dwordx4 v[112:115], v2, s[12:15], s35 offen sc1\n"
  "v_pk_add_f32 v[196:197], v[196:197], v[96:97]\n"
  "v_pk_add_f32 v[198:199], v[198:199], v[98:99]\n"
  "v_pk_add_f32 v[212:213], v[212:213], v[100:101]\n"
  "v_pk_add_f32 v[214:215], v[214:215], v[102:103]\n"
  "v_pk_mul_f32 v[64:65], v[196:197], v[196:197]\n"
  "v_pk_mul_f32 v[66:67], v[198:199], v[198:199]\n"
  "v_pk_mul_f32 v[68:69], v[212:213], v[212:213]\n"
  "v_pk_mul_f32 v[70:71], v[214:215], v[214:215]\n"
  "v_pk_fma_f32 v[64:65], v[64:65], s[84:85], v[24:25]\n"
  "v_pk_fma_f32 v[66:67], v[66:67], s[84:85], v[24:25]\n"
  "v_pk_fma_f32 v[68:69], v[68:69], s[84:85], v[24:25]\n"
  "v_pk_fma_f32 v[70:71], v[70:71], s[84:85], v[24:25]\n"
  "v_pk_mul_f32 v[64:65], v[64:65], v[196:197]\n"
  "v_pk_mul_f32 v[66:67], v[66:67], v[198:199]\n"
  "v_pk_mul_f32 v[68:69], v[68:69], v[212:213]\n"
  "v_pk_mul_f32 v[70:71], v[70:71], v[214:215]\n"
  "v_exp_f32 v64, v64\n"
  "v_exp_f32 v65, v65\n"
  "v_exp_f32 v66, v66\n"
  "v_exp_f32 v67, v67\n"
  "v_exp_f32 v68, v68\n"
  "v_exp_f32 v69, v69\n"
  "v_exp_f32 v70, v70\n"
  "v_exp_f32 v71, v71\n"
  "v_pk_add_f32 v[64:65], v[64:65], s[86:87]\n"
  "v_pk_add_f32 v[66:67], v[66:67], s[86:87]\n"
  "v_pk_add_f32 v[68:69], v[68:69], s[86:87]\n"
  "v_pk_add_f32 v[70:71], v[70:71], s[86:87]\n"
  "v_rcp_f32 v64, v64\n"
  "v_rcp_f32 v65, v65\n"
  "v_rcp_f32 v66, v66\n"
  "v_rcp_f32 v67, v67\n"
  "v_rcp_f32 v68, v68\n"
  "v_rcp_f32 v69, v69\n"
  "v_rcp_f32 v70, v70\n"
  "v_rcp_f32 v71, v71\n"
  "v_pk_mul_f32 v[64:65], v[64:65], v[196:197]\n"
  "v_pk_mul_f32 v[66:67], v[66:67], v[198:199]\n"
  "v_pk_mul_f32 v[68:69], v[68:69], v[212:213]\n"
  "v_pk_mul_f32 v[70:71], v[70:71], v[214:215]\n"
  "v_cvt_pk_bf16_f32 v116, v64, v65\n"
  "v_cvt_pk_bf16_f32 v117, v66, v67\n"
  "v_cvt_pk_bf16_f32 v118, v68, v69\n"
  "v_cvt_pk_bf16_f32 v119, v70, v71\n"
  "buffer_store_dwordx4 v[116:119], v2, s[12:15], s35 offen offset:256 sc1\n"
  "v_pk_add_f32 v[200:201], v[200:201], v[96:97]\n"
  "v_pk_add_f32 v[202:203], v[202:203], v[98:99]\n"
  "v_pk_add_f32 v[216:217], v[216:217], v[100:101]\n"
  "v_pk_add_f32 v[218:219], v[218:219], v[102:103]\n"
  "v_pk_mul_f32 v[64:65], v[200:201], v[200:201]\n"
  "v_pk_mul_f32 v[66:67], v[202:203], v[202:203]\n"
  "v_pk_mul_f32 v[68:69], v[216:217], v[216:217]\n"
  "v_pk_mul_f32 v[70:71], v[218:219], v[218:219]\n"
  "v_pk_fma_f32 v[64:65], v[64:65], s[84:85], v[24:25]\n"
  "v_pk_fma_f32 v[66:67], v[66:67], s[84:85], v[24:25]\n"
  "v_pk_fma_f32 v[68:69], v[68:69], s[84:85], v[24:25]\n"
  "v_pk_fma_f32 v[70:71], v[70:71], s[84:85], v[24:25]\n"
  "v_pk_mul_f32 v[64:65], v[64:65], v[200:201]\n"
  "v_pk_mul_f32 v[66:67], v[66:67], v[202:203]\n"
  "v_pk_mul_f32 v[68:69], v[68:69], v[216:217]\n"
  "v_pk_mul_f32 v[70:71], v[70:71], v[218:219]\n"
  "v_exp_f32 v64, v64\n"
  "v_exp_f32 v65, v65\n"
  "v_exp_f32 v66, v66\n"
  "v_exp_f32 v67, v67\n"
  "v_exp_f32 v68, v68\n"
  "v_exp_f32 v69, v69\n"
  "v_exp_f32 v70, v70\n"
  "v_exp_f32 v71, v71\n"
  "v_pk_add_f32 v[64:65], v[64:65], s[86:87]\n"
  "v_pk_add_f32 v[66:67], v[66:67], s[86:87]\n"
  "v_pk_add_f32 v[68:69], v[68:69], s[86:87]\n"
  "v_pk_add_f32 v[70:71], v[70:71], s[86:87]\n"
  "v_rcp_f32 v64, v64\n"
  "v_rcp_f32 v65, v65\n"
  "v_rcp_f32 v66, v66\n"
  "v_rcp_f32 v67, v67\n"
  "v_rcp_f32 v68, v68\n"
  "v_rcp_f32 v69, v69\n"
  "v_rcp_f32 v70, v70\n"
  "v_rcp_f32 v71, v71\n"
  "v_pk_mul_f32 v[64:65], v[64:65], v[200:201]\n"
  "v_pk_mul_f32 v[66:67], v[66:67], v[202:203]\n"
  "v_pk_mul_f32 v[68:69], v[68:69], v[216:217]\n"
  "v_pk_mul_f32 v[70:71], v[70:71], v[218:219]\n"
  "v_cvt_pk_bf16_f32 v120, v64, v65\n"
  "v_cvt_pk_bf16_f32 v121, v66, v67\n"
  "v_cvt_pk_bf16_f32 v122, v68, v69\n"
  "v_cvt_pk_bf16_f32 v123, v70, v71\n"
  "buffer_store_dwordx4 v[120:123], v2, s[12:15], s35 offen offset:512 sc1\n"
  "v_pk_add_f32 v[204:205], v[204:205], v[96:97]\n"
  "v_pk_add_f32 v[206:207], v[206:207], v[98:99]\n"
  "v_pk_add_f32 v[220:221], v[220:221], v[100:101]\n"
  "v_pk_add_f32 v[222:223], v[222:223], v[102:103]\n"
  "v_pk_mul_f32 v[64:65], v[204:205], v[204:205]\n"
  "v_pk_mul_f32 v[66:67], v[206:207], v[206:207]\n"
  "v_pk_mul_f32 v[68:69], v[220:221], v[220:221]\n"
  "v_pk_mul_f32 v[70:71], v[222:223], v[222:223]\n"
  "v_pk_fma_f32 v[64:65], v[64:65], s[84:85], v[24:25]\n"
  "v_pk_fma_f32 v[66:67], v[66:67], s[84:85], v[24:25]\n"
  "v_pk_fma_f32 v[68:69], v[68:69], s[84:85], v[24:25]\n"
  "v_pk_fma_f32 v[70:71], v[70:71], s[84:85], v[24:25]\n"
  "v_pk_mul_f32 v[64:65], v[64:65], v[204:205]\n"
  "v_pk_mul_f32 v[66:67], v[66:67], v[206:207]\n"
  "v_pk_mul_f32 v[68:69], v[68:69], v[220:221]\n"
  "v_pk_mul_f32 v[70:71], v[70:71], v[222:223]\n"
  "v_exp_f32 v64, v64\n"
  "v_exp_f32 v65, v65\n"
  "v_exp_f32 v66, v66\n"
  "v_exp_f32 v67, v67\n"
  "v_exp_f32 v68, v68\n"
  "v_exp_f32 v69, v69\n"
  "v_exp_f32 v70, v70\n"
  "v_exp_f32 v71, v71\n"
  "v_pk_add_f32 v[64:65], v[64:65], s[86:87]\n"
  "v_pk_add_f32 v[66:67], v[66:67], s[86:87]\n"
  "v_pk_add_f32 v[68:69], v[68:69], s[86:87]\n"
  "v_pk_add_f32 v[70:71], v[70:71], s[86:87]\n"
  "v_rcp_f32 v64, v64\n"
  "v_rcp_f32 v65, v65\n"
  "v_rcp_f32 v66, v66\n"
  "v_rcp_f32 v67, v67\n"
  "v_rcp_f32 v68, v68\n"
  "v_rcp_f32 v69, v69\n"
  "v_rcp_f32 v70, v70\n"
  "v_rcp_f32 v71, v71\n"
  "v_pk_mul_f32 v[64:65], v[64:65], v[204:205]\n"
  "v_pk_mul_f32 v[66:67], v[66:67], v[206:207]\n"
  "v_pk_mul_f32 v[68:69], v[68:69], v[220:221]\n"
  "v_pk_mul_f32 v[70:71], v[70:71], v[222:223]\n"
  "v_cvt_pk_bf16_f32 v124, v64, v65\n"
  "v_cvt_pk_bf16_f32 v125, v66, v67\n"
  "v_cvt_pk_bf16_f32 v126, v68, v69\n"
  "v_cvt_pk_bf16_f32 v127, v70, v71\n"
  "buffer_store_dwordx4 v[124:127], v2, s[12:15], s35 offen offset:768 sc1\n"
  "s_add_u32 s35, s35, 0x4000\n"
  "s_waitcnt vmcnt(28)\n"
  "s_barrier\n"
  "ds_read_b64_tr_b16 a[0:1], v7\n"
  "ds_read_b64_tr_b16 a[2:3], v7 offset:4096\n"
  "ds_read_b64_tr_b16 a[4:5], v8\n"
  "ds_read_b64_tr_b16 a[6:7], v8 offset:4096\n"
  "ds_read_b64_tr_b16 a[8:9], v9\n"
  "ds_read_b64_tr_b16 a[10:11], v9 offset:4096\n"
  "ds_read_b64_tr_b16 a[12:13], v10\n"
  "ds_read_b64_tr_b16 a[14:15], v10 offset:4096\n"
  "ds_read_b64_tr_b16 a[16:17], v11\n"
  "ds_read_b64_tr_b16 a[18:19], v11 offset:4096\n"
  "ds_read_b64_tr_b16 a[20:21], v12\n"
  "ds_read_b64_tr_b16 a[22:23], v12 offset:4096\n"
  "ds_read_b64_tr_b16 a[24:25], v13\n"
  "ds_read_b64_tr_b16 a[26:27], v13 offset:4096\n"
  "ds_read_b64_tr_b16 a[28:29], v14\n"
  "ds_read_b64_tr_b16 a[30:31], v14 offset:4096\n"
  "v_pk_add_f32 v[224:225], v[224:225], v[104:105]\n"
  "v_pk_add_f32 v[226:227], v[226:227], v[106:107]\n"
  "v_pk_add_f32 v[240:241], v[240:241], v[108:109]\n"
  "v_pk_add_f32 v[242:243], v[242:243], v[110:111]\n"
  "v_pk_mul_f32 v[64:65], v[224:225], v[224:225]\n"
  "v_pk_mul_f32 v[66:67], v[226:227], v[226:227]\n"
  "v_pk_mul_f32 v[68:69], v[240:241], v[240:241]\n"
  "v_pk_mul_f32 v[70:71], v[242:243], v[242:243]\n"
  "v_pk_fma_f32 v[64:65], v[64:65], s[84:85], v[24:25]\n"
  "v_pk_fma_f32 v[66:67], v[66:67], s[84:85], v[24:25]\n"
  "v_pk_fma_f32 v[68:69], v[68:69], s[84:85], v[24:25]\n"
  "v_pk_fma_f32 v[70:71], v[70:71], s[84:85], v[24:25]\n"
  "v_pk_mul_f32 v[64:65], v[64:65], v[224:225]\n"
  "v_pk_mul_f32 v[66:67], v[66:67], v[226:227]\n"
  "v_pk_mul_f32 v[68:69], v[68:69], v[240:241]\n"
  "v_pk_mul_f32 v[70:71], v[70:71], v[242:243]\n"
  "v_exp_f32 v64, v64\n"
  "v_exp_f32 v65, v65\n"
  "v_exp_f32 v66, v66\n"
  "v_exp_f32 v67, v67\n"
  "v_exp_f32 v68, v68\n"
  "v_exp_f32 v69, v69\n"
  "v_exp_f32 v70, v70\n"
  "v_exp_f32 v71, v71\n"
  "v_pk_add_f32 v[64:65], v[64:65], s[86:87]\n"
  "v_pk_add_f32 v[66:67], v[66:67], s[86:87]\n"
  "v_pk_add_f32 v[68:69], v[68:69], s[86:87]\n"
  "v_pk_add_f32 v[70:71], v[70:71], s[86:87]\n"
  "v_rcp_f32 v64, v64\n"
  "v_rcp_f32 v65, v65\n"
  "v_rcp_f32 v66, v66\n"
  "v_rcp_f32 v67, v67\n"
  "v_rcp_f32 v68, v68\n"
  "v_rcp_f32 v69, v69\n"
  "v_rcp_f32 v70, v70\n"
  "v_rcp_f32 v71, v71\n"
  "v_pk_mul_f32 v[64:65], v[64:65], v[224:225]\n"
  "v_pk_mul_f32 v[66:67], v[66:67], v[226:227]\n"
  "v_pk_mul_f32 v[68:69], v[68:69], v[240:241]\n"
  "v_pk_mul_f32 v[70:71], v[70:71], v[242:243]\n"
  "v_cvt_pk_bf16_f32 v112, v64, v65\n"
  "v_cvt_pk_bf16_f32 v113, v66, v67\n"
  "v_cvt_pk_bf16_f32 v114, v68, v69\n"
  "v_cvt_pk_bf16_f32 v115, v70, v71\n"
  "buffer_store_dwordx4 v[112:115], v2, s[12:15], s35 offen sc1\n"
  "v_pk_add_f32 v[228:229], v[228:229], v[104:105]\n"
  "v_pk_add_f32 v[230:231], v[230:231], v[106:107]\n"
  "v_pk_add_f32 v[244:245], v[244:245], v[108:109]\n"
  "v_pk_add_f32 v[246:247], v[246:247], v[110:111]\n"
  "v_pk_mul_f32 v[64:65], v[228:229], v[228:229]\n"
  "v_pk_mul_f32 v[66:67], v[230:231], v[230:231]\n"
  "v_pk_mul_f32 v[68:69], v[244:245], v[244:245]\n"
  "v_pk_mul_f32 v[70:71], v[246:247], v[246:247]\n"
  "v_pk_fma_f32 v[64:65], v[64:65], s[84:85], v[24:25]\n"
  "v_pk_fma_f32 v[66:67], v[66:67], s[84:85], v[24:25]\n"
  "v_pk_fma_f32 v[68:69], v[68:69], s[84:85], v[24:25]\n"
  "v_pk_fma_f32 v[70:71], v[70:71], s[84:85], v[24:25]\n"
  "v_pk_mul_f32 v[64:65], v[64:65], v[228:229]\n"
  "v_pk_mul_f32 v[66:67], v[66:67], v[230:231]\n"
  "v_pk_mul_f32 v[68:69], v[68:69], v[244:245]\n"
  "v_pk_mul_f32 v[70:71], v[70:71], v[246:247]\n"
  "v_exp_f32 v64, v64\n"
  "v_exp_f32 v65, v65\n"
  "v_exp_f32 v66, v66\n"
  "v_exp_f32 v67, v67\n"
  "v_exp_f32 v68, v68\n"
  "v_exp_f32 v69, v69\n"
  "v_exp_f32 v70, v70\n"
  "v_exp_f32 v71, v71\n"
  "v_pk_add_f32 v[64:65], v[64:65], s[86:87]\n"
  "v_pk_add_f32 v[66:67], v[66:67], s[86:87]\n"
  "v_pk_add_f32 v[68:69], v[68:69], s[86:87]\n"
  "v_pk_add_f32 v[70:71], v[70:71], s[86:87]\n"
  "v_rcp_f32 v64, v64\n"
  "v_rcp_f32 v65, v65\n"
  "v_rcp_f32 v66, v66\n"
  "v_rcp_f32 v67, v67\n"
  "v_rcp_f32 v68, v68\n"
  "v_rcp_f32 v69, v69\n"
  "v_rcp_f32 v70, v70\n"
  "v_rcp_f32 v71, v71\n"
  "v_pk_mul_f32 v[64:65], v[64:65], v[228:229]\n"
  "v_pk_mul_f32 v[66:67], v[66:67], v[230:231]\n"
  "v_pk_mul_f32 v[68:69], v[68:69], v[244:245]\n"
  "v_pk_mul_f32 v[70:71], v[70:71], v[246:247]\n"
  "v_cvt_pk_bf16_f32 v116, v64, v65\n"
  "v_cvt_pk_bf16_f32 v117, v66, v67\n"
  "v_cvt_pk_bf16_f32 v118, v68, v69\n"
  "v_cvt_pk_bf16_f32 v119, v70, v71\n"
  "buffer_store_dwordx4 v[116:119], v2, s[12:15], s35 offen offset:256 sc1\n"
  "v_pk_add_f32 v[232:233], v[232:233], v[104:105]\n"
  "v_pk_add_f32 v[234:235], v[234:235], v[106:107]\n"
  "v_pk_add_f32 v[248:249], v[248:249], v[108:109]\n"
  "v_pk_add_f32 v[250:251], v[250:251], v[110:111]\n"
  "v_pk_mul_f32 v[64:65], v[232:233], v[232:233]\n"
  "v_pk_mul_f32 v[66:67], v[234:235], v[234:235]\n"
  "v_pk_mul_f32 v[68:69], v[248:249], v[248:249]\n"
  "v_pk_mul_f32 v[70:71], v[250:251], v[250:251]\n"
  "v_pk_fma_f32 v[64:65], v[64:65], s[84:85], v[24:25]\n"
  "v_pk_fma_f32 v[66:67], v[66:67], s[84:85], v[24:25]\n"
  "v_pk_fma_f32 v[68:69], v[68:69], s[84:85], v[24:25]\n"
  "v_pk_fma_f32 v[70:71], v[70:71], s[84:85], v[24:25]\n"
  "v_pk_mul_f32 v[64:65], v[64:65], v[232:233]\n"
  "v_pk_mul_f32 v[66:67], v[66:67], v[234:235]\n"
  "v_pk_mul_f32 v[68:69], v[68:69], v[248:249]\n"
  "v_pk_mul_f32 v[70:71], v[70:71], v[250:251]\n"
  "v_exp_f32 v64, v64\n"
  "v_exp_f32 v65, v65\n"
  "v_exp_f32 v66, v66\n"
  "v_exp_f32 v67, v67\n"
  "v_exp_f32 v68, v68\n"
  "v_exp_f32 v69, v69\n"
  "v_exp_f32 v70, v70\n"
  "v_exp_f32 v71, v71\n"
  "v_pk_add_f32 v[64:65], v[64:65], s[86:87]\n"
  "v_pk_add_f32 v[66:67], v[66:67], s[86:87]\n"
  "v_pk_add_f32 v[68:69], v[68:69], s[86:87]\n"
  "v_pk_add_f32 v[70:71], v[70:71], s[86:87]\n"
  "v_rcp_f32 v64, v64\n"
  "v_rcp_f32 v65, v65\n"
  "v_rcp_f32 v66, v66\n"
  "v_rcp_f32 v67, v67\n"
  "v_rcp_f32 v68, v68\n"
  "v_rcp_f32 v69, v69\n"
  "v_rcp_f32 v70, v70\n"
  "v_rcp_f32 v71, v71\n"
  "v_pk_mul_f32 v[64:65], v[64:65], v[232:233]\n"
  "v_pk_mul_f32 v[66:67], v[66:67], v[234:235]\n"
  "v_pk_mul_f32 v[68:69], v[68:69], v[248:249]\n"
  "v_pk_mul_f32 v[70:71], v[70:71], v[250:251]\n"
  "v_cvt_pk_bf16_f32 v120, v64, v65\n"
  "v_cvt_pk_bf16_f32 v121, v66, v67\n"
  "v_cvt_pk_bf16_f32 v122, v68, v69\n"
  "v_cvt_pk_bf16_f32 v123, v70, v71\n"
  "buffer_store_dwordx4 v[120:123], v2, s[12:15], s35 offen offset:512 sc1\n"
  "v_pk_add_f32 v[236:237], v[236:237], v[104:105]\n"
  "v_pk_add_f32 v[238:239], v[238:239], v[106:107]\n"
  "v_pk_add_f32 v[252:253], v[252:253], v[108:109]\n"
  "v_pk_add_f32 v[254:255], v[254:255], v[110:111]\n"
  "v_pk_mul_f32 v[64:65], v[236:237], v[236:237]\n"
  "v_pk_mul_f32 v[66:67], v[238:239], v[238:239]\n"
  "v_pk_mul_f32 v[68:69], v[252:253], v[252:253]\n"
  "v_pk_mul_f32 v[70:71], v[254:255], v[254:255]\n"
  "v_pk_fma_f32 v[64:65], v[64:65], s[84:85], v[24:25]\n"
  "v_pk_fma_f32 v[66:67], v[66:67], s[84:85], v[24:25]\n"
  "v_pk_fma_f32 v[68:69], v[68:69], s[84:85], v[24:25]\n"
  "v_pk_fma_f32 v[70:71], v[70:71], s[84:85], v[24:25]\n"
  "v_pk_mul_f32 v[64:65], v[64:65], v[236:237]\n"
  "v_pk_mul_f32 v[66:67], v[66:67], v[238:239]\n"
  "v_pk_mul_f32 v[68:69], v[68:69], v[252:253]\n"
  "v_pk_mul_f32 v[70:71], v[70:71], v[254:255]\n"
  "v_exp_f32 v64, v64\n"
  "v_exp_f32 v65, v65\n"
  "v_exp_f32 v66, v66\n"
  "v_exp_f32 v67, v67\n"
  "v_exp_f32 v68, v68\n"
  "v_exp_f32 v69, v69\n"
  "v_exp_f32 v70, v70\n"
  "v_exp_f32 v71, v71\n"
  "v_pk_add_f32 v[64:65], v[64:65], s[86:87]\n"
  "v_pk_add_f32 v[66:67], v[66:67], s[86:87]\n"
  "v_pk_add_f32 v[68:69], v[68:69], s[86:87]\n"
  "v_pk_add_f32 v[70:71], v[70:71], s[86:87]\n"
  "v_rcp_f32 v64, v64\n"
  "v_rcp_f32 v65, v65\n"
  "v_rcp_f32 v66, v66\n"
  "v_rcp_f32 v67, v67\n"
  "v_rcp_f32 v68, v68\n"
  "v_rcp_f32 v69, v69\n"
  "v_rcp_f32 v70, v70\n"
  "v_rcp_f32 v71, v71\n"
  "v_pk_mul_f32 v[64:65], v[64:65], v[236:237]\n"
  "v_pk_mul_f32 v[66:67], v[66:67], v[238:239]\n"
  "v_pk_mul_f32 v[68:69], v[68:69], v[252:253]\n"
  "v_pk_mul_f32 v[70:71], v[70:71], v[254:255]\n"
  "v_cvt_pk_bf16_f32 v124, v64, v65\n"
  "v_cvt_pk_bf16_f32 v125, v66, v67\n"
  "v_cvt_pk_bf16_f32 v126, v68, v69\n"
  "v_cvt_pk_bf16_f32 v127, v70, v71\n"
  "buffer_store_dwordx4 v[124:127], v2, s[12:15], s35 offen offset:768 sc1\n"
  "s_add_u32 s35, s35, 0x4000\n"
  "s_mov_b32 s14, 0\n"
  "s_branch P3_ENTER_%=\n"
  "F1_G_%=:\n"
  "v_lshlrev_b32 v80, 16, v64\n"
  "v_and_b32 v81, 0xffff0000, v64\n"
  "v_lshlrev_b32 v82, 16, v65\n"
  "v_and_b32 v83, 0xffff0000, v65\n"
  "v_lshlrev_b32 v84, 16, v66\n"
  "v_and_b32 v85, 0xffff0000, v66\n"
  "v_lshlrev_b32 v86, 16, v67\n"
  "v_and_b32 v87, 0xffff0000, v67\n"
  "v_lshlrev_b32 v88, 16, v68\n"
  "v_and_b32 v89, 0xffff0000, v68\n"
  "v_lshlrev_b32 v90, 16, v69\n"
  "v_and_b32 v91, 0xffff0000, v69\n"
  "v_lshlrev_b32 v92, 16, v70\n"
  "v_and_b32 v93, 0xffff0000, v70\n"
  "v_lshlrev_b32 v94, 16, v71\n"
  "v_and_b32 v95, 0xffff0000, v71\n"
  "v_lshlrev_b32 v96, 16, v72\n"
  "v_and_b32 v97, 0xffff0000, v72\n"
  "v_lshlrev_b32 v98, 16, v73\n"
  "v_and_b32 v99, 0xffff0000, v73\n"
  "v_lshlrev_b32 v100, 16, v74\n"
  "v_and_b32 v101, 0xffff0000, v74\n"
  "v_lshlrev_b32 v102, 16, v75\n"
  "v_and_b32 v103, 0xffff0000, v75\n"
  "v_lshlrev_b32 v104, 16, v76\n"
  "v_and_b32 v105, 0xffff0000, v76\n"
  "v_lshlrev_b32 v106, 16, v77\n"
  "v_and_b32 v107, 0xffff0000, v77\n"
  "v_lshlrev_b32 v108, 16, v78\n"
  "v_and_b32 v109, 0xffff0000, v78\n"
  "v_lshlrev_b32 v110, 16, v79\n"
  "v_and_b32 v111, 0xffff0000, v79\n"
  "s_mov_b32 s84, 0xbdd2d3e8\n"
  "s_mov_b32 s85, 0xbdd2d3e8\n"
  "s_mov_b32 s86, 0x3f800000\n"
  "s_mov_b32 s87, 0x3f800000\n"
  "v_mov_b32 v24, 0xc0135761\n"
  "v_mov_b32 v25, 0xc0135761\n"
  "v_pk_add_f32 v[128:129], v[128:129], v[80:81]\n"
  "v_pk_add_f32 v[130:131], v[130:131], v[82:83]\n"
  "v_pk_add_f32 v[144:145], v[144:145], v[84:85]\n"
  "v_pk_add_f32 v[146:147], v[146:147], v[86:87]\n"
  "v_pk_mul_f32 v[64:65], v[128:129], v[128:129]\n"
  "v_pk_mul_f32 v[66:67], v[130:131], v[130:131]\n"
  "v_pk_mul_f32 v[68:69], v[144:145], v[144:145]\n"
  "v_pk_mul_f32 v[70:71], v[146:147], v[146:147]\n"
  "v_pk_fma_f32 v[64:65], v[64:65], s[84:85], v[24:25]\n"
  "v_pk_fma_f32 v[66:67], v[66:67], s[84:85], v[24:25]\n"
  "v_pk_fma_f32 v[68:69], v[68:69], s[84:85], v[24:25]\n"
  "v_pk_fma_f32 v[70:71], v[70:71], s[84:85], v[24:25]\n"
  "v_pk_mul_f32 v[64:65], v[64:65], v[128:129]\n"
  "v_pk_mul_f32 v[66:67], v[66:67], v[130:131]\n"
  "v_pk_mul_f32 v[68:69], v[68:69], v[144:145]\n"
  "v_pk_mul_f32 v[70:71], v[70:71], v[146:147]\n"
  "v_exp_f32 v64, v64\n"
  "v_exp_f32 v65, v65\n"
  "v_exp_f32 v66, v66\n"
  "v_exp_f32 v67, v67\n"
  "v_exp_f32 v68, v68\n"
  "v_exp_f32 v69, v69\n"
  "v_exp_f32 v70, v70\n"
  "v_exp_f32 v71, v71\n"
  "v_pk_add_f32 v[64:65], v[64:65], s[86:87]\n"
  "v_pk_add_f32 v[66:67], v[66:67], s[86:87]\n"
  "v_pk_add_f32 v[68:69], v[68:69], s[86:87]\n"
  "v_pk_add_f32 v[70:71], v[70:71], s[86:87]\n"
  "v_rcp_f32 v64, v64\n"
  "v_rcp_f32 v65, v65\n"
  "v_rcp_f32 v66, v66\n"
  "v_rcp_f32 v67, v67\n"
  "v_rcp_f32 v68, v68\n"
  "v_rcp_f32 v69, v69\n"
  "v_rcp_f32 v70, v70\n"
  "v_rcp_f32 v71, v71\n"
  "v_pk_mul_f32 v[64:65], v[64:65], v[128:129]\n"
  "v_pk_mul_f32 v[66:67], v[66:67], v[130:131]\n"
  "v_pk_mul_f32 v[68:69], v[68:69], v[144:145]\n"
  "v_pk_mul_f32 v[70:71], v[70:71], v[146:147]\n"
  "v_cvt_pk_bf16_f32 v112, v64, v65\n"
  "v_cvt_pk_bf16_f32 v113, v66, v67\n"
  "v_cvt_pk_bf16_f32 v114, v68, v69\n"
  "v_cvt_pk_bf16_f32 v115, v70, v71\n"
  "buffer_store_dwordx4 v[112:115], v2, s[12:15], s35 offen sc1\n"
  "v_pk_add_f32 v[132:133], v[132:133], v[80:81]\n"
  "v_pk_add_f32 v[134:135], v[134:135], v[82:83]\n"
  "v_pk_add_f32 v[148:149], v[148:149], v[84:85]\n"
  "v_pk_add_f32 v[150:151], v[150:151], v[86:87]\n"
  "v_pk_mul_f32 v[64:65], v[132:133], v[132:133]\n"
  "v_pk_mul_f32 v[66:67], v[134:135], v[134:135]\n"
  "v_pk_mul_f32 v[68:69], v[148:149], v[148:149]\n"
  "v_pk_mul_f32 v[70:71], v[150:151], v[150:151]\n"
  "v_pk_fma_f32 v[64:65], v[64:65], s[84:85], v[24:25]\n"
  "v_pk_fma_f32 v[66:67], v[66:67], s[84:85], v[24:25]\n"
  "v_pk_fma_f32 v[68:69], v[68:69], s[84:85], v[24:25]\n"
  "v_pk_fma_f32 v[70:71], v[70:71], s[84:85], v[24:25]\n"
  "v_pk_mul_f32 v[64:65], v[64:65], v[132:133]\n"
  "v_pk_mul_f32 v[66:67], v[66:67], v[134:135]\n"
  "v_pk_mul_f32 v[68:69], v[68:69], v[148:149]\n"
  "v_pk_mul_f32 v[70:71], v[70:71], v[150:151]\n"
  "v_exp_f32 v64, v64\n"
  "v_exp_f32 v65, v65\n"
  "v_exp_f32 v66, v66\n"
  "v_exp_f32 v67, v67\n"
  "v_exp_f32 v68, v68\n"
  "v_exp_f32 v69, v69\n"
  "v_exp_f32 v70, v70\n"
  "v_exp_f32 v71, v71\n"
  "v_pk_add_f32 v[64:65], v[64:65], s[86:87]\n"
  "v_pk_add_f32 v[66:67], v[66:67], s[86:87]\n"
  "v_pk_add_f32 v[68:69], v[68:69], s[86:87]\n"
  "v_pk_add_f32 v[70:71], v[70:71], s[86:87]\n"
  "v_rcp_f32 v64, v64\n"
  "v_rcp_f32 v65, v65\n"
  "v_rcp_f32 v66, v66\n"
  "v_rcp_f32 v67, v67\n"
  "v_rcp_f32 v68, v68\n"
  "v_rcp_f32 v69, v69\n"
  "v_rcp_f32 v70, v70\n"
  "v_rcp_f32 v71, v71\n"
  "v_pk_mul_f32 v[64:65], v[64:65], v[132:133]\n"
  "v_pk_mul_f32 v[66:67], v[66:67], v[134:135]\n"
  "v_pk_mul_f32 v[68:69], v[68:69], v[148:149]\n"
  "v_pk_mul_f32 v[70:71], v[70:71], v[150:151]\n"
  "v_cvt_pk_bf16_f32 v116, v64, v65\n"
  "v_cvt_pk_bf16_f32 v117, v66, v67\n"
  "v_cvt_pk_bf16_f32 v118, v68, v69\n"
  "v_cvt_pk_bf16_f32 v119, v70, v71\n"
  "buffer_store_dwordx4 v[116:119], v2, s[12:15], s35 offen offset:256 sc1\n"
  "v_pk_add_f32 v[136:137], v[136:137], v[80:81]\n"
  "v_pk_add_f32 v[138:139], v[138:139], v[82:83]\n"
  "v_pk_add_f32 v[152:153], v[152:153], v[84:85]\n"
  "v_pk_add_f32 v[154:155], v[154:155], v[86:87]\n"
  "v_pk_mul_f32 v[64:65], v[136:137], v[136:137]\n"
  "v_pk_mul_f32 v[66:67], v[138:139], v[138:139]\n"
  "v_pk_mul_f32 v[68:69], v[152:153], v[152:153]\n"
  "v_pk_mul_f32 v[70:71], v[154:155], v[154:155]\n"
  "v_pk_fma_f32 v[64:65], v[64:65], s[84:85], v[24:25]\n"
  "v_pk_fma_f32 v[66:67], v[66:67], s[84:85], v[24:25]\n"
  "v_pk_fma_f32 v[68:69], v[68:69], s[84:85], v[24:25]\n"
  "v_pk_fma_f32 v[70:71], v[70:71], s[84:85], v[24:25]\n"
  "v_pk_mul_f32 v[64:65], v[64:65], v[136:137]\n"
  "v_pk_mul_f32 v[66:67], v[66:67], v[138:139]\n"
  "v_pk_mul_f32 v[68:69], v[68:69], v[152:153]\n"
  "v_pk_mul_f32 v[70:71], v[70:71], v[154:155]\n"
  "v_exp_f32 v64, v64\n"
  "v_exp_f32 v65, v65\n"
  "v_exp_f32 v66, v66\n"
  "v_exp_f32 v67, v67\n"
  "v_exp_f32 v68, v68\n"
  "v_exp_f32 v69, v69\n"
  "v_exp_f32 v70, v70\n"
  "v_exp_f32 v71, v71\n"
  "v_pk_add_f32 v[64:65], v[64:65], s[86:87]\n"
  "v_pk_add_f32 v[66:67], v[66:67], s[86:87]\n"
  "v_pk_add_f32 v[68:69], v[68:69], s[86:87]\n"
  "v_pk_add_f32 v[70:71], v[70:71], s[86:87]\n"
  "v_rcp_f32 v64, v64\n"
  "v_rcp_f32 v65, v65\n"
  "v_rcp_f32 v66, v66\n"
  "v_rcp_f32 v67, v67\n"
  "v_rcp_f32 v68, v68\n"
  "v_rcp_f32 v69, v69\n"
  "v_rcp_f32 v70, v70\n"
  "v_rcp_f32 v71, v71\n"
  "v_pk_mul_f32 v[64:65], v[64:65], v[136:137]\n"
  "v_pk_mul_f32 v[66:67], v[66:67], v[138:139]\n"
  "v_pk_mul_f32 v[68:69], v[68:69], v[152:153]\n"
  "v_pk_mul_f32 v[70:71], v[70:71], v[154:155]\n"
  "v_cvt_pk_bf16_f32 v120, v64, v65\n"
  "v_cvt_pk_bf16_f32 v121, v66, v67\n"
  "v_cvt_pk_bf16_f32 v122, v68, v69\n"
  "v_cvt_pk_bf16_f32 v123, v70, v71\n"
  "buffer_store_dwordx4 v[120:123], v2, s[12:15], s35 offen offset:512 sc1\n"
  "v_pk_add_f32 v[140:141], v[140:141], v[80:81]\n"
  "v_pk_add_f32 v[142:143], v[142:143], v[82:83]\n"
  "v_pk_add_f32 v[156:157], v[156:157], v[84:85]\n"
  "v_pk_add_f32 v[158:159], v[158:159], v[86:87]\n"
  "v_pk_mul_f32 v[64:65], v[140:141], v[140:141]\n"
  "v_pk_mul_f32 v[66:67], v[142:143], v[142:143]\n"
  "v_pk_mul_f32 v[68:69], v[156:157], v[156:157]\n"
  "v_pk_mul_f32 v[70:71], v[158:159], v[158:159]\n"
  "v_pk_fma_f32 v[64:65], v[64:65], s[84:85], v[24:25]\n"
  "v_pk_fma_f32 v[66:67], v[66:67], s[84:85], v[24:25]\n"
  "v_pk_fma_f32 v[68:69], v[68:69], s[84:85], v[24:25]\n"
  "v_pk_fma_f32 v[70:71], v[70:71], s[84:85], v[24:25]\n"
  "v_pk_mul_f32 v[64:65], v[64:65], v[140:141]\n"
  "v_pk_mul_f32 v[66:67], v[66:67], v[142:143]\n"
  "v_pk_mul_f32 v[68:69], v[68:69], v[156:157]\n"
  "v_pk_mul_f32 v[70:71], v[70:71], v[158:159]\n"
  "v_exp_f32 v64, v64\n"
  "v_exp_f32 v65, v65\n"
  "v_exp_f32 v66, v66\n"
  "v_exp_f32 v67, v67\n"
  "v_exp_f32 v68, v68\n"
  "v_exp_f32 v69, v69\n"
  "v_exp_f32 v70, v70\n"
  "v_exp_f32 v71, v71\n"
  "v_pk_add_f32 v[64:65], v[64:65], s[86:87]\n"
  "v_pk_add_f32 v[66:67], v[66:67], s[86:87]\n"
  "v_pk_add_f32 v[68:69], v[68:69], s[86:87]\n"
  "v_pk_add_f32 v[70:71], v[70:71], s[86:87]\n"
  "v_rcp_f32 v64, v64\n"
  "v_rcp_f32 v65, v65\n"
  "v_rcp_f32 v66, v66\n"
  "v_rcp_f32 v67, v67\n"
  "v_rcp_f32 v68, v68\n"
  "v_rcp_f32 v69, v69\n"
  "v_rcp_f32 v70, v70\n"
  "v_rcp_f32 v71, v71\n"
  "v_pk_mul_f32 v[64:65], v[64:65], v[140:141]\n"
  "v_pk_mul_f32 v[66:67], v[66:67], v[142:143]\n"
  "v_pk_mul_f32 v[68:69], v[68:69], v[156:157]\n"
  "v_pk_mul_f32 v[70:71], v[70:71], v[158:159]\n"
  "v_cvt_pk_bf16_f32 v124, v64, v65\n"
  "v_cvt_pk_bf16_f32 v125, v66, v67\n"
  "v_cvt_pk_bf16_f32 v126, v68, v69\n"
  "v_cvt_pk_bf16_f32 v127, v70, v71\n"
  "buffer_store_dwordx4 v[124:127], v2, s[12:15], s35 offen offset:768 sc1\n"
  "s_add_u32 s35, s35, 0x4000\n"
  "v_pk_add_f32 v[160:161], v[160:161], v[88:89]\n"
  "v_pk_add_f32 v[162:163], v[162:163], v[90:91]\n"
  "v_pk_add_f32 v[176:177], v[176:177], v[92:93]\n"
  "v_pk_add_f32 v[178:179], v[178:179], v[94:95]\n"
  "v_pk_mul_f32 v[64:65], v[160:161], v[160:161]\n"
  "v_pk_mul_f32 v[66:67], v[162:163], v[162:163]\n"
  "v_pk_mul_f32 v[68:69], v[176:177], v[176:177]\n"
  "v_pk_mul_f32 v[70:71], v[178:179], v[178:179]\n"
  "v_pk_fma_f32 v[64:65], v[64:65], s[84:85], v[24:25]\n"
  "v_pk_fma_f32 v[66:67], v[66:67], s[84:85], v[24:25]\n"
  "v_pk_fma_f32 v[68:69], v[68:69], s[84:85], v[24:25]\n"
  "v_pk_fma_f32 v[70:71], v[70:71], s[84:85], v[24:25]\n"
  "v_pk_mul_f32 v[64:65], v[64:65], v[160:161]\n"
  "v_pk_mul_f32 v[66:67], v[66:67], v[162:163]\n"
  "v_pk_mul_f32 v[68:69], v[68:69], v[176:177]\n"
  "v_pk_mul_f32 v[70:71], v[70:71], v[178:179]\n"
  "v_exp_f32 v64, v64\n"
  "v_exp_f32 v65, v65\n"
  "v_exp_f32 v66, v66\n"
  "v_exp_f32 v67, v67\n"
  "v_exp_f32 v68, v68\n"
  "v_exp_f32 v69, v69\n"
  "v_exp_f32 v70, v70\n"
  "v_exp_f32 v71, v71\n"
  "v_pk_add_f32 v[64:65], v[64:65], s[86:87]\n"
  "v_pk_add_f32 v[66:67], v[66:67], s[86:87]\n"
  "v_pk_add_f32 v[68:69], v[68:69], s[86:87]\n"
  "v_pk_add_f32 v[70:71], v[70:71], s[86:87]\n"
  "v_rcp_f32 v64, v64\n"
  "v_rcp_f32 v65, v65\n"
  "v_rcp_f32 v66, v66\n"
  "v_rcp_f32 v67, v67\n"
  "v_rcp_f32 v68, v68\n"
  "v_rcp_f32 v69, v69\n"
  "v_rcp_f32 v70, v70\n"
  "v_rcp_f32 v71, v71\n"
  "v_pk_mul_f32 v[64:65], v[64:65], v[160:161]\n"
  "v_pk_mul_f32 v[66:67], v[66:67], v[162:163]\n"
  "v_pk_mul_f32 v[68:69], v[68:69], v[176:177]\n"
  "v_pk_mul_f32 v[70:71], v[70:71], v[178:179]\n"
  "v_cvt_pk_bf16_f32 v112, v64, v65\n"
  "v_cvt_pk_bf16_f32 v113, v66, v67\n"
  "v_cvt_pk_bf16_f32 v114, v68, v69\n"
  "v_cvt_pk_bf16_f32 v115, v70, v71\n"
  "buffer_store_dwordx4 v[112:115], v2, s[12:15], s35 offen sc1\n"
  "v_pk_add_f32 v[164:165], v[164:165], v[88:89]\n"
  "v_pk_add_f32 v[166:167], v[166:167], v[90:91]\n"
  "v_pk_add_f32 v[180:181], v[180:181], v[92:93]\n"
  "v_pk_add_f32 v[182:183], v[182:183], v[94:95]\n"
  "v_pk_mul_f32 v[64:65], v[164:165], v[164:165]\n"
  "v_pk_mul_f32 v[66:67], v[166:167], v[166:167]\n"
  "v_pk_mul_f32 v[68:69], v[180:181], v[180:181]\n"
  "v_pk_mul_f32 v[70:71], v[182:183], v[182:183]\n"
  "v_pk_fma_f32 v[64:65], v[64:65], s[84:85], v[24:25]\n"
  "v_pk_fma_f32 v[66:67], v[66:67], s[84:85], v[24:25]\n"
  "v_pk_fma_f32 v[68:69], v[68:69], s[84:85], v[24:25]\n"
  "v_pk_fma_f32 v[70:71], v[70:71], s[84:85], v[24:25]\n"
  "v_pk_mul_f32 v[64:65], v[64:65], v[164:165]\n"
  "v_pk_mul_f32 v[66:67], v[66:67], v[166:167]\n"
  "v_pk_mul_f32 v[68:69], v[68:69], v[180:181]\n"
  "v_pk_mul_f32 v[70:71], v[70:71], v[182:183]\n"
  "v_exp_f32 v64, v64\n"
  "v_exp_f32 v65, v65\n"
  "v_exp_f32 v66, v66\n"
  "v_exp_f32 v67, v67\n"
  "v_exp_f32 v68, v68\n"
  "v_exp_f32 v69, v69\n"
  "v_exp_f32 v70, v70\n"
  "v_exp_f32 v71, v71\n"
  "v_pk_add_f32 v[64:65], v[64:65], s[86:87]\n"
  "v_pk_add_f32 v[66:67], v[66:67], s[86:87]\n"
  "v_pk_add_f32 v[68:69], v[68:69], s[86:87]\n"
  "v_pk_add_f32 v[70:71], v[70:71], s[86:87]\n"
  "v_rcp_f32 v64, v64\n"
  "v_rcp_f32 v65, v65\n"
  "v_rcp_f32 v66, v66\n"
  "v_rcp_f32 v67, v67\n"
  "v_rcp_f32 v68, v68\n"
  "v_rcp_f32 v69, v69\n"
  "v_rcp_f32 v70, v70\n"
  "v_rcp_f32 v71, v71\n"
  "v_pk_mul_f32 v[64:65], v[64:65], v[164:165]\n"
  "v_pk_mul_f32 v[66:67], v[66:67], v[166:167]\n"
  "v_pk_mul_f32 v[68:69], v[68:69], v[180:181]\n"
  "v_pk_mul_f32 v[70:71], v[70:71], v[182:183]\n"
  "v_cvt_pk_bf16_f32 v116, v64, v65\n"
  "v_cvt_pk_bf16_f32 v117, v66, v67\n"
  "v_cvt_pk_bf16_f32 v118, v68, v69\n"
  "v_cvt_pk_bf16_f32 v119, v70, v71\n"
  "buffer_store_dwordx4 v[116:119], v2, s[12:15], s35 offen offset:256 sc1\n"
  "v_pk_add_f32 v[168:169], v[168:169], v[88:89]\n"
  "v_pk_add_f32 v[170:171], v[170:171], v[90:91]\n"
  "v_pk_add_f32 v[184:185], v[184:185], v[92:93]\n"
  "v_pk_add_f32 v[186:187], v[186:187], v[94:95]\n"
  "v_pk_mul_f32 v[64:65], v[168:169], v[168:169]\n"
  "v_pk_mul_f32 v[66:67], v[170:171], v[170:171]\n"
  "v_pk_mul_f32 v[68:69], v[184:185], v[184:185]\n"
  "v_pk_mul_f32 v[70:71], v[186:187], v[186:187]\n"
  "v_pk_fma_f32 v[64:65], v[64:65], s[84:85], v[24:25]\n"
  "v_pk_fma_f32 v[66:67], v[66:67], s[84:85], v[24:25]\n"
  "v_pk_fma_f32 v[68:69], v[68:69], s[84:85], v[24:25]\n"
  "v_pk_fma_f32 v[70:71], v[70:71], s[84:85], v[24:25]\n"
  "v_pk_mul_f32 v[64:65], v[64:65], v[168:169]\n"
  "v_pk_mul_f32 v[66:67], v[66:67], v[170:171]\n"
  "v_pk_mul_f32 v[68:69], v[68:69], v[184:185]\n"
  "v_pk_mul_f32 v[70:71], v[70:71], v[186:187]\n"
  "v_exp_f32 v64, v64\n"
  "v_exp_f32 v65, v65\n"
  "v_exp_f32 v66, v66\n"
  "v_exp_f32 v67, v67\n"
  "v_exp_f32 v68, v68\n"
  "v_exp_f32 v69, v69\n"
  "v_exp_f32 v70, v70\n"
  "v_exp_f32 v71, v71\n"
  "v_pk_add_f32 v[64:65], v[64:65], s[86:87]\n"
  "v_pk_add_f32 v[66:67], v[66:67], s[86:87]\n"
  "v_pk_add_f32 v[68:69], v[68:69], s[86:87]\n"
  "v_pk_add_f32 v[70:71], v[70:71], s[86:87]\n"
  "v_rcp_f32 v64, v64\n"
  "v_rcp_f32 v65, v65\n"
  "v_rcp_f32 v66, v66\n"
  "v_rcp_f32 v67, v67\n"
  "v_rcp_f32 v68, v68\n"
  "v_rcp_f32 v69, v69\n"
  "v_rcp_f32 v70, v70\n"
  "v_rcp_f32 v71, v71\n"
  "v_pk_mul_f32 v[64:65], v[64:65], v[168:169]\n"
  "v_pk_mul_f32 v[66:67], v[66:67], v[170:171]\n"
  "v_pk_mul_f32 v[68:69], v[68:69], v[184:185]\n"
  "v_pk_mul_f32 v[70:71], v[70:71], v[186:187]\n"
  "v_cvt_pk_bf16_f32 v120, v64, v65\n"
  "v_cvt_pk_bf16_f32 v121, v66, v67\n"
  "v_cvt_pk_bf16_f32 v122, v68, v69\n"
  "v_cvt_pk_bf16_f32 v123, v70, v71\n"
  "buffer_store_dwordx4 v[120:123], v2, s[12:15], s35 offen offset:512 sc1\n"
  "v_pk_add_f32 v[172:173], v[172:173], v[88:89]\n"
  "v_pk_add_f32 v[174:175], v[174:175], v[90:91]\n"
  "v_pk_add_f32 v[188:189], v[188:189], v[92:93]\n"
  "v_pk_add_f32 v[190:191], v[190:191], v[94:95]\n"
  "v_pk_mul_f32 v[64:65], v[172:173], v[172:173]\n"
  "v_pk_mul_f32 v[66:67], v[174:175], v[174:175]\n"
  "v_pk_mul_f32 v[68:69], v[188:189], v[188:189]\n"
  "v_pk_mul_f32 v[70:71], v[190:191], v[190:191]\n"
  "v_pk_fma_f32 v[64:65], v[64:65], s[84:85], v[24:25]\n"
  "v_pk_fma_f32 v[66:67], v[66:67], s[84:85], v[24:25]\n"
  "v_pk_fma_f32 v[68:69], v[68:69], s[84:85], v[24:25]\n"
  "v_pk_fma_f32 v[70:71], v[70:71], s[84:85], v[24:25]\n"
  "v_pk_mul_f32 v[64:65], v[64:65], v[172:173]\n"
  "v_pk_mul_f32 v[66:67], v[66:67], v[174:175]\n"
  "v_pk_mul_f32 v[68:69], v[68:69], v[188:189]\n"
  "v_pk_mul_f32 v[70:71], v[70:71], v[190:191]\n"
  "v_exp_f32 v64, v64\n"
  "v_exp_f32 v65, v65\n"
  "v_exp_f32 v66, v66\n"
  "v_exp_f32 v67, v67\n"
  "v_exp_f32 v68, v68\n"
  "v_exp_f32 v69, v69\n"
  "v_exp_f32 v70, v70\n"
  "v_exp_f32 v71, v71\n"
  "v_pk_add_f32 v[64:65], v[64:65], s[86:87]\n"
  "v_pk_add_f32 v[66:67], v[66:67], s[86:87]\n"
  "v_pk_add_f32 v[68:69], v[68:69], s[86:87]\n"
  "v_pk_add_f32 v[70:71], v[70:71], s[86:87]\n"
  "v_rcp_f32 v64, v64\n"
  "v_rcp_f32 v65, v65\n"
  "v_rcp_f32 v66, v66\n"
  "v_rcp_f32 v67, v67\n"
  "v_rcp_f32 v68, v68\n"
  "v_rcp_f32 v69, v69\n"
  "v_rcp_f32 v70, v70\n"
  "v_rcp_f32 v71, v71\n"
  "v_pk_mul_f32 v[64:65], v[64:65], v[172:173]\n"
  "v_pk_mul_f32 v[66:67], v[66:67], v[174:175]\n"
  "v_pk_mul_f32 v[68:69], v[68:69], v[188:189]\n"
  "v_pk_mul_f32 v[70:71], v[70:71], v[190:191]\n"
  "v_cvt_pk_bf16_f32 v124, v64, v65\n"
  "v_cvt_pk_bf16_f32 v125, v66, v67\n"
  "v_cvt_pk_bf16_f32 v126, v68, v69\n"
  "v_cvt_pk_bf16_f32 v127, v70, v71\n"
  "buffer_store_dwordx4 v[124:127], v2, s[12:15], s35 offen offset:768 sc1\n"
  "s_add_u32 s35, s35, 0x4000\n"
  "v_pk_add_f32 v[192:193], v[192:193], v[96:97]\n"
  "v_pk_add_f32 v[194:195], v[194:195], v[98:99]\n"
  "v_pk_add_f32 v[208:209], v[208:209], v[100:101]\n"
  "v_pk_add_f32 v[210:211], v[210:211], v[102:103]\n"
  "v_pk_mul_f32 v[64:65], v[192:193], v[192:193]\n"
  "v_pk_mul_f32 v[66:67], v[194:195], v[194:195]\n"
  "v_pk_mul_f32 v[68:69], v[208:209], v[208:209]\n"
  "v_pk_mul_f32 v[70:71], v[210:211], v[210:211]\n"
  "v_pk_fma_f32 v[64:65], v[64:65], s[84:85], v[24:25]\n"
  "v_pk_fma_f32 v[66:67], v[66:67], s[84:85], v[24:25]\n"
  "v_pk_fma_f32 v[68:69], v[68:69], s[84:85], v[24:25]\n"
  "v_pk_fma_f32 v[70:71], v[70:71], s[84:85], v[24:25]\n"
  "v_pk_mul_f32 v[64:65], v[64:65], v[192:193]\n"
  "v_pk_mul_f32 v[66:67], v[66:67], v[194:195]\n"
  "v_pk_mul_f32 v[68:69], v[68:69], v[208:209]\n"
  "v_pk_mul_f32 v[70:71], v[70:71], v[210:211]\n"
  "v_exp_f32 v64, v64\n"
  "v_exp_f32 v65, v65\n"
  "v_exp_f32 v66, v66\n"
  "v_exp_f32 v67, v67\n"
  "v_exp_f32 v68, v68\n"
  "v_exp_f32 v69, v69\n"
  "v_exp_f32 v70, v70\n"
  "v_exp_f32 v71, v71\n"
  "v_pk_add_f32 v[64:65], v[64:65], s[86:87]\n"
  "v_pk_add_f32 v[66:67], v[66:67], s[86:87]\n"
  "v_pk_add_f32 v[68:69], v[68:69], s[86:87]\n"
  "v_pk_add_f32 v[70:71], v[70:71], s[86:87]\n"
  "v_rcp_f32 v64, v64\n"
  "v_rcp_f32 v65, v65\n"
  "v_rcp_f32 v66, v66\n"
  "v_rcp_f32 v67, v67\n"
  "v_rcp_f32 v68, v68\n"
  "v_rcp_f32 v69, v69\n"
  "v_rcp_f32 v70, v70\n"
  "v_rcp_f32 v71, v71\n"
  "v_pk_mul_f32 v[64:65], v[64:65], v[192:193]\n"
  "v_pk_mul_f32 v[66:67], v[66:67], v[194:195]\n"
  "v_pk_mul_f32 v[68:69], v[68:69], v[208:209]\n"
  "v_pk_mul_f32 v[70:71], v[70:71], v[210:211]\n"
  "v_cvt_pk_bf16_f32 v112, v64, v65\n"
  "v_cvt_pk_bf16_f32 v113, v66, v67\n"
  "v_cvt_pk_bf16_f32 v114, v68, v69\n"
  "v_cvt_pk_bf16_f32 v115, v70, v71\n"
  "buffer_store_dwordx4 v[112:115], v2, s[12:15], s35 offen sc1\n"
  "v_pk_add_f32 v[196:197], v[196:197], v[96:97]\n"
  "v_pk_add_f32 v[198:199], v[198:199], v[98:99]\n"
  "v_pk_add_f32 v[212:213], v[212:213], v[100:101]\n"
  "v_pk_add_f32 v[214:215], v[214:215], v[102:103]\n"
  "v_pk_mul_f32 v[64:65], v[196:197], v[196:197]\n"
  "v_pk_mul_f32 v[66:67], v[198:199], v[198:199]\n"
  "v_pk_mul_f32 v[68:69], v[212:213], v[212:213]\n"
  "v_pk_mul_f32 v[70:71], v[214:215], v[214:215]\n"
  "v_pk_fma_f32 v[64:65], v[64:65], s[84:85], v[24:25]\n"
  "v_pk_fma_f32 v[66:67], v[66:67], s[84:85], v[24:25]\n"
  "v_pk_fma_f32 v[68:69], v[68:69], s[84:85], v[24:25]\n"
  "v_pk_fma_f32 v[70:71], v[70:71], s[84:85], v[24:25]\n"
  "v_pk_mul_f32 v[64:65], v[64:65], v[196:197]\n"
  "v_pk_mul_f32 v[66:67], v[66:67], v[198:199]\n"
  "v_pk_mul_f32 v[68:69], v[68:69], v[212:213]\n"
  "v_pk_mul_f32 v[70:71], v[70:71], v[214:215]\n"
  "v_exp_f32 v64, v64\n"
  "v_exp_f32 v65, v65\n"
  "v_exp_f32 v66, v66\n"
  "v_exp_f32 v67, v67\n"
  "v_exp_f32 v68, v68\n"
  "v_exp_f32 v69, v69\n"
  "v_exp_f32 v70, v70\n"
  "v_exp_f32 v71, v71\n"
  "v_pk_add_f32 v[64:65], v[64:65], s[86:87]\n"
  "v_pk_add_f32 v[66:67], v[66:67], s[86:87]\n"
  "v_pk_add_f32 v[68:69], v[68:69], s[86:87]\n"
  "v_pk_add_f32 v[70:71], v[70:71], s[86:87]\n"
  "v_rcp_f32 v64, v64\n"
  "v_rcp_f32 v65, v65\n"
  "v_rcp_f32 v66, v66\n"
  "v_rcp_f32 v67, v67\n"
  "v_rcp_f32 v68, v68\n"
  "v_rcp_f32 v69, v69\n"
  "v_rcp_f32 v70, v70\n"
  "v_rcp_f32 v71, v71\n"
  "v_pk_mul_f32 v[64:65], v[64:65], v[196:197]\n"
  "v_pk_mul_f32 v[66:67], v[66:67], v[198:199]\n"
  "v_pk_mul_f32 v[68:69], v[68:69], v[212:213]\n"
  "v_pk_mul_f32 v[70:71], v[70:71], v[214:215]\n"
  "v_cvt_pk_bf16_f32 v116, v64, v65\n"
  "v_cvt_pk_bf16_f32 v117, v66, v67\n"
  "v_cvt_pk_bf16_f32 v118, v68, v69\n"
  "v_cvt_pk_bf16_f32 v119, v70, v71\n"
  "buffer_store_dwordx4 v[116:119], v2, s[12:15], s35 offen offset:256 sc1\n"
  "v_pk_add_f32 v[200:201], v[200:201], v[96:97]\n"
  "v_pk_add_f32 v[202:203], v[202:203], v[98:99]\n"
  "v_pk_add_f32 v[216:217], v[216:217], v[100:101]\n"
  "v_pk_add_f32 v[218:219], v[218:219], v[102:103]\n"
  "v_pk_mul_f32 v[64:65], v[200:201], v[200:201]\n"
  "v_pk_mul_f32 v[66:67], v[202:203], v[202:203]\n"
  "v_pk_mul_f32 v[68:69], v[216:217], v[216:217]\n"
  "v_pk_mul_f32 v[70:71], v[218:219], v[218:219]\n"
  "v_pk_fma_f32 v[64:65], v[64:65], s[84:85], v[24:25]\n"
  "v_pk_fma_f32 v[66:67], v[66:67], s[84:85], v[24:25]\n"
  "v_pk_fma_f32 v[68:69], v[68:69], s[84:85], v[24:25]\n"
  "v_pk_fma_f32 v[70:71], v[70:71], s[84:85], v[24:25]\n"
  "v_pk_mul_f32 v[64:65], v[64:65], v[200:201]\n"
  "v_pk_mul_f32 v[66:67], v[66:67], v[202:203]\n"
  "v_pk_mul_f32 v[68:69], v[68:69], v[216:217]\n"
  "v_pk_mul_f32 v[70:71], v[70:71], v[218:219]\n"
  "v_exp_f32 v64, v64\n"
  "v_exp_f32 v65, v65\n"
  "v_exp_f32 v66, v66\n"
  "v_exp_f32 v67, v67\n"
  "v_exp_f32 v68, v68\n"
  "v_exp_f32 v69, v69\n"
  "v_exp_f32 v70, v70\n"
  "v_exp_f32 v71, v71\n"
  "v_pk_add_f32 v[64:65], v[64:65], s[86:87]\n"
  "v_pk_add_f32 v[66:67], v[66:67], s[86:87]\n"
  "v_pk_add_f32 v[68:69], v[68:69], s[86:87]\n"
  "v_pk_add_f32 v[70:71], v[70:71], s[86:87]\n"
  "v_rcp_f32 v64, v64\n"
  "v_rcp_f32 v65, v65\n"
  "v_rcp_f32 v66, v66\n"
  "v_rcp_f32 v67, v67\n"
  "v_rcp_f32 v68, v68\n"
  "v_rcp_f32 v69, v69\n"
  "v_rcp_f32 v70, v70\n"
  "v_rcp_f32 v71, v71\n"
  "v_pk_mul_f32 v[64:65], v[64:65], v[200:201]\n"
  "v_pk_mul_f32 v[66:67], v[66:67], v[202:203]\n"
  "v_pk_mul_f32 v[68:69], v[68:69], v[216:217]\n"
  "v_pk_mul_f32 v[70:71], v[70:71], v[218:219]\n"
  "v_cvt_pk_bf16_f32 v120, v64, v65\n"
  "v_cvt_pk_bf16_f32 v121, v66, v67\n"
  "v_cvt_pk_bf16_f32 v122, v68, v69\n"
  "v_cvt_pk_bf16_f32 v123, v70, v71\n"
  "buffer_store_dwordx4 v[120:123], v2, s[12:15], s35 offen offset:512 sc1\n"
  "v_pk_add_f32 v[204:205], v[204:205], v[96:97]\n"
  "v_pk_add_f32 v[206:207], v[206:207], v[98:99]\n"
  "v_pk_add_f32 v[220:221], v[220:221], v[100:101]\n"
  "v_pk_add_f32 v[222:223], v[222:223], v[102:103]\n"
  "v_pk_mul_f32 v[64:65], v[204:205], v[204:205]\n"
  "v_pk_mul_f32 v[66:67], v[206:207], v[206:207]\n"
  "v_pk_mul_f32 v[68:69], v[220:221], v[220:221]\n"
  "v_pk_mul_f32 v[70:71], v[222:223], v[222:223]\n"
  "v_pk_fma_f32 v[64:65], v[64:65], s[84:85], v[24:25]\n"
  "v_pk_fma_f32 v[66:67], v[66:67], s[84:85], v[24:25]\n"
  "v_pk_fma_f32 v[68:69], v[68:69], s[84:85], v[24:25]\n"
  "v_pk_fma_f32 v[70:71], v[70:71], s[84:85], v[24:25]\n"
  "v_pk_mul_f32 v[64:65], v[64:65], v[204:205]\n"
  "v_pk_mul_f32 v[66:67], v[66:67], v[206:207]\n"
  "v_pk_mul_f32 v[68:69], v[68:69], v[220:221]\n"
  "v_pk_mul_f32 v[70:71], v[70:71], v[222:223]\n"
  "v_exp_f32 v64, v64\n"
  "v_exp_f32 v65, v65\n"
  "v_exp_f32 v66, v66\n"
  "v_exp_f32 v67, v67\n"
  "v_exp_f32 v68, v68\n"
  "v_exp_f32 v69, v69\n"
  "v_exp_f32 v70, v70\n"
  "v_exp_f32 v71, v71\n"
  "v_pk_add_f32 v[64:65], v[64:65], s[86:87]\n"
  "v_pk_add_f32 v[66:67], v[66:67], s[86:87]\n"
  "v_pk_add_f32 v[68:69], v[68:69], s[86:87]\n"
  "v_pk_add_f32 v[70:71], v[70:71], s[86:87]\n"
  "v_rcp_f32 v64, v64\n"
  "v_rcp_f32 v65, v65\n"
  "v_rcp_f32 v66, v66\n"
  "v_rcp_f32 v67, v67\n"
  "v_rcp_f32 v68, v68\n"
  "v_rcp_f32 v69, v69\n"
  "v_rcp_f32 v70, v70\n"
  "v_rcp_f32 v71, v71\n"
  "v_pk_mul_f32 v[64:65], v[64:65], v[204:205]\n"
  "v_pk_mul_f32 v[66:67], v[66:67], v[206:207]\n"
  "v_pk_mul_f32 v[68:69], v[68:69], v[220:221]\n"
  "v_pk_mul_f32 v[70:71], v[70:71], v[222:223]\n"
  "v_cvt_pk_bf16_f32 v124, v64, v65\n"
  "v_cvt_pk_bf16_f32 v125, v66, v67\n"
  "v_cvt_pk_bf16_f32 v126, v68, v69\n"
  "v_cvt_pk_bf16_f32 v127, v70, v71\n"
  "buffer_store_dwordx4 v[124:127], v2, s[12:15], s35 offen offset:768 sc1\n"
  "s_add_u32 s35, s35, 0x4000\n"
  "v_pk_add_f32 v[224:225], v[224:225], v[104:105]\n"
  "v_pk_add_f32 v[226:227], v[226:227], v[106:107]\n"
  "v_pk_add_f32 v[240:241], v[240:241], v[108:109]\n"
  "v_pk_add_f32 v[242:243], v[242:243], v[110:111]\n"
  "v_pk_mul_f32 v[64:65], v[224:225], v[224:225]\n"
  "v_pk_mul_f32 v[66:67], v[226:227], v[226:227]\n"
  "v_pk_mul_f32 v[68:69], v[240:241], v[240:241]\n"
  "v_pk_mul_f32 v[70:71], v[242:243], v[242:243]\n"
  "v_pk_fma_f32 v[64:65], v[64:65], s[84:85], v[24:25]\n"
  "v_pk_fma_f32 v[66:67], v[66:67], s[84:85], v[24:25]\n"
  "v_pk_fma_f32 v[68:69], v[68:69], s[84:85], v[24:25]\n"
  "v_pk_fma_f32 v[70:71], v[70:71], s[84:85], v[24:25]\n"
  "v_pk_mul_f32 v[64:65], v[64:65], v[224:225]\n"
  "v_pk_mul_f32 v[66:67], v[66:67], v[226:227]\n"
  "v_pk_mul_f32 v[68:69], v[68:69], v[240:241]\n"
  "v_pk_mul_f32 v[70:71], v[70:71], v[242:243]\n"
  "v_exp_f32 v64, v64\n"
  "v_exp_f32 v65, v65\n"
  "v_exp_f32 v66, v66\n"
  "v_exp_f32 v67, v67\n"
  "v_exp_f32 v68, v68\n"
  "v_exp_f32 v69, v69\n"
  "v_exp_f32 v70, v70\n"
  "v_exp_f32 v71, v71\n"
  "v_pk_add_f32 v[64:65], v[64:65], s[86:87]\n"
  "v_pk_add_f32 v[66:67], v[66:67], s[86:87]\n"
  "v_pk_add_f32 v[68:69], v[68:69], s[86:87]\n"
  "v_pk_add_f32 v[70:71], v[70:71], s[86:87]\n"
  "v_rcp_f32 v64, v64\n"
  "v_rcp_f32 v65, v65\n"
  "v_rcp_f32 v66, v66\n"
  "v_rcp_f32 v67, v67\n"
  "v_rcp_f32 v68, v68\n"
  "v_rcp_f32 v69, v69\n"
  "v_rcp_f32 v70, v70\n"
  "v_rcp_f32 v71, v71\n"
  "v_pk_mul_f32 v[64:65], v[64:65], v[224:225]\n"
  "v_pk_mul_f32 v[66:67], v[66:67], v[226:227]\n"
  "v_pk_mul_f32 v[68:69], v[68:69], v[240:241]\n"
  "v_pk_mul_f32 v[70:71], v[70:71], v[242:243]\n"
  "v_cvt_pk_bf16_f32 v112, v64, v65\n"
  "v_cvt_pk_bf16_f32 v113, v66, v67\n"
  "v_cvt_pk_bf16_f32 v114, v68, v69\n"
  "v_cvt_pk_bf16_f32 v115, v70, v71\n"
  "buffer_store_dwordx4 v[112:115], v2, s[12:15], s35 offen sc1\n"
  "v_pk_add_f32 v[228:229], v[228:229], v[104:105]\n"
  "v_pk_add_f32 v[230:231], v[230:231], v[106:107]\n"
  "v_pk_add_f32 v[244:245], v[244:245], v[108:109]\n"
  "v_pk_add_f32 v[246:247], v[246:247], v[110:111]\n"
  "v_pk_mul_f32 v[64:65], v[228:229], v[228:229]\n"
  "v_pk_mul_f32 v[66:67], v[230:231], v[230:231]\n"
  "v_pk_mul_f32 v[68:69], v[244:245], v[244:245]\n"
  "v_pk_mul_f32 v[70:71], v[246:247], v[246:247]\n"
  "v_pk_fma_f32 v[64:65], v[64:65], s[84:85], v[24:25]\n"
  "v_pk_fma_f32 v[66:67], v[66:67], s[84:85], v[24:25]\n"
  "v_pk_fma_f32 v[68:69], v[68:69], s[84:85], v[24:25]\n"
  "v_pk_fma_f32 v[70:71], v[70:71], s[84:85], v[24:25]\n"
  "v_pk_mul_f32 v[64:65], v[64:65], v[228:229]\n"
  "v_pk_mul_f32 v[66:67], v[66:67], v[230:231]\n"
  "v_pk_mul_f32 v[68:69], v[68:69], v[244:245]\n"
  "v_pk_mul_f32 v[70:71], v[70:71], v[246:247]\n"
  "v_exp_f32 v64, v64\n"
  "v_exp_f32 v65, v65\n"
  "v_exp_f32 v66, v66\n"
  "v_exp_f32 v67, v67\n"
  "v_exp_f32 v68, v68\n"
  "v_exp_f32 v69, v69\n"
  "v_exp_f32 v70, v70\n"
  "v_exp_f32 v71, v71\n"
  "v_pk_add_f32 v[64:65], v[64:65], s[86:87]\n"
  "v_pk_add_f32 v[66:67], v[66:67], s[86:87]\n"
  "v_pk_add_f32 v[68:69], v[68:69], s[86:87]\n"
  "v_pk_add_f32 v[70:71], v[70:71], s[86:87]\n"
  "v_rcp_f32 v64, v64\n"
  "v_rcp_f32 v65, v65\n"
  "v_rcp_f32 v66, v66\n"
  "v_rcp_f32 v67, v67\n"
  "v_rcp_f32 v68, v68\n"
  "v_rcp_f32 v69, v69\n"
  "v_rcp_f32 v70, v70\n"
  "v_rcp_f32 v71, v71\n"
  "v_pk_mul_f32 v[64:65], v[64:65], v[228:229]\n"
  "v_pk_mul_f32 v[66:67], v[66:67], v[230:231]\n"
  "v_pk_mul_f32 v[68:69], v[68:69], v[244:245]\n"
  "v_pk_mul_f32 v[70:71], v[70:71], v[246:247]\n"
  "v_cvt_pk_bf16_f32 v116, v64, v65\n"
  "v_cvt_pk_bf16_f32 v117, v66, v67\n"
  "v_cvt_pk_bf16_f32 v118, v68, v69\n"
  "v_cvt_pk_bf16_f32 v119, v70, v71\n"
  "buffer_store_dwordx4 v[116:119], v2, s[12:15], s35 offen offset:256 sc1\n"
  "v_pk_add_f32 v[232:233], v[232:233], v[104:105]\n"
  "v_pk_add_f32 v[234:235], v[234:235], v[106:107]\n"
  "v_pk_add_f32 v[248:249], v[248:249], v[108:109]\n"
  "v_pk_add_f32 v[250:251], v[250:251], v[110:111]\n"
  "v_pk_mul_f32 v[64:65], v[232:233], v[232:233]\n"
  "v_pk_mul_f32 v[66:67], v[234:235], v[234:235]\n"
  "v_pk_mul_f32 v[68:69], v[248:249], v[248:249]\n"
  "v_pk_mul_f32 v[70:71], v[250:251], v[250:251]\n"
  "v_pk_fma_f32 v[64:65], v[64:65], s[84:85], v[24:25]\n"
  "v_pk_fma_f32 v[66:67], v[66:67], s[84:85], v[24:25]\n"
  "v_pk_fma_f32 v[68:69], v[68:69], s[84:85], v[24:25]\n"
  "v_pk_fma_f32 v[70:71], v[70:71], s[84:85], v[24:25]\n"
  "v_pk_mul_f32 v[64:65], v[64:65], v[232:233]\n"
  "v_pk_mul_f32 v[66:67], v[66:67], v[234:235]\n"
  "v_pk_mul_f32 v[68:69], v[68:69], v[248:249]\n"
  "v_pk_mul_f32 v[70:71], v[70:71], v[250:251]\n"
  "v_exp_f32 v64, v64\n"
  "v_exp_f32 v65, v65\n"
  "v_exp_f32 v66, v66\n"
  "v_exp_f32 v67, v67\n"
  "v_exp_f32 v68, v68\n"
  "v_exp_f32 v69, v69\n"
  "v_exp_f32 v70, v70\n"
  "v_exp_f32 v71, v71\n"
  "v_pk_add_f32 v[64:65], v[64:65], s[86:87]\n"
  "v_pk_add_f32 v[66:67], v[66:67], s[86:87]\n"
  "v_pk_add_f32 v[68:69], v[68:69], s[86:87]\n"
  "v_pk_add_f32 v[70:71], v[70:71], s[86:87]\n"
  "v_rcp_f32 v64, v64\n"
  "v_rcp_f32 v65, v65\n"
  "v_rcp_f32 v66, v66\n"
  "v_rcp_f32 v67, v67\n"
  "v_rcp_f32 v68, v68\n"
  "v_rcp_f32 v69, v69\n"
  "v_rcp_f32 v70, v70\n"
  "v_rcp_f32 v71, v71\n"
  "v_pk_mul_f32 v[64:65], v[64:65], v[232:233]\n"
  "v_pk_mul_f32 v[66:67], v[66:67], v[234:235]\n"
  "v_pk_mul_f32 v[68:69], v[68:69], v[248:249]\n"
  "v_pk_mul_f32 v[70:71], v[70:71], v[250:251]\n"
  "v_cvt_pk_bf16_f32 v120, v64, v65\n"
  "v_cvt_pk_bf16_f32 v121, v66, v67\n"
  "v_cvt_pk_bf16_f32 v122, v68, v69\n"
  "v_cvt_pk_bf16_f32 v123, v70, v71\n"
  "buffer_store_dwordx4 v[120:123], v2, s[12:15], s35 offen offset:512 sc1\n"
  "v_pk_add_f32 v[236:237], v[236:237], v[104:105]\n"
  "v_pk_add_f32 v[238:239], v[238:239], v[106:107]\n"
  "v_pk_add_f32 v[252:253], v[252:253], v[108:109]\n"
  "v_pk_add_f32 v[254:255], v[254:255], v[110:111]\n"
  "v_pk_mul_f32 v[64:65], v[236:237], v[236:237]\n"
  "v_pk_mul_f32 v[66:67], v[238:239], v[238:239]\n"
  "v_pk_mul_f32 v[68:69], v[252:253], v[252:253]\n"
  "v_pk_mul_f32 v[70:71], v[254:255], v[254:255]\n"
  "v_pk_fma_f32 v[64:65], v[64:65], s[84:85], v[24:25]\n"
  "v_pk_fma_f32 v[66:67], v[66:67], s[84:85], v[24:25]\n"
  "v_pk_fma_f32 v[68:69], v[68:69], s[84:85], v[24:25]\n"
  "v_pk_fma_f32 v[70:71], v[70:71], s[84:85], v[24:25]\n"
  "v_pk_mul_f32 v[64:65], v[64:65], v[236:237]\n"
  "v_pk_mul_f32 v[66:67], v[66:67], v[238:239]\n"
  "v_pk_mul_f32 v[68:69], v[68:69], v[252:253]\n"
  "v_pk_mul_f32 v[70:71], v[70:71], v[254:255]\n"
  "v_exp_f32 v64, v64\n"
  "v_exp_f32 v65, v65\n"
  "v_exp_f32 v66, v66\n"
  "v_exp_f32 v67, v67\n"
  "v_exp_f32 v68, v68\n"
  "v_exp_f32 v69, v69\n"
  "v_exp_f32 v70, v70\n"
  "v_exp_f32 v71, v71\n"
  "v_pk_add_f32 v[64:65], v[64:65], s[86:87]\n"
  "v_pk_add_f32 v[66:67], v[66:67], s[86:87]\n"
  "v_pk_add_f32 v[68:69], v[68:69], s[86:87]\n"
  "v_pk_add_f32 v[70:71], v[70:71], s[86:87]\n"
  "v_rcp_f32 v64, v64\n"
  "v_rcp_f32 v65, v65\n"
  "v_rcp_f32 v66, v66\n"
  "v_rcp_f32 v67, v67\n"
  "v_rcp_f32 v68, v68\n"
  "v_rcp_f32 v69, v69\n"
  "v_rcp_f32 v70, v70\n"
  "v_rcp_f32 v71, v71\n"
  "v_pk_mul_f32 v[64:65], v[64:65], v[236:237]\n"
  "v_pk_mul_f32 v[66:67], v[66:67], v[238:239]\n"
  "v_pk_mul_f32 v[68:69], v[68:69], v[252:253]\n"
  "v_pk_mul_f32 v[70:71], v[70:71], v[254:255]\n"
  "v_cvt_pk_bf16_f32 v124, v64, v65\n"
  "v_cvt_pk_bf16_f32 v125, v66, v67\n"
  "v_cvt_pk_bf16_f32 v126, v68, v69\n"
  "v_cvt_pk_bf16_f32 v127, v70, v71\n"
  "buffer_store_dwordx4 v[124:127], v2, s[12:15], s35 offen offset:768 sc1\n"
  "s_add_u32 s35, s35, 0x4000\n"
  "s_waitcnt vmcnt(0)\n"
  "s_barrier\n"
  "s_cmp_lg_u32 s25, 0\n"
  "s_cbranch_scc1 PUBSKIP_13_%=\n"
  "v_mov_b32 v56, s46\n"
  "v_mov_b32 v57, s90\n"
  "s_mov_b64 exec, 1\n"
  "global_store_dword v56, v57, s[80:81] sc1\n"
  "s_mov_b64 exec, -1\n"
  "PUBSKIP_13_%=:\n"
  "s_waitcnt lgkmcnt(15)\n"
  "ds_read_b64_tr_b16 a[0:1], v7\n"
  "s_waitcnt lgkmcnt(15)\n"
  "ds_read_b64_tr_b16 a[2:3], v7 offset:4096\n"
  "s_waitcnt lgkmcnt(15)\n"
  "ds_read_b64_tr_b16 a[4:5], v8\n"
  "s_waitcnt lgkmcnt(15)\n"
  "ds_read_b64_tr_b16 a[6:7], v8 offset:4096\n"
  "s_waitcnt lgkmcnt(15)\n"
  "ds_read_b64_tr_b16 a[8:9], v9\n"
  "s_waitcnt lgkmcnt(15)\n"
  "ds_read_b64_tr_b16 a[10:11], v9 offset:4096\n"
  "s_waitcnt lgkmcnt(15)\n"
  "ds_read_b64_tr_b16 a[12:13], v10\n"
  "s_waitcnt lgkmcnt(15)\n"
  "ds_read_b64_tr_b16 a[14:15], v10 offset:4096\n"
  "s_waitcnt lgkmcnt(15)\n"
  "ds_read_b64_tr_b16 a[16:17], v11\n"
  "s_waitcnt lgkmcnt(15)\n"
  "ds_read_b64_tr_b16 a[18:19], v11 offset:4096\n"
  "s_waitcnt lgkmcnt(15)\n"
  "ds_read_b64_tr_b16 a[20:21], v12\n"
  "s_waitcnt lgkmcnt(15)\n"
  "ds_read_b64_tr_b16 a[22:23], v12 offset:4096\n"
  "s_waitcnt lgkmcnt(15)\n"
  "ds_read_b64_tr_b16 a[24:25], v13\n"
  "s_waitcnt lgkmcnt(15)\n"
  "ds_read_b64_tr_b16 a[26:27], v13 offset:4096\n"
  "s_waitcnt lgkmcnt(15)\n"
  "ds_read_b64_tr_b16 a[28:29], v14\n"
  "s_waitcnt lgkmcnt(15)\n"
  "ds_read_b64_tr_b16 a[30:31], v14 offset:4096\n"
  "s_lshl_b32 s29, s89, 7\n"
  "s_and_b32 s30, s42, 1\n"
  "s_mul_i32 s30, s30, 56\n"
  "s_add_u32 s29, s29, s30\n"
  "s_add_u32 s29, s29, 0x2400\n"
  "v_min_u32 v56, 6, v1\n"
  "v_lshlrev_b32 v56, 2, v56\n"
  "v_add_u32 v56, s29, v56\n"
  "s_mov_b32 s63, 0x30d40\n"
  "POLL_14_%=:\n"
  "global_load_dword v57, v56, s[80:81] sc1\n"
  "s_waitcnt vmcnt(0)\n"
  "v_cmp_ne_u32 vcc, s90, v57\n"
  "s_nop 1\n"
  "s_cmp_eq_u64 vcc, 0\n"
  "s_cbranch_scc1 POLLOK_15_%=\n"
  "s_sleep 1\n"
  "s_sub_u32 s63, s63, 1\n"
  "s_cmp_lg_u32 s63, 0\n"
  "s_cbranch_scc1 POLL_14_%=\n"
  "POLLOK_15_%=:\n"
  "buffer_load_dwordx4 a[64:67], v2, s[4:7], s21 offen sc1\n"
  "buffer_load_dwordx4 a[68:71], v2, s[4:7], s21 offen offset:256 sc1\n"
  "buffer_load_dwordx4 a[72:75], v2, s[4:7], s21 offen offset:512 sc1\n"
  "buffer_load_dwordx4 a[76:79], v2, s[4:7], s21 offen offset:768 sc1\n"
  "s_add_u32 s21, s21, 0x4000\n"
  "buffer_load_dwordx4 a[80:83], v2, s[4:7], s21 offen sc1\n"
  "buffer_load_dwordx4 a[84:87], v2, s[4:7], s21 offen offset:256 sc1\n"
  "buffer_load_dwordx4 a[88:91], v2, s[4:7], s21 offen offset:512 sc1\n"
  "buffer_load_dwordx4 a[92:95], v2, s[4:7], s21 offen offset:768 sc1\n"
  "s_add_u32 s21, s21, 0x4000\n"
  "buffer_load_dwordx4 a[96:99], v2, s[4:7], s21 offen sc1\n"
  "buffer_load_dwordx4 a[100:103], v2, s[4:7], s21 offen offset:256 sc1\n"
  "buffer_load_dwordx4 a[104:107], v2, s[4:7], s21 offen offset:512 sc1\n"
  "buffer_load_dwordx4 a[108:111], v2, s[4:7], s21 offen offset:768 sc1\n"
  "s_add_u32 s21, s21, 0x4000\n"
  "buffer_load_dwordx4 a[112:115], v2, s[4:7], s21 offen sc1\n"
  "buffer_load_dwordx4 a[116:119], v2, s[4:7], s21 offen offset:256 sc1\n"
  "buffer_load_dwordx4 a[120:123], v2, s[4:7], s21 offen offset:512 sc1\n"
  "buffer_load_dwordx4 a[124:127], v2, s[4:7], s21 offen offset:768 sc1\n"
  "s_add_u32 s21, s21, 0x4000\n"
  "buffer_load_dwordx4 a[128:131], v2, s[4:7], s21 offen sc1\n"
  "buffer_load_dwordx4 a[132:135], v2, s[4:7], s21 offen offset:256 sc1\n"
  "buffer_load_dwordx4 a[136:139], v2, s[4:7], s21 offen offset:512 sc1\n"
  "buffer_load_dwordx4 a[140:143], v2, s[4:7], s21 offen offset:768 sc1\n"
  "s_add_u32 s21, s21, 0x4000\n"
  "buffer_load_dwordx4 a[144:147], v2, s[4:7], s21 offen sc1\n"
  "buffer_load_dwordx4 a[148:151], v2, s[4:7], s21 offen offset:256 sc1\n"
  "buffer_load_dwordx4 a[152:155], v2, s[4:7], s21 offen offset:512 sc1\n"
  "buffer_load_dwordx4 a[156:159], v2, s[4:7], s21 offen offset:768 sc1\n"
  "s_add_u32 s21, s21, 0x4000\n"
  "buffer_load_dwordx4 a[160:163], v2, s[4:7], s21 offen sc1\n"
  "buffer_load_dwordx4 a[164:167], v2, s[4:7], s21 offen offset:256 sc1\n"
  "buffer_load_dwordx4 a[168:171], v2, s[4:7], s21 offen offset:512 sc1\n"
  "buffer_load_dwordx4 a[172:175], v2, s[4:7], s21 offen offset:768 sc1\n"
  "s_add_u32 s21, s21, 0x4000\n"
  "s_mov_b32 s88, -1\n"
  "s_mov_b32 s14, 0\n"
  "s_waitcnt vmcnt(24)\n"
  "s_branch P3_ENTER_%=\n"
  "F1_SKIP_%=:\n"
  "s_lshl_b32 s29, s38, 21\n"
  "s_add_u32 s12, s80, 0x600000\n"
  "s_addc_u32 s13, s81, 0\n"
  "s_add_u32 s12, s12, s29\n"
  "s_addc_u32 s13, s13, 0\n"
  "s_and_b32 s13, s13, 0xffff\n"
  "s_mov_b32 s14, 0x200000\n"
  "s_mov_b32 s15, 0x20000\n"
  "s_lshl_b32 s91, s39, 16\n"
  "s_mov_b32 s88, -1\n"
  "v_rcp_f32 v26, v240\n"
  "v_rcp_f32 v27, v244\n"
  "v_rcp_f32 v28, v248\n"
  "v_rcp_f32 v29, v252\n"
  "v_accvgpr_read_b32 v136, a128\n"
  "v_accvgpr_read_b32 v137, a129\n"
  "v_accvgpr_read_b32 v138, a130\n"
  "v_accvgpr_read_b32 v139, a131\n"
  "v_accvgpr_read_b32 v140, a144\n"
  "v_accvgpr_read_b32 v141, a145\n"
  "v_accvgpr_read_b32 v142, a146\n"
  "v_accvgpr_read_b32 v143, a147\n"
  "v_mul_f32 v136, v136, v26\n"
  "v_mul_f32 v137, v137, v26\n"
  "v_mul_f32 v138, v138, v26\n"
  "v_mul_f32 v139, v139, v26\n"
  "v_mul_f32 v140, v140, v26\n"
  "v_mul_f32 v141, v141, v26\n"
  "v_mul_f32 v142, v142, v26\n"
  "v_mul_f32 v143, v143, v26\n"
  "v_cvt_pk_bf16_f32 v64, v136, v137\n"
  "v_cvt_pk_bf16_f32 v65, v138, v139\n"
  "v_cvt_pk_bf16_f32 v66, v140, v141\n"
  "v_cvt_pk_bf16_f32 v67, v142, v143\n"
  "v_accvgpr_read_b32 v144, a132\n"
  "v_accvgpr_read_b32 v145, a133\n"
  "v_accvgpr_read_b32 v146, a134\n"
  "v_accvgpr_read_b32 v147, a135\n"
  "v_accvgpr_read_b32 v148, a148\n"
  "v_accvgpr_read_b32 v149, a149\n"
  "v_accvgpr_read_b32 v150, a150\n"
  "v_accvgpr_read_b32 v151, a151\n"
  "v_mul_f32 v144, v144, v27\n"
  "v_mul_f32 v145, v145, v27\n"
  "v_mul_f32 v146, v146, v27\n"
  "v_mul_f32 v147, v147, v27\n"
  "v_mul_f32 v148, v148, v27\n"
  "v_mul_f32 v149, v149, v27\n"
  "v_mul_f32 v150, v150, v27\n"
  "v_mul_f32 v151, v151, v27\n"
  "v_cvt_pk_bf16_f32 v68, v144, v145\n"
  "v_cvt_pk_bf16_f32 v69, v146, v147\n"
  "v_cvt_pk_bf16_f32 v70, v148, v149\n"
  "v_cvt_pk_bf16_f32 v71, v150, v151\n"
  "v_accvgpr_read_b32 v152, a136\n"
  "v_accvgpr_read_b32 v153, a137\n"
  "v_accvgpr_read_b32 v154, a138\n"
  "v_accvgpr_read_b32 v155, a139\n"
  "v_accvgpr_read_b32 v156, a152\n"
  "v_accvgpr_read_b32 v157, a153\n"
  "v_accvgpr_read_b32 v158, a154\n"
  "v_accvgpr_read_b32 v159, a155\n"
  "v_mul_f32 v152, v152, v28\n"
  "v_mul_f32 v153, v153, v28\n"
  "v_mul_f32 v154, v154, v28\n"
  "v_mul_f32 v155, v155, v28\n"
  "v_mul_f32 v156, v156, v28\n"
  "v_mul_f32 v157, v157, v28\n"
  "v_mul_f32 v158, v158, v28\n"
  "v_mul_f32 v159, v159, v28\n"
  "v_cvt_pk_bf16_f32 v72, v152, v153\n"
  "v_cvt_pk_bf16_f32 v73, v154, v155\n"
  "v_cvt_pk_bf16_f32 v74, v156, v157\n"
  "v_cvt_pk_bf16_f32 v75, v158, v159\n"
  "v_accvgpr_read_b32 v128, a140\n"
  "v_accvgpr_read_b32 v129, a141\n"
  "v_accvgpr_read_b32 v130, a142\n"
  "v_accvgpr_read_b32 v131, a143\n"
  "v_accvgpr_read_b32 v132, a156\n"
  "v_accvgpr_read_b32 v133, a157\n"
  "v_accvgpr_read_b32 v134, a158\n"
  "v_accvgpr_read_b32 v135, a159\n"
  "v_mul_f32 v128, v128, v29\n"
  "v_mul_f32 v129, v129, v29\n"
  "v_mul_f32 v130, v130, v29\n"
  "v_mul_f32 v131, v131, v29\n"
  "v_mul_f32 v132, v132, v29\n"
  "v_mul_f32 v133, v133, v29\n"
  "v_mul_f32 v134, v134, v29\n"
  "v_mul_f32 v135, v135, v29\n"
  "v_cvt_pk_bf16_f32 v76, v128, v129\n"
  "v_cvt_pk_bf16_f32 v77, v130, v131\n"
  "v_cvt_pk_bf16_f32 v78, v132, v133\n"
  "v_cvt_pk_bf16_f32 v79, v134, v135\n"
  "v_accvgpr_read_b32 v136, a160\n"
  "v_accvgpr_read_b32 v137, a161\n"
  "v_accvgpr_read_b32 v138, a162\n"
  "v_accvgpr_read_b32 v139, a163\n"
  "v_accvgpr_read_b32 v140, a176\n"
  "v_accvgpr_read_b32 v141, a177\n"
  "v_accvgpr_read_b32 v142, a178\n"
  "v_accvgpr_read_b32 v143, a179\n"
  "v_mul_f32 v136, v136, v26\n"
  "v_mul_f32 v137, v137, v26\n"
  "v_mul_f32 v138, v138, v26\n"
  "v_mul_f32 v139, v139, v26\n"
  "v_mul_f32 v140, v140, v26\n"
  "v_mul_f32 v141, v141, v26\n"
  "v_mul_f32 v142, v142, v26\n"
  "v_mul_f32 v143, v143, v26\n"
  "v_cvt_pk_bf16_f32 v80, v136, v137\n"
  "v_cvt_pk_bf16_f32 v81, v138, v139\n"
  "v_cvt_pk_bf16_f32 v82, v140, v141\n"
  "v_cvt_pk_bf16_f32 v83, v142, v143\n"
  "v_accvgpr_read_b32 v144, a164\n"
  "v_accvgpr_read_b32 v145, a165\n"
  "v_accvgpr_read_b32 v146, a166\n"
  "v_accvgpr_read_b32 v147, a167\n"
  "v_accvgpr_read_b32 v148, a180\n"
  "v_accvgpr_read_b32 v149, a181\n"
  "v_accvgpr_read_b32 v150, a182\n"
  "v_accvgpr_read_b32 v151, a183\n"
  "v_mul_f32 v144, v144, v27\n"
  "v_mul_f32 v145, v145, v27\n"
  "v_mul_f32 v146, v146, v27\n"
  "v_mul_f32 v147, v147, v27\n"
  "v_mul_f32 v148, v148, v27\n"
  "v_mul_f32 v149, v149, v27\n"
  "v_mul_f32 v150, v150, v27\n"
  "v_mul_f32 v151, v151, v27\n"
  "v_cvt_pk_bf16_f32 v84, v144, v145\n"
  "v_cvt_pk_bf16_f32 v85, v146, v147\n"
  "v_cvt_pk_bf16_f32 v86, v148, v149\n"
  "v_cvt_pk_bf16_f32 v87, v150, v151\n"
  "v_accvgpr_read_b32 v152, a168\n"
  "v_accvgpr_read_b32 v153, a169\n"
  "v_accvgpr_read_b32 v154, a170\n"
  "v_accvgpr_read_b32 v155, a171\n"
  "v_accvgpr_read_b32 v156, a184\n"
  "v_accvgpr_read_b32 v157, a185\n"
  "v_accvgpr_read_b32 v158, a186\n"
  "v_accvgpr_read_b32 v159, a187\n"
  "v_mul_f32 v152, v152, v28\n"
  "v_mul_f32 v153, v153, v28\n"
  "v_mul_f32 v154, v154, v28\n"
  "v_mul_f32 v155, v155, v28\n"
  "v_mul_f32 v156, v156, v28\n"
  "v_mul_f32 v157, v157, v28\n"
  "v_mul_f32 v158, v158, v28\n"
  "v_mul_f32 v159, v159, v28\n"
  "v_cvt_pk_bf16_f32 v88, v152, v153\n"
  "v_cvt_pk_bf16_f32 v89, v154, v155\n"
  "v_cvt_pk_bf16_f32 v90, v156, v157\n"
  "v_cvt_pk_bf16_f32 v91, v158, v159\n"
  "v_accvgpr_read_b32 v128, a172\n"
  "v_accvgpr_read_b32 v129, a173\n"
  "v_accvgpr_read_b32 v130, a174\n"
  "v_accvgpr_read_b32 v131, a175\n"
  "v_accvgpr_read_b32 v132, a188\n"
  "v_accvgpr_read_b32 v133, a189\n"
  "v_accvgpr_read_b32 v134, a190\n"
  "v_accvgpr_read_b32 v135, a191\n"
  "v_mul_f32 v128, v128, v29\n"
  "v_mul_f32 v129, v129, v29\n"
  "v_mul_f32 v130, v130, v29\n"
  "v_mul_f32 v131, v131, v29\n"
  "v_mul_f32 v132, v132, v29\n"
  "v_mul_f32 v133, v133, v29\n"
  "v_mul_f32 v134, v134, v29\n"
  "v_mul_f32 v135, v135, v29\n"
  "v_cvt_pk_bf16_f32 v92, v128, v129\n"
  "v_cvt_pk_bf16_f32 v93, v130, v131\n"
  "v_cvt_pk_bf16_f32 v94, v132, v133\n"
  "v_cvt_pk_bf16_f32 v95, v134, v135\n"
  "v_accvgpr_read_b32 v136, a192\n"
  "v_accvgpr_read_b32 v137, a193\n"
  "v_accvgpr_read_b32 v138, a194\n"
  "v_accvgpr_read_b32 v139, a195\n"
  "v_accvgpr_read_b32 v140, a208\n"
  "v_accvgpr_read_b32 v141, a209\n"
  "v_accvgpr_read_b32 v142, a210\n"
  "v_accvgpr_read_b32 v143, a211\n"
  "v_mul_f32 v136, v136, v26\n"
  "v_mul_f32 v137, v137, v26\n"
  "v_mul_f32 v138, v138, v26\n"
  "v_mul_f32 v139, v139, v26\n"
  "v_mul_f32 v140, v140, v26\n"
  "v_mul_f32 v141, v141, v26\n"
  "v_mul_f32 v142, v142, v26\n"
  "v_mul_f32 v143, v143, v26\n"
  "v_cvt_pk_bf16_f32 v96, v136, v137\n"
  "v_cvt_pk_bf16_f32 v97, v138, v139\n"
  "v_cvt_pk_bf16_f32 v98, v140, v141\n"
  "v_cvt_pk_bf16_f32 v99, v142, v143\n"
  "v_accvgpr_read_b32 v144, a196\n"
  "v_accvgpr_read_b32 v145, a197\n"
  "v_accvgpr_read_b32 v146, a198\n"
  "v_accvgpr_read_b32 v147, a199\n"
  "v_accvgpr_read_b32 v148, a212\n"
  "v_accvgpr_read_b32 v149, a213\n"
  "v_accvgpr_read_b32 v150, a214\n"
  "v_accvgpr_read_b32 v151, a215\n"
  "v_mul_f32 v144, v144, v27\n"
  "v_mul_f32 v145, v145, v27\n"
  "v_mul_f32 v146, v146, v27\n"
  "v_mul_f32 v147, v147, v27\n"
  "v_mul_f32 v148, v148, v27\n"
  "v_mul_f32 v149, v149, v27\n"
  "v_mul_f32 v150, v150, v27\n"
  "v_mul_f32 v151, v151, v27\n"
  "v_cvt_pk_bf16_f32 v100, v144, v145\n"
  "v_cvt_pk_bf16_f32 v101, v146, v147\n"
  "v_cvt_pk_bf16_f32 v102, v148, v149\n"
  "v_cvt_pk_bf16_f32 v103, v150, v151\n"
  "v_accvgpr_read_b32 v152, a200\n"
  "v_accvgpr_read_b32 v153, a201\n"
  "v_accvgpr_read_b32 v154, a202\n"
  "v_accvgpr_read_b32 v155, a203\n"
  "v_accvgpr_read_b32 v156, a216\n"
  "v_accvgpr_read_b32 v157, a217\n"
  "v_accvgpr_read_b32 v158, a218\n"
  "v_accvgpr_read_b32 v159, a219\n"
  "v_mul_f32 v152, v152, v28\n"
  "v_mul_f32 v153, v153, v28\n"
  "v_mul_f32 v154, v154, v28\n"
  "v_mul_f32 v155, v155, v28\n"
  "v_mul_f32 v156, v156, v28\n"
  "v_mul_f32 v157, v157, v28\n"
  "v_mul_f32 v158, v158, v28\n"
  "v_mul_f32 v159, v159, v28\n"
  "v_cvt_pk_bf16_f32 v104, v152, v153\n"
  "v_cvt_pk_bf16_f32 v105, v154, v155\n"
  "v_cvt_pk_bf16_f32 v106, v156, v157\n"
  "v_cvt_pk_bf16_f32 v107, v158, v159\n"
  "v_accvgpr_read_b32 v128, a204\n"
  "v_accvgpr_read_b32 v129, a205\n"
  "v_accvgpr_read_b32 v130, a206\n"
  "v_accvgpr_read_b32 v131, a207\n"
  "v_accvgpr_read_b32 v132, a220\n"
  "v_accvgpr_read_b32 v133, a221\n"
  "v_accvgpr_read_b32 v134, a222\n"
  "v_accvgpr_read_b32 v135, a223\n"
  "v_mul_f32 v128, v128, v29\n"
  "v_mul_f32 v129, v129, v29\n"
  "v_mul_f32 v130, v130, v29\n"
  "v_mul_f32 v131, v131, v29\n"
  "v_mul_f32 v132, v132, v29\n"
  "v_mul_f32 v133, v133, v29\n"
  "v_mul_f32 v134, v134, v29\n"
  "v_mul_f32 v135, v135, v29\n"
  "v_cvt_pk_bf16_f32 v108, v128, v129\n"
  "v_cvt_pk_bf16_f32 v109, v130, v131\n"
  "v_cvt_pk_bf16_f32 v110, v132, v133\n"
  "v_cvt_pk_bf16_f32 v111, v134, v135\n"
  "v_accvgpr_read_b32 v136, a224\n"
  "v_accvgpr_read_b32 v137, a225\n"
  "v_accvgpr_read_b32 v138, a226\n"
  "v_accvgpr_read_b32 v139, a227\n"
  "v_accvgpr_read_b32 v140, a240\n"
  "v_accvgpr_read_b32 v141, a241\n"
  "v_accvgpr_read_b32 v142, a242\n"
  "v_accvgpr_read_b32 v143, a243\n"
  "v_mul_f32 v136, v136, v26\n"
  "v_mul_f32 v137, v137, v26\n"
  "v_mul_f32 v138, v138, v26\n"
  "v_mul_f32 v139, v139, v26\n"
  "v_mul_f32 v140, v140, v26\n"
  "v_mul_f32 v141, v141, v26\n"
  "v_mul_f32 v142, v142, v26\n"
  "v_mul_f32 v143, v143, v26\n"
  "v_cvt_pk_bf16_f32 v112, v136, v137\n"
  "v_cvt_pk_bf16_f32 v113, v138, v139\n"
  "v_cvt_pk_bf16_f32 v114, v140, v141\n"
  "v_cvt_pk_bf16_f32 v115, v142, v143\n"
  "v_accvgpr_read_b32 v144, a228\n"
  "v_accvgpr_read_b32 v145, a229\n"
  "v_accvgpr_read_b32 v146, a230\n"
  "v_accvgpr_read_b32 v147, a231\n"
  "v_accvgpr_read_b32 v148, a244\n"
  "v_accvgpr_read_b32 v149, a245\n"
  "v_accvgpr_read_b32 v150, a246\n"
  "v_accvgpr_read_b32 v151, a247\n"
  "v_mul_f32 v144, v144, v27\n"
  "v_mul_f32 v145, v145, v27\n"
  "v_mul_f32 v146, v146, v27\n"
  "v_mul_f32 v147, v147, v27\n"
  "v_mul_f32 v148, v148, v27\n"
  "v_mul_f32 v149, v149, v27\n"
  "v_mul_f32 v150, v150, v27\n"
  "v_mul_f32 v151, v151, v27\n"
  "v_cvt_pk_bf16_f32 v116, v144, v145\n"
  "v_cvt_pk_bf16_f32 v117, v146, v147\n"
  "v_cvt_pk_bf16_f32 v118, v148, v149\n"
  "v_cvt_pk_bf16_f32 v119, v150, v151\n"
  "v_accvgpr_read_b32 v152, a232\n"
  "v_accvgpr_read_b32 v153, a233\n"
  "v_accvgpr_read_b32 v154, a234\n"
  "v_accvgpr_read_b32 v155, a235\n"
  "v_accvgpr_read_b32 v156, a248\n"
  "v_accvgpr_read_b32 v157, a249\n"
  "v_accvgpr_read_b32 v158, a250\n"
  "v_accvgpr_read_b32 v159, a251\n"
  "v_mul_f32 v152, v152, v28\n"
  "v_mul_f32 v153, v153, v28\n"
  "v_mul_f32 v154, v154, v28\n"
  "v_mul_f32 v155, v155, v28\n"
  "v_mul_f32 v156, v156, v28\n"
  "v_mul_f32 v157, v157, v28\n"
  "v_mul_f32 v158, v158, v28\n"
  "v_mul_f32 v159, v159, v28\n"
  "v_cvt_pk_bf16_f32 v120, v152, v153\n"
  "v_cvt_pk_bf16_f32 v121, v154, v155\n"
  "v_cvt_pk_bf16_f32 v122, v156, v157\n"
  "v_cvt_pk_bf16_f32 v123, v158, v159\n"
  "v_accvgpr_read_b32 v128, a236\n"
  "v_accvgpr_read_b32 v129, a237\n"
  "v_accvgpr_read_b32 v130, a238\n"
  "v_accvgpr_read_b32 v131, a239\n"
  "v_accvgpr_read_b32 v132, a252\n"
  "v_accvgpr_read_b32 v133, a253\n"
  "v_accvgpr_read_b32 v134, a254\n"
  "v_accvgpr_read_b32 v135, a255\n"
  "v_mul_f32 v128, v128, v29\n"
  "v_mul_f32 v129, v129, v29\n"
  "v_mul_f32 v130, v130, v29\n"
  "v_mul_f32 v131, v131, v29\n"
  "v_mul_f32 v132, v132, v29\n"
  "v_mul_f32 v133, v133, v29\n"
  "v_mul_f32 v134, v134, v29\n"
  "v_mul_f32 v135, v135, v29\n"
  "v_cvt_pk_bf16_f32 v124, v128, v129\n"
  "v_cvt_pk_bf16_f32 v125, v130, v131\n"
  "v_cvt_pk_bf16_f32 v126, v132, v133\n"
  "v_cvt_pk_bf16_f32 v127, v134, v135\n"
  "buffer_store_dwordx4 v[64:67], v2, s[12:15], s91 offen sc1\n"
  "buffer_store_dwordx4 v[68:71], v2, s[12:15], s91 offen offset:256 sc1\n"
  "buffer_store_dwordx4 v[72:75], v2, s[12:15], s91 offen offset:512 sc1\n"
  "buffer_store_dwordx4 v[76:79], v2, s[12:15], s91 offen offset:768 sc1\n"
  "s_add_u32 s91, s91, 0x4000\n"
  "buffer_store_dwordx4 v[80:83], v2, s[12:15], s91 offen sc1\n"
  "buffer_store_dwordx4 v[84:87], v2, s[12:15], s91 offen offset:256 sc1\n"
  "buffer_store_dwordx4 v[88:91], v2, s[12:15], s91 offen offset:512 sc1\n"
  "buffer_store_dwordx4 v[92:95], v2, s[12:15], s91 offen offset:768 sc1\n"
  "s_add_u32 s91, s91, 0x4000\n"
  "buffer_store_dwordx4 v[96:99], v2, s[12:15], s91 offen sc1\n"
  "buffer_store_dwordx4 v[100:103], v2, s[12:15], s91 offen offset:256 sc1\n"
  "buffer_store_dwordx4 v[104:107], v2, s[12:15], s91 offen offset:512 sc1\n"
  "buffer_store_dwordx4 v[108:111], v2, s[12:15], s91 offen offset:768 sc1\n"
  "s_add_u32 s91, s91, 0x4000\n"
  "buffer_store_dwordx4 v[112:115], v2, s[12:15], s91 offen sc1\n"
  "buffer_store_dwordx4 v[116:119], v2, s[12:15], s91 offen offset:256 sc1\n"
  "buffer_store_dwordx4 v[120:123], v2, s[12:15], s91 offen offset:512 sc1\n"
  "buffer_store_dwordx4 v[124:127], v2, s[12:15], s91 offen offset:768 sc1\n"
  "s_add_u32 s91, s91, 0x4000\n"
  "s_lshl_b32 s29, s38, 7\n"
  "s_lshl_b32 s30, s39, 2\n"
  "s_add_u32 s29, s29, s30\n"
  "s_add_u32 s29, s29, 0x2000\n"
  "s_waitcnt vmcnt(0)\n"
  "s_barrier\n"
  "s_cmp_lg_u32 s25, 0\n"
  "s_cbranch_scc1 PUBSKIP_16_%=\n"
  "v_mov_b32 v56, s29\n"
  "v_mov_b32 v57, s90\n"
  "s_mov_b64 exec, 1\n"
  "global_store_dword v56, v57, s[80:81] sc1\n"
  "s_mov_b64 exec, -1\n"
  "PUBSKIP_16_%=:\n"
  "s_cmp_ge_u32 s42, 2\n"
  "s_cbranch_scc1 P3G_17_%=\n"
  "s_lshl_b32 s31, s42, 5\n"
  "s_lshl_b32 s29, s89, 21\n"
  "s_lshl_b32 s30, s31, 15\n"
  "s_add_u32 s29, s29, s30\n"
  "s_lshl_b32 s30, s31, 17\n"
  "s_lshl_b32 s34, s43, 8\n"
  "s_add_u32 s30, s30, s34\n"
  "s_add_u32 s4, s80, 0x600000\n"
  "s_addc_u32 s5, s81, 0\n"
  "s_add_u32 s4, s4, s29\n"
  "s_addc_u32 s5, s5, 0\n"
  "s_and_b32 s5, s5, 0xffff\n"
  "s_mov_b32 s6, 0x100000\n"
  "s_mov_b32 s7, 0x20000\n"
  "s_add_u32 s8, s68, s30\n"
  "s_addc_u32 s9, s69, 0\n"
  "s_and_b32 s9, s9, 0xffff\n"
  "s_mov_b32 s10, 0x3ff900\n"
  "s_mov_b32 s11, 0x20000\n"
  "s_branch P3GO_18_%=\n"
  "P3G_17_%=:\n"
  "s_and_b32 s31, s42, 1\n"
  "s_mul_i32 s31, s31, 28\n"
  "s_mul_i32 s29, s89, 0x1c0000\n"
  "s_lshl_b32 s30, s31, 15\n"
  "s_add_u32 s29, s29, s30\n"
  "s_lshl_b32 s30, s31, 17\n"
  "s_lshl_b32 s34, s43, 8\n"
  "s_add_u32 s30, s30, s34\n"
  "s_add_u32 s4, s80, 0x1600000\n"
  "s_addc_u32 s5, s81, 0\n"
  "s_add_u32 s4, s4, s29\n"
  "s_addc_u32 s5, s5, 0\n"
  "s_and_b32 s5, s5, 0xffff\n"
  "s_mov_b32 s6, 0xe0000\n"
  "s_mov_b32 s7, 0x20000\n"
  "s_add_u32 s8, s74, s30\n"
  "s_addc_u32 s9, s75, 0\n"
  "s_and_b32 s9, s9, 0xffff\n"
  "s_mov_b32 s10, 0x37f900\n"
  "s_mov_b32 s11, 0x20000\n"
  "P3GO_18_%=:\n"
  "s_mov_b32 s21, 0\n"
  "s_mul_i32 s22, s25, 0x8000\n"
  "s_mov_b32 s23, 0x20000\n"
  "v_lshrrev_b32 v18, 4, v1\n"
  "v_bfe_u32 v19, v1, 1, 3\n"
  "v_and_b32 v20, 1, v1\n"
  "v_lshlrev_b32 v20, 4, v20\n"
  "s_mov_b32 s36, 0x800\n"
  "v_add_u32 v21, 0, v18\n"
  "v_xor_b32 v21, v21, v19\n"
  "v_lshl_add_u32 v21, v21, 5, v20\n"
  "v_add_u32 v22, 0, v18\n"
  "v_mul_lo_u32 v22, v22, s36\n"
  "v_add_u32 v3, v22, v21\n"
  "v_add_u32 v21, 4, v18\n"
  "v_xor_b32 v21, v21, v19\n"
  "v_lshl_add_u32 v21, v21, 5, v20\n"
  "v_add_u32 v22, 4, v18\n"
  "v_mul_lo_u32 v22, v22, s36\n"
  "v_add_u32 v4, v22, v21\n"
  "v_subrev_u32 v4, 0x400, v4\n"
  "v_add_u32 v21, 0, v18\n"
  "v_xor_b32 v21, v21, v19\n"
  "v_lshl_add_u32 v21, v21, 5, v20\n"
  "v_add_u32 v22, 8, v18\n"
  "v_mul_lo_u32 v22, v22, s36\n"
  "v_add_u32 v5, v22, v21\n"
  "v_subrev_u32 v5, 0x800, v5\n"
  "v_add_u32 v21, 4, v18\n"
  "v_xor_b32 v21, v21, v19\n"
  "v_lshl_add_u32 v21, v21, 5, v20\n"
  "v_add_u32 v22, 12, v18\n"
  "v_mul_lo_u32 v22, v22, s36\n"
  "v_add_u32 v6, v22, v21\n"
  "v_subrev_u32 v6, 0xc00, v6\n"
  "s_cmp_ge_u32 s42, 2\n"
  "s_cbranch_scc1 SK_G_%=\n"
  "s_lshl_b32 s29, s89, 7\n"
  "s_add_u32 s29, s29, 0x2000\n"
  "s_cmp_lg_u32 s25, 0\n"
  "s_cbranch_scc1 POLLSKIP_19_%=\n"
  "v_min_u32 v56, 31, v1\n"
  "v_lshlrev_b32 v56, 2, v56\n"
  "v_add_u32 v56, s29, v56\n"
  "s_mov_b32 s63, 0x30d40\n"
  "POLL_20_%=:\n"
  "global_load_dword v57, v56, s[80:81] sc1\n"
  "s_waitcnt vmcnt(0)\n"
  "v_cmp_ne_u32 vcc, s90, v57\n"
  "s_nop 1\n"
  "s_cmp_eq_u64 vcc, 0\n"
  "s_cbranch_scc1 POLLOK_21_%=\n"
  "s_sleep 1\n"
  "s_sub_u32 s63, s63, 1\n"
  "s_cmp_lg_u32 s63, 0\n"
  "s_cbranch_scc1 POLL_20_%=\n"
  "POLLOK_21_%=:\n"
  "POLLSKIP_19_%=:\n"
  "s_barrier\n"
  "buffer_load_dwordx4 a[64:67], v2, s[4:7], s21 offen sc1\n"
  "buffer_load_dwordx4 a[68:71], v2, s[4:7], s21 offen offset:256 sc1\n"
  "buffer_load_dwordx4 a[72:75], v2, s[4:7], s21 offen offset:512 sc1\n"
  "buffer_load_dwordx4 a[76:79], v2, s[4:7], s21 offen offset:768 sc1\n"
  "s_add_u32 s21, s21, 0x4000\n"
  "s_add_u32 m0, s24, 0\n"
  "s_nop 0\n"
  "buffer_load_dwordx4 v3, s[8:11], s22 offen lds\n"
  "buffer_load_dwordx4 v4, s[8:11], s22 offen offset:1024 lds\n"
  "buffer_load_dwordx4 v5, s[8:11], s22 offen offset:2048 lds\n"
  "buffer_load_dwordx4 v6, s[8:11], s22 offen offset:3072 lds\n"
  "s_add_u32 s22, s22, s23\n"
  "buffer_load_dwordx4 a[80:83], v2, s[4:7], s21 offen sc1\n"
  "buffer_load_dwordx4 a[84:87], v2, s[4:7], s21 offen offset:256 sc1\n"
  "buffer_load_dwordx4 a[88:91], v2, s[4:7], s21 offen offset:512 sc1\n"
  "buffer_load_dwordx4 a[92:95], v2, s[4:7], s21 offen offset:768 sc1\n"
  "s_add_u32 s21, s21, 0x4000\n"
  "buffer_load_dwordx4 a[96:99], v2, s[4:7], s21 offen sc1\n"
  "buffer_load_dwordx4 a[100:103], v2, s[4:7], s21 offen offset:256 sc1\n"
  "buffer_load_dwordx4 a[104:107], v2, s[4:7], s21 offen offset:512 sc1\n"
  "buffer_load_dwordx4 a[108:111], v2, s[4:7], s21 offen offset:768 sc1\n"
  "s_add_u32 s21, s21, 0x4000\n"
  "s_add_u32 m0, s24, 0x4000\n"
  "s_nop 0\n"
  "buffer_load_dwordx4 v3, s[8:11], s22 offen lds\n"
  "buffer_load_dwordx4 v4, s[8:11], s22 offen offset:1024 lds\n"
  "buffer_load_dwordx4 v5, s[8:11], s22 offen offset:2048 lds\n"
  "buffer_load_dwordx4 v6, s[8:11], s22 offen offset:3072 lds\n"
  "s_add_u32 s22, s22, s23\n"
  "buffer_load_dwordx4 a[112:115], v2, s[4:7], s21 offen sc1\n"
  "buffer_load_dwordx4 a[116:119], v2, s[4:7], s21 offen offset:256 sc1\n"
  "buffer_load_dwordx4 a[120:123], v2, s[4:7], s21 offen offset:512 sc1\n"
  "buffer_load_dwordx4 a[124:127], v2, s[4:7], s21 offen offset:768 sc1\n"
  "s_add_u32 s21, s21, 0x4000\n"
  "buffer_load_dwordx4 a[128:131], v2, s[4:7], s21 offen sc1\n"
  "buffer_load_dwordx4 a[132:135], v2, s[4:7], s21 offen offset:256 sc1\n"
  "buffer_load_dwordx4 a[136:139], v2, s[4:7], s21 offen offset:512 sc1\n"
  "buffer_load_dwordx4 a[140:143], v2, s[4:7], s21 offen offset:768 sc1\n"
  "s_add_u32 s21, s21, 0x4000\n"
  "s_add_u32 m0, s24, 0x8000\n"
  "s_nop 0\n"
  "buffer_load_dwordx4 v3, s[8:11], s22 offen lds\n"
  "buffer_load_dwordx4 v4, s[8:11], s22 offen offset:1024 lds\n"
  "buffer_load_dwordx4 v5, s[8:11], s22 offen offset:2048 lds\n"
  "buffer_load_dwordx4 v6, s[8:11], s22 offen offset:3072 lds\n"
  "s_add_u32 s22, s22, s23\n"
  "buffer_load_dwordx4 a[144:147], v2, s[4:7], s21 offen sc1\n"
  "buffer_load_dwordx4 a[148:151], v2, s[4:7], s21 offen offset:256 sc1\n"
  "buffer_load_dwordx4 a[152:155], v2, s[4:7], s21 offen offset:512 sc1\n"
  "buffer_load_dwordx4 a[156:159], v2, s[4:7], s21 offen offset:768 sc1\n"
  "s_add_u32 s21, s21, 0x4000\n"
  "buffer_load_dwordx4 a[160:163], v2, s[4:7], s21 offen sc1\n"
  "buffer_load_dwordx4 a[164:167], v2, s[4:7], s21 offen offset:256 sc1\n"
  "buffer_load_dwordx4 a[168:171], v2, s[4:7], s21 offen offset:512 sc1\n"
  "buffer_load_dwordx4 a[172:175], v2, s[4:7], s21 offen offset:768 sc1\n"
  "s_add_u32 s21, s21, 0x4000\n"
  "s_add_u32 m0, s24, 0xc000\n"
  "s_nop 0\n"
  "buffer_load_dwordx4 v3, s[8:11], s22 offen lds\n"
  "buffer_load_dwordx4 v4, s[8:11], s22 offen offset:1024 lds\n"
  "buffer_load_dwordx4 v5, s[8:11], s22 offen offset:2048 lds\n"
  "buffer_load_dwordx4 v6, s[8:11], s22 offen offset:3072 lds\n"
  "s_add_u32 s22, s22, s23\n"
  "s_mov_b32 s88, -1\n"
  "s_mov_b32 s14, 0\n"
  "s_waitcnt vmcnt(36)\n"
  "s_barrier\n"
  "s_waitcnt lgkmcnt(15)\n"
  "ds_read_b64_tr_b16 a[0:1], v7\n"
  "s_waitcnt lgkmcnt(15)\n"
  "ds_read_b64_tr_b16 a[2:3], v7 offset:4096\n"
  "s_waitcnt lgkmcnt(15)\n"
  "ds_read_b64_tr_b16 a[4:5], v8\n"
  "s_waitcnt lgkmcnt(15)\n"
  "ds_read_b64_tr_b16 a[6:7], v8 offset:4096\n"
  "s_waitcnt lgkmcnt(15)\n"
  "ds_read_b64_tr_b16 a[8:9], v9\n"
  "s_waitcnt lgkmcnt(15)\n"
  "ds_read_b64_tr_b16 a[10:11], v9 offset:4096\n"
  "s_waitcnt lgkmcnt(15)\n"
  "ds_read_b64_tr_b16 a[12:13], v10\n"
  "s_waitcnt lgkmcnt(15)\n"
  "ds_read_b64_tr_b16 a[14:15], v10 offset:4096\n"
  "s_waitcnt lgkmcnt(15)\n"
  "ds_read_b64_tr_b16 a[16:17], v11\n"
  "s_waitcnt lgkmcnt(15)\n"
  "ds_read_b64_tr_b16 a[18:19], v11 offset:4096\n"
  "s_waitcnt lgkmcnt(15)\n"
  "ds_read_b64_tr_b16 a[20:21], v12\n"
  "s_waitcnt lgkmcnt(15)\n"
  "ds_read_b64_tr_b16 a[22:23], v12 offset:4096\n"
  "s_waitcnt lgkmcnt(15)\n"
  "ds_read_b64_tr_b16 a[24:25], v13\n"
  "s_waitcnt lgkmcnt(15)\n"
  "ds_read_b64_tr_b16 a[26:27], v13 offset:4096\n"
  "s_waitcnt lgkmcnt(15)\n"
  "ds_read_b64_tr_b16 a[28:29], v14\n"
  "s_waitcnt lgkmcnt(15)\n"
  "ds_read_b64_tr_b16 a[30:31], v14 offset:4096\n"
  "s_branch P3_ENTER_%=\n"
  "SK_G_%=:\n"
  "s_add_u32 m0, s24, 0\n"
  "s_nop 0\n"
  "buffer_load_dwordx4 v3, s[8:11], s22 offen lds\n"
  "buffer_load_dwordx4 v4, s[8:11], s22 offen offset:1024 lds\n"
  "buffer_load_dwordx4 v5, s[8:11], s22 offen offset:2048 lds\n"
  "buffer_load_dwordx4 v6, s[8:11], s22 offen offset:3072 lds\n"
  "s_add_u32 s22, s22, s23\n"
  "s_add_u32 m0, s24, 0x4000\n"
  "s_nop 0\n"
  "buffer_load_dwordx4 v3, s[8:11], s22 offen lds\n"
  "buffer_load_dwordx4 v4, s[8:11], s22 offen offset:1024 lds\n"
  "buffer_load_dwordx4 v5, s[8:11], s22 offen offset:2048 lds\n"
  "buffer_load_dwordx4 v6, s[8:11], s22 offen offset:3072 lds\n"
  "s_add_u32 s22, s22, s23\n"
  "s_add_u32 m0, s24, 0x8000\n"
  "s_nop 0\n"
  "buffer_load_dwordx4 v3, s[8:11], s22 offen lds\n"
  "buffer_load_dwordx4 v4, s[8:11], s22 offen offset:1024 lds\n"
  "buffer_load_dwordx4 v5, s[8:11], s22 offen offset:2048 lds\n"
  "buffer_load_dwordx4 v6, s[8:11], s22 offen offset:3072 lds\n"
  "s_add_u32 s22, s22, s23\n"
  "s_add_u32 m0, s24, 0xc000\n"
  "s_nop 0\n"
  "buffer_load_dwordx4 v3, s[8:11], s22 offen lds\n"
  "buffer_load_dwordx4 v4, s[8:11], s22 offen offset:1024 lds\n"
  "buffer_load_dwordx4 v5, s[8:11], s22 offen offset:2048 lds\n"
  "buffer_load_dwordx4 v6, s[8:11], s22 offen offset:3072 lds\n"
  "s_add_u32 s22, s22, s23\n"
  "s_waitcnt vmcnt(0)\n"
  "s_barrier\n"
  "s_waitcnt lgkmcnt(15)\n"
  "ds_read_b64_tr_b16 a[0:1], v7\n"
  "s_waitcnt lgkmcnt(15)\n"
  "ds_read_b64_tr_b16 a[2:3], v7 offset:4096\n"
  "s_waitcnt lgkmcnt(15)\n"
  "ds_read_b64_tr_b16 a[4:5], v8\n"
  "s_waitcnt lgkmcnt(15)\n"
  "ds_read_b64_tr_b16 a[6:7], v8 offset:4096\n"
  "s_waitcnt lgkmcnt(15)\n"
  "ds_read_b64_tr_b16 a[8:9], v9\n"
  "s_waitcnt lgkmcnt(15)\n"
  "ds_read_b64_tr_b16 a[10:11], v9 offset:4096\n"
  "s_waitcnt lgkmcnt(15)\n"
  "ds_read_b64_tr_b16 a[12:13], v10\n"
  "s_waitcnt lgkmcnt(15)\n"
  "ds_read_b64_tr_b16 a[14:15], v10 offset:4096\n"
  "s_waitcnt lgkmcnt(15)\n"
  "ds_read_b64_tr_b16 a[16:17], v11\n"
  "s_waitcnt lgkmcnt(15)\n"
  "ds_read_b64_tr_b16 a[18:19], v11 offset:4096\n"
  "s_waitcnt lgkmcnt(15)\n"
  "ds_read_b64_tr_b16 a[20:21], v12\n"
  "s_waitcnt lgkmcnt(15)\n"
  "ds_read_b64_tr_b16 a[22:23], v12 offset:4096\n"
  "s_waitcnt lgkmcnt(15)\n"
  "ds_read_b64_tr_b16 a[24:25], v13\n"
  "s_waitcnt lgkmcnt(15)\n"
  "ds_read_b64_tr_b16 a[26:27], v13 offset:4096\n"
  "s_waitcnt lgkmcnt(15)\n"
  "ds_read_b64_tr_b16 a[28:29], v14\n"
  "s_waitcnt lgkmcnt(15)\n"
  "ds_read_b64_tr_b16 a[30:31], v14 offset:4096\n"
  "s_lshl_b32 s29, s89, 7\n"
  "s_and_b32 s30, s42, 1\n"
  "s_mul_i32 s30, s30, 56\n"
  "s_add_u32 s29, s29, s30\n"
  "s_add_u32 s29, s29, 0x2400\n"
  "v_min_u32 v56, 6, v1\n"
  "v_lshlrev_b32 v56, 2, v56\n"
  "v_add_u32 v56, s29, v56\n"
  "s_mov_b32 s63, 0x30d40\n"
  "POLL_22_%=:\n"
  "global_load_dword v57, v56, s[80:81] sc1\n"
  "s_waitcnt vmcnt(0)\n"
  "v_cmp_ne_u32 vcc, s90, v57\n"
  "s_nop 1\n"
  "s_cmp_eq_u64 vcc, 0\n"
  "s_cbranch_scc1 POLLOK_23_%=\n"
  "s_sleep 1\n"
  "s_sub_u32 s63, s63, 1\n"
  "s_cmp_lg_u32 s63, 0\n"
  "s_cbranch_scc1 POLL_22_%=\n"
  "POLLOK_23_%=:\n"
  "buffer_load_dwordx4 a[64:67], v2, s[4:7], s21 offen sc1\n"
  "buffer_load_dwordx4 a[68:71], v2, s[4:7], s21 offen offset:256 sc1\n"
  "buffer_load_dwordx4 a[72:75], v2, s[4:7], s21 offen offset:512 sc1\n"
  "buffer_load_dwordx4 a[76:79], v2, s[4:7], s21 offen offset:768 sc1\n"
  "s_add_u32 s21, s21, 0x4000\n"
  "buffer_load_dwordx4 a[80:83], v2, s[4:7], s21 offen sc1\n"
  "buffer_load_dwordx4 a[84:87], v2, s[4:7], s21 offen offset:256 sc1\n"
  "buffer_load_dwordx4 a[88:91], v2, s[4:7], s21 offen offset:512 sc1\n"
  "buffer_load_dwordx4 a[92:95], v2, s[4:7], s21 offen offset:768 sc1\n"
  "s_add_u32 s21, s21, 0x4000\n"
  "buffer_load_dwordx4 a[96:99], v2, s[4:7], s21 offen sc1\n"
  "buffer_load_dwordx4 a[100:103], v2, s[4:7], s21 offen offset:256 sc1\n"
  "buffer_load_dwordx4 a[104:107], v2, s[4:7], s21 offen offset:512 sc1\n"
  "buffer_load_dwordx4 a[108:111], v2, s[4:7], s21 offen offset:768 sc1\n"
  "s_add_u32 s21, s21, 0x4000\n"
  "buffer_load_dwordx4 a[112:115], v2, s[4:7], s21 offen sc1\n"
  "buffer_load_dwordx4 a[116:119], v2, s[4:7], s21 offen offset:256 sc1\n"
  "buffer_load_dwordx4 a[120:123], v2, s[4:7], s21 offen offset:512 sc1\n"
  "buffer_load_dwordx4 a[124:127], v2, s[4:7], s21 offen offset:768 sc1\n"
  "s_add_u32 s21, s21, 0x4000\n"
  "buffer_load_dwordx4 a[128:131], v2, s[4:7], s21 offen sc1\n"
  "buffer_load_dwordx4 a[132:135], v2, s[4:7], s21 offen offset:256 sc1\n"
  "buffer_load_dwordx4 a[136:139], v2, s[4:7], s21 offen offset:512 sc1\n"
  "buffer_load_dwordx4 a[140:143], v2, s[4:7], s21 offen offset:768 sc1\n"
  "s_add_u32 s21, s21, 0x4000\n"
  "buffer_load_dwordx4 a[144:147], v2, s[4:7], s21 offen sc1\n"
  "buffer_load_dwordx4 a[148:151], v2, s[4:7], s21 offen offset:256 sc1\n"
  "buffer_load_dwordx4 a[152:155], v2, s[4:7], s21 offen offset:512 sc1\n"
  "buffer_load_dwordx4 a[156:159], v2, s[4:7], s21 offen offset:768 sc1\n"
  "s_add_u32 s21, s21, 0x4000\n"
  "buffer_load_dwordx4 a[160:163], v2, s[4:7], s21 offen sc1\n"
  "buffer_load_dwordx4 a[164:167], v2, s[4:7], s21 offen offset:256 sc1\n"
  "buffer_load_dwordx4 a[168:171], v2, s[4:7], s21 offen offset:512 sc1\n"
  "buffer_load_dwordx4 a[172:175], v2, s[4:7], s21 offen offset:768 sc1\n"
  "s_add_u32 s21, s21, 0x4000\n"
  "s_mov_b32 s88, -1\n"
  "s_mov_b32 s14, 0\n"
  "s_waitcnt vmcnt(24)\n"
  "P3_ENTER_%=:\n"
  "v_and_b32 v18, 15, v1\n"
  "v_lshlrev_b32 v18, 11, v18\n"
  "v_lshrrev_b32 v19, 4, v1\n"
  "v_and_b32 v21, 1, v19\n"
  "v_lshlrev_b32 v21, 5, v21\n"
  "v_lshrrev_b32 v19, 1, v19\n"
  "v_lshlrev_b32 v19, 4, v19\n"
  "v_add3_u32 v48, v18, v19, v21\n"
  "s_cmp_lt_u32 s42, 2\n"
  "s_cbranch_scc1 P3_SLOOP_%=\n"
  "s_mov_b32 s20, 0\n"
  "s_waitcnt lgkmcnt(0)\n"
  "s_swappc_b64 s[48:49], s[52:53]\n"
  "s_nop 0\n"
  "s_lshl_b32 s29, s89, 7\n"
  "s_and_b32 s30, s42, 1\n"
  "s_mul_i32 s30, s30, 56\n"
  "s_add_u32 s29, s29, s30\n"
  "s_add_u32 s29, s29, 0x241c\n"
  "v_min_u32 v54, 6, v1\n"
  "v_lshlrev_b32 v54, 2, v54\n"
  "v_add_u32 v54, s29, v54\n"
  "global_load_dword v59, v54, s[80:81] sc1\n"
  "s_mov_b32 s20, 1\n"
  "s_waitcnt lgkmcnt(0)\n"
  "s_swappc_b64 s[48:49], s[54:55]\n"
  "s_nop 0\n"
  "v_cmp_ne_u32 vcc, s90, v59\n"
  "s_nop 1\n"
  "s_cmp_eq_u64 vcc, 0\n"
  "s_cbranch_scc1 FLAGOK_24_%=\n"
  "s_lshl_b32 s29, s89, 7\n"
  "s_and_b32 s30, s42, 1\n"
  "s_mul_i32 s30, s30, 56\n"
  "s_add_u32 s29, s29, s30\n"
  "s_add_u32 s29, s29, 0x241c\n"
  "v_min_u32 v56, 6, v1\n"
  "v_lshlrev_b32 v56, 2, v56\n"
  "v_add_u32 v56, s29, v56\n"
  "s_mov_b32 s63, 0x30d40\n"
  "POLL_25_%=:\n"
  "global_load_dword v57, v56, s[80:81] sc1\n"
  "s_waitcnt vmcnt(0)\n"
  "v_cmp_ne_u32 vcc, s90, v57\n"
  "s_nop 1\n"
  "s_cmp_eq_u64 vcc, 0\n"
  "s_cbranch_scc1 POLLOK_26_%=\n"
  "s_sleep 1\n"
  "s_sub_u32 s63, s63, 1\n"
  "s_cmp_lg_u32 s63, 0\n"
  "s_cbranch_scc1 POLL_25_%=\n"
  "POLLOK_26_%=:\n"
  "FLAGOK_24_%=:\n"
  "s_mov_b32 s20, 5\n"
  "s_waitcnt lgkmcnt(0)\n"
  "s_swappc_b64 s[48:49], s[54:55]\n"
  "s_nop 0\n"
  "s_branch P3_LDONE_%=\n"
  "P3_SLOOP_%=:\n"
  "s_mov_b32 s20, 7\n"
  "s_waitcnt lgkmcnt(0)\n"
  "s_swappc_b64 s[48:49], s[52:53]\n"
  "s_nop 0\n"
  "P3_LDONE_%=:\n"
  "s_lshl_b32 s29, s89, 8\n"
  "s_lshl_b32 s30, s25, 6\n"
  "s_add_u32 s29, s29, s30\n"
  "s_lshl_b32 s29, s29, 11\n"
  "s_lshl_b32 s30, s43, 8\n"
  "s_add_u32 s29, s29, s30\n"
  "s_lshl_b32 s30, s42, 6\n"
  "s_add_u32 s46, s29, s30\n"
  "s_add_u32 s16, s64, s46\n"
  "s_addc_u32 s17, s65, 0\n"
  "s_and_b32 s17, s17, 0xffff\n"
  "s_mov_b32 s18, 0x1f840\n"
  "s_mov_b32 s19, 0x20000\n"
  "s_mov_b32 s30, 0\n"
  "buffer_load_dwordx4 v[112:115], v48, s[16:19], s30 offen\n"
  "s_mov_b32 s30, 0x8000\n"
  "buffer_load_dwordx4 v[116:119], v48, s[16:19], s30 offen\n"
  "s_mov_b32 s30, 0x10000\n"
  "buffer_load_dwordx4 v[120:123], v48, s[16:19], s30 offen\n"
  "s_mov_b32 s30, 0x18000\n"
  "buffer_load_dwordx4 v[124:127], v48, s[16:19], s30 offen\n"
  "s_mov_b32 s56, s76\n"
  "s_and_b32 s57, s77, 0xffff\n"
  "s_mov_b32 s58, 0x800\n"
  "s_mov_b32 s59, 0x20000\n"
  "v_lshrrev_b32 v18, 4, v1\n"
  "v_lshlrev_b32 v18, 3, v18\n"
  "s_lshl_b32 s30, s43, 8\n"
  "s_lshl_b32 s31, s42, 6\n"
  "s_add_u32 s30, s30, s31\n"
  "buffer_load_dwordx2 v[108:109], v18, s[56:59], s30 offen\n"
  "buffer_load_dwordx2 v[110:111], v18, s[56:59], s30 offen offset:32\n"
  "s_lshl_b32 s29, s41, 18\n"
  "s_add_u32 s12, s80, 0x2400000\n"
  "s_addc_u32 s13, s81, 0\n"
  "s_add_u32 s12, s12, s29\n"
  "s_addc_u32 s13, s13, 0\n"
  "s_and_b32 s13, s13, 0xffff\n"
  "s_mov_b32 s14, 0x40000\n"
  "s_mov_b32 s15, 0x20000\n"
  "s_cmp_eq_u32 s42, 1\n"
  "s_cbranch_scc1 EX_R1_%=\n"
  "s_cmp_eq_u32 s42, 2\n"
  "s_cbranch_scc1 EX_R2_%=\n"
  "s_cmp_eq_u32 s42, 3\n"
  "s_cbranch_scc1 EX_R3_%=\n"
  "EX_R0_%=:\n"
  "s_mov_b32 s30, 0x4000\n"
  "v_cvt_pk_bf16_f32 v64, v160, v161\n"
  "v_cvt_pk_bf16_f32 v65, v162, v163\n"
  "v_cvt_pk_bf16_f32 v66, v176, v177\n"
  "v_cvt_pk_bf16_f32 v67, v178, v179\n"
  "buffer_store_dwordx4 v[64:67], v2, s[12:15], s30 offen sc1\n"
  "v_cvt_pk_bf16_f32 v68, v164, v165\n"
  "v_cvt_pk_bf16_f32 v69, v166, v167\n"
  "v_cvt_pk_bf16_f32 v70, v180, v181\n"
  "v_cvt_pk_bf16_f32 v71, v182, v183\n"
  "buffer_store_dwordx4 v[68:71], v2, s[12:15], s30 offen offset:256 sc1\n"
  "v_cvt_pk_bf16_f32 v72, v168, v169\n"
  "v_cvt_pk_bf16_f32 v73, v170, v171\n"
  "v_cvt_pk_bf16_f32 v74, v184, v185\n"
  "v_cvt_pk_bf16_f32 v75, v186, v187\n"
  "buffer_store_dwordx4 v[72:75], v2, s[12:15], s30 offen offset:512 sc1\n"
  "v_cvt_pk_bf16_f32 v76, v172, v173\n"
  "v_cvt_pk_bf16_f32 v77, v174, v175\n"
  "v_cvt_pk_bf16_f32 v78, v188, v189\n"
  "v_cvt_pk_bf16_f32 v79, v190, v191\n"
  "buffer_store_dwordx4 v[76:79], v2, s[12:15], s30 offen offset:768 sc1\n"
  "s_mov_b32 s30, 0x8000\n"
  "v_cvt_pk_bf16_f32 v80, v192, v193\n"
  "v_cvt_pk_bf16_f32 v81, v194, v195\n"
  "v_cvt_pk_bf16_f32 v82, v208, v209\n"
  "v_cvt_pk_bf16_f32 v83, v210, v211\n"
  "buffer_store_dwordx4 v[80:83], v2, s[12:15], s30 offen sc1\n"
  "v_cvt_pk_bf16_f32 v84, v196, v197\n"
  "v_cvt_pk_bf16_f32 v85, v198, v199\n"
  "v_cvt_pk_bf16_f32 v86, v212, v213\n"
  "v_cvt_pk_bf16_f32 v87, v214, v215\n"
  "buffer_store_dwordx4 v[84:87], v2, s[12:15], s30 offen offset:256 sc1\n"
  "v_cvt_pk_bf16_f32 v88, v200, v201\n"
  "v_cvt_pk_bf16_f32 v89, v202, v203\n"
  "v_cvt_pk_bf16_f32 v90, v216, v217\n"
  "v_cvt_pk_bf16_f32 v91, v218, v219\n"
  "buffer_store_dwordx4 v[88:91], v2, s[12:15], s30 offen offset:512 sc1\n"
  "v_cvt_pk_bf16_f32 v92, v204, v205\n"
  "v_cvt_pk_bf16_f32 v93, v206, v207\n"
  "v_cvt_pk_bf16_f32 v94, v220, v221\n"
  "v_cvt_pk_bf16_f32 v95, v222, v223\n"
  "buffer_store_dwordx4 v[92:95], v2, s[12:15], s30 offen offset:768 sc1\n"
  "s_mov_b32 s30, 0xc000\n"
  "v_cvt_pk_bf16_f32 v64, v224, v225\n"
  "v_cvt_pk_bf16_f32 v65, v226, v227\n"
  "v_cvt_pk_bf16_f32 v66, v240, v241\n"
  "v_cvt_pk_bf16_f32 v67, v242, v243\n"
  "buffer_store_dwordx4 v[64:67], v2, s[12:15], s30 offen sc1\n"
  "v_cvt_pk_bf16_f32 v68, v228, v229\n"
  "v_cvt_pk_bf16_f32 v69, v230, v231\n"
  "v_cvt_pk_bf16_f32 v70, v244, v245\n"
  "v_cvt_pk_bf16_f32 v71, v246, v247\n"
  "buffer_store_dwordx4 v[68:71], v2, s[12:15], s30 offen offset:256 sc1\n"
  "v_cvt_pk_bf16_f32 v72, v232, v233\n"
  "v_cvt_pk_bf16_f32 v73, v234, v235\n"
  "v_cvt_pk_bf16_f32 v74, v248, v249\n"
  "v_cvt_pk_bf16_f32 v75, v250, v251\n"
  "buffer_store_dwordx4 v[72:75], v2, s[12:15], s30 offen offset:512 sc1\n"
  "v_cvt_pk_bf16_f32 v76, v236, v237\n"
  "v_cvt_pk_bf16_f32 v77, v238, v239\n"
  "v_cvt_pk_bf16_f32 v78, v252, v253\n"
  "v_cvt_pk_bf16_f32 v79, v254, v255\n"
  "buffer_store_dwordx4 v[76:79], v2, s[12:15], s30 offen offset:768 sc1\n"
  "s_branch EX_END_%=\n"
  "EX_R1_%=:\n"
  "s_mov_b32 s30, 0x10000\n"
  "v_cvt_pk_bf16_f32 v64, v128, v129\n"
  "v_cvt_pk_bf16_f32 v65, v130, v131\n"
  "v_cvt_pk_bf16_f32 v66, v144, v145\n"
  "v_cvt_pk_bf16_f32 v67, v146, v147\n"
  "buffer_store_dwordx4 v[64:67], v2, s[12:15], s30 offen sc1\n"
  "v_cvt_pk_bf16_f32 v68, v132, v133\n"
  "v_cvt_pk_bf16_f32 v69, v134, v135\n"
  "v_cvt_pk_bf16_f32 v70, v148, v149\n"
  "v_cvt_pk_bf16_f32 v71, v150, v151\n"
  "buffer_store_dwordx4 v[68:71], v2, s[12:15], s30 offen offset:256 sc1\n"
  "v_cvt_pk_bf16_f32 v72, v136, v137\n"
  "v_cvt_pk_bf16_f32 v73, v138, v139\n"
  "v_cvt_pk_bf16_f32 v74, v152, v153\n"
  "v_cvt_pk_bf16_f32 v75, v154, v155\n"
  "buffer_store_dwordx4 v[72:75], v2, s[12:15], s30 offen offset:512 sc1\n"
  "v_cvt_pk_bf16_f32 v76, v140, v141\n"
  "v_cvt_pk_bf16_f32 v77, v142, v143\n"
  "v_cvt_pk_bf16_f32 v78, v156, v157\n"
  "v_cvt_pk_bf16_f32 v79, v158, v159\n"
  "buffer_store_dwordx4 v[76:79], v2, s[12:15], s30 offen offset:768 sc1\n"
  "s_mov_b32 s30, 0x1c000\n"
  "v_cvt_pk_bf16_f32 v80, v224, v225\n"
  "v_cvt_pk_bf16_f32 v81, v226, v227\n"
  "v_cvt_pk_bf16_f32 v82, v240, v241\n"
  "v_cvt_pk_bf16_f32 v83, v242, v243\n"
  "buffer_store_dwordx4 v[80:83], v2, s[12:15], s30 offen sc1\n"
  "v_cvt_pk_bf16_f32 v84, v228, v229\n"
  "v_cvt_pk_bf16_f32 v85, v230, v231\n"
  "v_cvt_pk_bf16_f32 v86, v244, v245\n"
  "v_cvt_pk_bf16_f32 v87, v246, v247\n"
  "buffer_store_dwordx4 v[84:87], v2, s[12:15], s30 offen offset:256 sc1\n"
  "v_cvt_pk_bf16_f32 v88, v232, v233\n"
  "v_cvt_pk_bf16_f32 v89, v234, v235\n"
  "v_cvt_pk_bf16_f32 v90, v248, v249\n"
  "v_cvt_pk_bf16_f32 v91, v250, v251\n"
  "buffer_store_dwordx4 v[88:91], v2, s[12:15], s30 offen offset:512 sc1\n"
  "v_cvt_pk_bf16_f32 v92, v236, v237\n"
  "v_cvt_pk_bf16_f32 v93, v238, v239\n"
  "v_cvt_pk_bf16_f32 v94, v252, v253\n"
  "v_cvt_pk_bf16_f32 v95, v254, v255\n"
  "buffer_store_dwordx4 v[92:95], v2, s[12:15], s30 offen offset:768 sc1\n"
  "s_mov_b32 s30, 0x18000\n"
  "v_cvt_pk_bf16_f32 v64, v192, v193\n"
  "v_cvt_pk_bf16_f32 v65, v194, v195\n"
  "v_cvt_pk_bf16_f32 v66, v208, v209\n"
  "v_cvt_pk_bf16_f32 v67, v210, v211\n"
  "buffer_store_dwordx4 v[64:67], v2, s[12:15], s30 offen sc1\n"
  "v_cvt_pk_bf16_f32 v68, v196, v197\n"
  "v_cvt_pk_bf16_f32 v69, v198, v199\n"
  "v_cvt_pk_bf16_f32 v70, v212, v213\n"
  "v_cvt_pk_bf16_f32 v71, v214, v215\n"
  "buffer_store_dwordx4 v[68:71], v2, s[12:15], s30 offen offset:256 sc1\n"
  "v_cvt_pk_bf16_f32 v72, v200, v201\n"
  "v_cvt_pk_bf16_f32 v73, v202, v203\n"
  "v_cvt_pk_bf16_f32 v74, v216, v217\n"
  "v_cvt_pk_bf16_f32 v75, v218, v219\n"
  "buffer_store_dwordx4 v[72:75], v2, s[12:15], s30 offen offset:512 sc1\n"
  "v_cvt_pk_bf16_f32 v76, v204, v205\n"
  "v_cvt_pk_bf16_f32 v77, v206, v207\n"
  "v_cvt_pk_bf16_f32 v78, v220, v221\n"
  "v_cvt_pk_bf16_f32 v79, v222, v223\n"
  "buffer_store_dwordx4 v[76:79], v2, s[12:15], s30 offen offset:768 sc1\n"
  "v_mov_b32 v128, v160\n"
  "v_mov_b32 v129, v161\n"
  "v_mov_b32 v130, v162\n"
  "v_mov_b32 v131, v163\n"
  "v_mov_b32 v132, v164\n"
  "v_mov_b32 v133, v165\n"
  "v_mov_b32 v134, v166\n"
  "v_mov_b32 v135, v167\n"
  "v_mov_b32 v136, v168\n"
  "v_mov_b32 v137, v169\n"
  "v_mov_b32 v138, v170\n"
  "v_mov_b32 v139, v171\n"
  "v_mov_b32 v140, v172\n"
  "v_mov_b32 v141, v173\n"
  "v_mov_b32 v142, v174\n"
  "v_mov_b32 v143, v175\n"
  "v_mov_b32 v144, v176\n"
  "v_mov_b32 v145, v177\n"
  "v_mov_b32 v146, v178\n"
  "v_mov_b32 v147, v179\n"
  "v_mov_b32 v148, v180\n"
  "v_mov_b32 v149, v181\n"
  "v_mov_b32 v150, v182\n"
  "v_mov_b32 v151, v183\n"
  "v_mov_b32 v152, v184\n"
  "v_mov_b32 v153, v185\n"
  "v_mov_b32 v154, v186\n"
  "v_mov_b32 v155, v187\n"
  "v_mov_b32 v156, v188\n"
  "v_mov_b32 v157, v189\n"
  "v_mov_b32 v158, v190\n"
  "v_mov_b32 v159, v191\n"
  "s_branch EX_END_%=\n"
  "EX_R2_%=:\n"
  "s_mov_b32 s30, 0x2c000\n"
  "v_cvt_pk_bf16_f32 v64, v224, v225\n"
  "v_cvt_pk_bf16_f32 v65, v226, v227\n"
  "v_cvt_pk_bf16_f32 v66, v240, v241\n"
  "v_cvt_pk_bf16_f32 v67, v242, v243\n"
  "buffer_store_dwordx4 v[64:67], v2, s[12:15], s30 offen sc1\n"
  "v_cvt_pk_bf16_f32 v68, v228, v229\n"
  "v_cvt_pk_bf16_f32 v69, v230, v231\n"
  "v_cvt_pk_bf16_f32 v70, v244, v245\n"
  "v_cvt_pk_bf16_f32 v71, v246, v247\n"
  "buffer_store_dwordx4 v[68:71], v2, s[12:15], s30 offen offset:256 sc1\n"
  "v_cvt_pk_bf16_f32 v72, v232, v233\n"
  "v_cvt_pk_bf16_f32 v73, v234, v235\n"
  "v_cvt_pk_bf16_f32 v74, v248, v249\n"
  "v_cvt_pk_bf16_f32 v75, v250, v251\n"
  "buffer_store_dwordx4 v[72:75], v2, s[12:15], s30 offen offset:512 sc1\n"
  "v_cvt_pk_bf16_f32 v76, v236, v237\n"
  "v_cvt_pk_bf16_f32 v77, v238, v239\n"
  "v_cvt_pk_bf16_f32 v78, v252, v253\n"
  "v_cvt_pk_bf16_f32 v79, v254, v255\n"
  "buffer_store_dwordx4 v[76:79], v2, s[12:15], s30 offen offset:768 sc1\n"
  "s_mov_b32 s30, 0x20000\n"
  "v_cvt_pk_bf16_f32 v80, v128, v129\n"
  "v_cvt_pk_bf16_f32 v81, v130, v131\n"
  "v_cvt_pk_bf16_f32 v82, v144, v145\n"
  "v_cvt_pk_bf16_f32 v83, v146, v147\n"
  "buffer_store_dwordx4 v[80:83], v2, s[12:15], s30 offen sc1\n"
  "v_cvt_pk_bf16_f32 v84, v132, v133\n"
  "v_cvt_pk_bf16_f32 v85, v134, v135\n"
  "v_cvt_pk_bf16_f32 v86, v148, v149\n"
  "v_cvt_pk_bf16_f32 v87, v150, v151\n"
  "buffer_store_dwordx4 v[84:87], v2, s[12:15], s30 offen offset:256 sc1\n"
  "v_cvt_pk_bf16_f32 v88, v136, v137\n"
  "v_cvt_pk_bf16_f32 v89, v138, v139\n"
  "v_cvt_pk_bf16_f32 v90, v152, v153\n"
  "v_cvt_pk_bf16_f32 v91, v154, v155\n"
  "buffer_store_dwordx4 v[88:91], v2, s[12:15], s30 offen offset:512 sc1\n"
  "v_cvt_pk_bf16_f32 v92, v140, v141\n"
  "v_cvt_pk_bf16_f32 v93, v142, v143\n"
  "v_cvt_pk_bf16_f32 v94, v156, v157\n"
  "v_cvt_pk_bf16_f32 v95, v158, v159\n"
  "buffer_store_dwordx4 v[92:95], v2, s[12:15], s30 offen offset:768 sc1\n"
  "s_mov_b32 s30, 0x24000\n"
  "v_cvt_pk_bf16_f32 v64, v160, v161\n"
  "v_cvt_pk_bf16_f32 v65, v162, v163\n"
  "v_cvt_pk_bf16_f32 v66, v176, v177\n"
  "v_cvt_pk_bf16_f32 v67, v178, v179\n"
  "buffer_store_dwordx4 v[64:67], v2, s[12:15], s30 offen sc1\n"
  "v_cvt_pk_bf16_f32 v68, v164, v165\n"
  "v_cvt_pk_bf16_f32 v69, v166, v167\n"
  "v_cvt_pk_bf16_f32 v70, v180, v181\n"
  "v_cvt_pk_bf16_f32 v71, v182, v183\n"
  "buffer_store_dwordx4 v[68:71], v2, s[12:15], s30 offen offset:256 sc1\n"
  "v_cvt_pk_bf16_f32 v72, v168, v169\n"
  "v_cvt_pk_bf16_f32 v73, v170, v171\n"
  "v_cvt_pk_bf16_f32 v74, v184, v185\n"
  "v_cvt_pk_bf16_f32 v75, v186, v187\n"
  "buffer_store_dwordx4 v[72:75], v2, s[12:15], s30 offen offset:512 sc1\n"
  "v_cvt_pk_bf16_f32 v76, v172, v173\n"
  "v_cvt_pk_bf16_f32 v77, v174, v175\n"
  "v_cvt_pk_bf16_f32 v78, v188, v189\n"
  "v_cvt_pk_bf16_f32 v79, v190, v191\n"
  "buffer_store_dwordx4 v[76:79], v2, s[12:15], s30 offen offset:768 sc1\n"
  "v_mov_b32 v128, v192\n"
  "v_mov_b32 v129, v193\n"
  "v_mov_b32 v130, v194\n"
  "v_mov_b32 v131, v195\n"
  "v_mov_b32 v132, v196\n"
  "v_mov_b32 v133, v197\n"
  "v_mov_b32 v134, v198\n"
  "v_mov_b32 v135, v199\n"
  "v_mov_b32 v136, v200\n"
  "v_mov_b32 v137, v201\n"
  "v_mov_b32 v138, v202\n"
  "v_mov_b32 v139, v203\n"
  "v_mov_b32 v140, v204\n"
  "v_mov_b32 v141, v205\n"
  "v_mov_b32 v142, v206\n"
  "v_mov_b32 v143, v207\n"
  "v_mov_b32 v144, v208\n"
  "v_mov_b32 v145, v209\n"
  "v_mov_b32 v146, v210\n"
  "v_mov_b32 v147, v211\n"
  "v_mov_b32 v148, v212\n"
  "v_mov_b32 v149, v213\n"
  "v_mov_b32 v150, v214\n"
  "v_mov_b32 v151, v215\n"
  "v_mov_b32 v152, v216\n"
  "v_mov_b32 v153, v217\n"
  "v_mov_b32 v154, v218\n"
  "v_mov_b32 v155, v219\n"
  "v_mov_b32 v156, v220\n"
  "v_mov_b32 v157, v221\n"
  "v_mov_b32 v158, v222\n"
  "v_mov_b32 v159, v223\n"
  "s_branch EX_END_%=\n"
  "EX_R3_%=:\n"
  "s_mov_b32 s30, 0x38000\n"
  "v_cvt_pk_bf16_f32 v64, v192, v193\n"
  "v_cvt_pk_bf16_f32 v65, v194, v195\n"
  "v_cvt_pk_bf16_f32 v66, v208, v209\n"
  "v_cvt_pk_bf16_f32 v67, v210, v211\n"
  "buffer_store_dwordx4 v[64:67], v2, s[12:15], s30 offen sc1\n"
  "v_cvt_pk_bf16_f32 v68, v196, v197\n"
  "v_cvt_pk_bf16_f32 v69, v198, v199\n"
  "v_cvt_pk_bf16_f32 v70, v212, v213\n"
  "v_cvt_pk_bf16_f32 v71, v214, v215\n"
  "buffer_store_dwordx4 v[68:71], v2, s[12:15], s30 offen offset:256 sc1\n"
  "v_cvt_pk_bf16_f32 v72, v200, v201\n"
  "v_cvt_pk_bf16_f32 v73, v202, v203\n"
  "v_cvt_pk_bf16_f32 v74, v216, v217\n"
  "v_cvt_pk_bf16_f32 v75, v218, v219\n"
  "buffer_store_dwordx4 v[72:75], v2, s[12:15], s30 offen offset:512 sc1\n"
  "v_cvt_pk_bf16_f32 v76, v204, v205\n"
  "v_cvt_pk_bf16_f32 v77, v206, v207\n"
  "v_cvt_pk_bf16_f32 v78, v220, v221\n"
  "v_cvt_pk_bf16_f32 v79, v222, v223\n"
  "buffer_store_dwordx4 v[76:79], v2, s[12:15], s30 offen offset:768 sc1\n"
  "s_mov_b32 s30, 0x34000\n"
  "v_cvt_pk_bf16_f32 v80, v160, v161\n"
  "v_cvt_pk_bf16_f32 v81, v162, v163\n"
  "v_cvt_pk_bf16_f32 v82, v176, v177\n"
  "v_cvt_pk_bf16_f32 v83, v178, v179\n"
  "buffer_store_dwordx4 v[80:83], v2, s[12:15], s30 offen sc1\n"
  "v_cvt_pk_bf16_f32 v84, v164, v165\n"
  "v_cvt_pk_bf16_f32 v85, v166, v167\n"
  "v_cvt_pk_bf16_f32 v86, v180, v181\n"
  "v_cvt_pk_bf16_f32 v87, v182, v183\n"
  "buffer_store_dwordx4 v[84:87], v2, s[12:15], s30 offen offset:256 sc1\n"
  "v_cvt_pk_bf16_f32 v88, v168, v169\n"
  "v_cvt_pk_bf16_f32 v89, v170, v171\n"
  "v_cvt_pk_bf16_f32 v90, v184, v185\n"
  "v_cvt_pk_bf16_f32 v91, v186, v187\n"
  "buffer_store_dwordx4 v[88:91], v2, s[12:15], s30 offen offset:512 sc1\n"
  "v_cvt_pk_bf16_f32 v92, v172, v173\n"
  "v_cvt_pk_bf16_f32 v93, v174, v175\n"
  "v_cvt_pk_bf16_f32 v94, v188, v189\n"
  "v_cvt_pk_bf16_f32 v95, v190, v191\n"
  "buffer_store_dwordx4 v[92:95], v2, s[12:15], s30 offen offset:768 sc1\n"
  "s_mov_b32 s30, 0x30000\n"
  "v_cvt_pk_bf16_f32 v64, v128, v129\n"
  "v_cvt_pk_bf16_f32 v65, v130, v131\n"
  "v_cvt_pk_bf16_f32 v66, v144, v145\n"
  "v_cvt_pk_bf16_f32 v67, v146, v147\n"
  "buffer_store_dwordx4 v[64:67], v2, s[12:15], s30 offen sc1\n"
  "v_cvt_pk_bf16_f32 v68, v132, v133\n"
  "v_cvt_pk_bf16_f32 v69, v134, v135\n"
  "v_cvt_pk_bf16_f32 v70, v148, v149\n"
  "v_cvt_pk_bf16_f32 v71, v150, v151\n"
  "buffer_store_dwordx4 v[68:71], v2, s[12:15], s30 offen offset:256 sc1\n"
  "v_cvt_pk_bf16_f32 v72, v136, v137\n"
  "v_cvt_pk_bf16_f32 v73, v138, v139\n"
  "v_cvt_pk_bf16_f32 v74, v152, v153\n"
  "v_cvt_pk_bf16_f32 v75, v154, v155\n"
  "buffer_store_dwordx4 v[72:75], v2, s[12:15], s30 offen offset:512 sc1\n"
  "v_cvt_pk_bf16_f32 v76, v140, v141\n"
  "v_cvt_pk_bf16_f32 v77, v142, v143\n"
  "v_cvt_pk_bf16_f32 v78, v156, v157\n"
  "v_cvt_pk_bf16_f32 v79, v158, v159\n"
  "buffer_store_dwordx4 v[76:79], v2, s[12:15], s30 offen offset:768 sc1\n"
  "v_mov_b32 v128, v224\n"
  "v_mov_b32 v129, v225\n"
  "v_mov_b32 v130, v226\n"
  "v_mov_b32 v131, v227\n"
  "v_mov_b32 v132, v228\n"
  "v_mov_b32 v133, v229\n"
  "v_mov_b32 v134, v230\n"
  "v_mov_b32 v135, v231\n"
  "v_mov_b32 v136, v232\n"
  "v_mov_b32 v137, v233\n"
  "v_mov_b32 v138, v234\n"
  "v_mov_b32 v139, v235\n"
  "v_mov_b32 v140, v236\n"
  "v_mov_b32 v141, v237\n"
  "v_mov_b32 v142, v238\n"
  "v_mov_b32 v143, v239\n"
  "v_mov_b32 v144, v240\n"
  "v_mov_b32 v145, v241\n"
  "v_mov_b32 v146, v242\n"
  "v_mov_b32 v147, v243\n"
  "v_mov_b32 v148, v244\n"
  "v_mov_b32 v149, v245\n"
  "v_mov_b32 v150, v246\n"
  "v_mov_b32 v151, v247\n"
  "v_mov_b32 v152, v248\n"
  "v_mov_b32 v153, v249\n"
  "v_mov_b32 v154, v250\n"
  "v_mov_b32 v155, v251\n"
  "v_mov_b32 v156, v252\n"
  "v_mov_b32 v157, v253\n"
  "v_mov_b32 v158, v254\n"
  "v_mov_b32 v159, v255\n"
  "EX_END_%=:\n"
  "s_lshl_b32 s29, s41, 7\n"
  "s_add_u32 s47, s29, 0x3000\n"
  "s_lshl_b32 s30, s42, 2\n"
  "s_add_u32 s29, s47, s30\n"
  "s_waitcnt vmcnt(0)\n"
  "s_barrier\n"
  "s_cmp_lg_u32 s25, 0\n"
  "s_cbranch_scc1 PUBSKIP_27_%=\n"
  "v_mov_b32 v56, s29\n"
  "v_mov_b32 v57, s90\n"
  "s_mov_b64 exec, 1\n"
  "global_store_dword v56, v57, s[80:81] sc1\n"
  "s_mov_b64 exec, -1\n"
  "PUBSKIP_27_%=:\n"
  "s_add_u32 s16, s78, s46\n"
  "s_addc_u32 s17, s79, 0\n"
  "s_and_b32 s17, s17, 0xffff\n"
  "s_mov_b32 s18, 0x1f840\n"
  "s_mov_b32 s19, 0x20000\n"
  "v_lshlrev_b32 v100, 16, v108\n"
  "v_and_b32 v101, 0xffff0000, v108\n"
  "v_lshlrev_b32 v102, 16, v109\n"
  "v_and_b32 v103, 0xffff0000, v109\n"
  "v_lshlrev_b32 v104, 16, v110\n"
  "v_and_b32 v105, 0xffff0000, v110\n"
  "v_lshlrev_b32 v106, 16, v111\n"
  "v_and_b32 v107, 0xffff0000, v111\n"
  "s_mov_b32 s82, 0xbfb8aa3b\n"
  "s_mov_b32 s83, 0xbfb8aa3b\n"
  "s_mov_b32 s86, 0x3f800000\n"
  "s_mov_b32 s87, 0x3f800000\n"
  "s_lshl_b32 s31, s42, 14\n"
  "v_permlane16_swap_b32 v112, v114\n"
  "v_permlane16_swap_b32 v113, v115\n"
  "v_lshlrev_b32 v224, 16, v112\n"
  "v_and_b32 v225, 0xffff0000, v112\n"
  "v_lshlrev_b32 v226, 16, v113\n"
  "v_and_b32 v227, 0xffff0000, v113\n"
  "v_lshlrev_b32 v228, 16, v114\n"
  "v_and_b32 v229, 0xffff0000, v114\n"
  "v_lshlrev_b32 v230, 16, v115\n"
  "v_and_b32 v231, 0xffff0000, v115\n"
  "v_permlane16_swap_b32 v116, v118\n"
  "v_permlane16_swap_b32 v117, v119\n"
  "v_lshlrev_b32 v232, 16, v116\n"
  "v_and_b32 v233, 0xffff0000, v116\n"
  "v_lshlrev_b32 v234, 16, v117\n"
  "v_and_b32 v235, 0xffff0000, v117\n"
  "v_lshlrev_b32 v236, 16, v118\n"
  "v_and_b32 v237, 0xffff0000, v118\n"
  "v_lshlrev_b32 v238, 16, v119\n"
  "v_and_b32 v239, 0xffff0000, v119\n"
  "v_permlane16_swap_b32 v120, v122\n"
  "v_permlane16_swap_b32 v121, v123\n"
  "v_lshlrev_b32 v240, 16, v120\n"
  "v_and_b32 v241, 0xffff0000, v120\n"
  "v_lshlrev_b32 v242, 16, v121\n"
  "v_and_b32 v243, 0xffff0000, v121\n"
  "v_lshlrev_b32 v244, 16, v122\n"
  "v_and_b32 v245, 0xffff0000, v122\n"
  "v_lshlrev_b32 v246, 16, v123\n"
  "v_and_b32 v247, 0xffff0000, v123\n"
  "v_permlane16_swap_b32 v124, v126\n"
  "v_permlane16_swap_b32 v125, v127\n"
  "v_lshlrev_b32 v248, 16, v124\n"
  "v_and_b32 v249, 0xffff0000, v124\n"
  "v_lshlrev_b32 v250, 16, v125\n"
  "v_and_b32 v251, 0xffff0000, v125\n"
  "v_lshlrev_b32 v252, 16, v126\n"
  "v_and_b32 v253, 0xffff0000, v126\n"
  "v_lshlrev_b32 v254, 16, v127\n"
  "v_and_b32 v255, 0xffff0000, v127\n"
  "v_min_u32 v56, 3, v1\n"
  "v_lshlrev_b32 v56, 2, v56\n"
  "v_add_u32 v56, s47, v56\n"
  "s_mov_b32 s63, 0x30d40\n"
  "POLL_28_%=:\n"
  "global_load_dword v57, v56, s[80:81] sc1\n"
  "s_waitcnt vmcnt(0)\n"
  "v_cmp_ne_u32 vcc, s90, v57\n"
  "s_nop 1\n"
  "s_cmp_eq_u64 vcc, 0\n"
  "s_cbranch_scc1 POLLOK_29_%=\n"
  "s_sleep 1\n"
  "s_sub_u32 s63, s63, 1\n"
  "s_cmp_lg_u32 s63, 0\n"
  "s_cbranch_scc1 POLL_28_%=\n"
  "POLLOK_29_%=:\n"
  "s_xor_b32 s30, s42, 1\n"
  "s_lshl_b32 s30, s30, 16\n"
  "s_add_u32 s30, s30, s31\n"
  "buffer_load_dwordx4 v[160:163], v2, s[12:15], s30 offen sc1\n"
  "buffer_load_dwordx4 v[164:167], v2, s[12:15], s30 offen offset:256 sc1\n"
  "buffer_load_dwordx4 v[168:171], v2, s[12:15], s30 offen offset:512 sc1\n"
  "buffer_load_dwordx4 v[172:175], v2, s[12:15], s30 offen offset:768 sc1\n"
  "s_xor_b32 s30, s42, 2\n"
  "s_lshl_b32 s30, s30, 16\n"
  "s_add_u32 s30, s30, s31\n"
  "buffer_load_dwordx4 v[176:179], v2, s[12:15], s30 offen sc1\n"
  "buffer_load_dwordx4 v[180:183], v2, s[12:15], s30 offen offset:256 sc1\n"
  "buffer_load_dwordx4 v[184:187], v2, s[12:15], s30 offen offset:512 sc1\n"
  "buffer_load_dwordx4 v[188:191], v2, s[12:15], s30 offen offset:768 sc1\n"
  "s_xor_b32 s30, s42, 3\n"
  "s_lshl_b32 s30, s30, 16\n"
  "s_add_u32 s30, s30, s31\n"
  "buffer_load_dwordx4 v[192:195], v2, s[12:15], s30 offen sc1\n"
  "buffer_load_dwordx4 v[196:199], v2, s[12:15], s30 offen offset:256 sc1\n"
  "buffer_load_dwordx4 v[200:203], v2, s[12:15], s30 offen offset:512 sc1\n"
  "buffer_load_dwordx4 v[204:207], v2, s[12:15], s30 offen offset:768 sc1\n"
  "s_cmp_ge_u32 s42, 2\n"
  "s_cbranch_scc1 FIN_G_%=\n"
  "s_waitcnt vmcnt(0)\n"
  "v_lshlrev_b32 v208, 16, v160\n"
  "v_and_b32 v209, 0xffff0000, v160\n"
  "v_lshlrev_b32 v210, 16, v161\n"
  "v_and_b32 v211, 0xffff0000, v161\n"
  "v_lshlrev_b32 v212, 16, v176\n"
  "v_and_b32 v213, 0xffff0000, v176\n"
  "v_lshlrev_b32 v214, 16, v177\n"
  "v_and_b32 v215, 0xffff0000, v177\n"
  "v_lshlrev_b32 v216, 16, v192\n"
  "v_and_b32 v217, 0xffff0000, v192\n"
  "v_lshlrev_b32 v218, 16, v193\n"
  "v_and_b32 v219, 0xffff0000, v193\n"
  "v_pk_add_f32 v[128:129], v[128:129], v[208:209]\n"
  "v_pk_add_f32 v[130:131], v[130:131], v[210:211]\n"
  "v_pk_add_f32 v[212:213], v[212:213], v[216:217]\n"
  "v_pk_add_f32 v[214:215], v[214:215], v[218:219]\n"
  "v_pk_add_f32 v[212:213], v[212:213], v[100:101]\n"
  "v_pk_add_f32 v[214:215], v[214:215], v[102:103]\n"
  "v_pk_mul_f32 v[208:209], v[212:213], s[82:83]\n"
  "v_pk_mul_f32 v[210:211], v[214:215], s[82:83]\n"
  "v_exp_f32 v208, v208\n"
  "v_exp_f32 v209, v209\n"
  "v_exp_f32 v210, v210\n"
  "v_exp_f32 v211, v211\n"
  "v_pk_add_f32 v[208:209], v[208:209], s[86:87]\n"
  "v_pk_add_f32 v[210:211], v[210:211], s[86:87]\n"
  "v_rcp_f32 v208, v208\n"
  "v_rcp_f32 v209, v209\n"
  "v_rcp_f32 v210, v210\n"
  "v_rcp_f32 v211, v211\n"
  "v_pk_mul_f32 v[208:209], v[208:209], v[212:213]\n"
  "v_pk_mul_f32 v[210:211], v[210:211], v[214:215]\n"
  "v_pk_fma_f32 v[208:209], v[128:129], v[208:209], v[224:225]\n"
  "v_pk_fma_f32 v[210:211], v[130:131], v[210:211], v[226:227]\n"
  "v_cvt_pk_bf16_f32 v112, v208, v209\n"
  "v_cvt_pk_bf16_f32 v113, v210, v211\n"
  "v_lshlrev_b32 v208, 16, v162\n"
  "v_and_b32 v209, 0xffff0000, v162\n"
  "v_lshlrev_b32 v210, 16, v163\n"
  "v_and_b32 v211, 0xffff0000, v163\n"
  "v_lshlrev_b32 v212, 16, v178\n"
  "v_and_b32 v213, 0xffff0000, v178\n"
  "v_lshlrev_b32 v214, 16, v179\n"
  "v_and_b32 v215, 0xffff0000, v179\n"
  "v_lshlrev_b32 v216, 16, v194\n"
  "v_and_b32 v217, 0xffff0000, v194\n"
  "v_lshlrev_b32 v218, 16, v195\n"
  "v_and_b32 v219, 0xffff0000, v195\n"
  "v_pk_add_f32 v[144:145], v[144:145], v[208:209]\n"
  "v_pk_add_f32 v[146:147], v[146:147], v[210:211]\n"
  "v_pk_add_f32 v[212:213], v[212:213], v[216:217]\n"
  "v_pk_add_f32 v[214:215], v[214:215], v[218:219]\n"
  "v_pk_add_f32 v[212:213], v[212:213], v[104:105]\n"
  "v_pk_add_f32 v[214:215], v[214:215], v[106:107]\n"
  "v_pk_mul_f32 v[208:209], v[212:213], s[82:83]\n"
  "v_pk_mul_f32 v[210:211], v[214:215], s[82:83]\n"
  "v_exp_f32 v208, v208\n"
  "v_exp_f32 v209, v209\n"
  "v_exp_f32 v210, v210\n"
  "v_exp_f32 v211, v211\n"
  "v_pk_add_f32 v[208:209], v[208:209], s[86:87]\n"
  "v_pk_add_f32 v[210:211], v[210:211], s[86:87]\n"
  "v_rcp_f32 v208, v208\n"
  "v_rcp_f32 v209, v209\n"
  "v_rcp_f32 v210, v210\n"
  "v_rcp_f32 v211, v211\n"
  "v_pk_mul_f32 v[208:209], v[208:209], v[212:213]\n"
  "v_pk_mul_f32 v[210:211], v[210:211], v[214:215]\n"
  "v_pk_fma_f32 v[208:209], v[144:145], v[208:209], v[228:229]\n"
  "v_pk_fma_f32 v[210:211], v[146:147], v[210:211], v[230:231]\n"
  "v_cvt_pk_bf16_f32 v114, v208, v209\n"
  "v_cvt_pk_bf16_f32 v115, v210, v211\n"
  "s_nop 0\n"
  "v_permlane16_swap_b32 v112, v114\n"
  "v_permlane16_swap_b32 v113, v115\n"
  "s_mov_b32 s30, 0\n"
  "buffer_store_dwordx4 v[112:115], v48, s[16:19], s30 offen\n"
  "v_lshlrev_b32 v208, 16, v164\n"
  "v_and_b32 v209, 0xffff0000, v164\n"
  "v_lshlrev_b32 v210, 16, v165\n"
  "v_and_b32 v211, 0xffff0000, v165\n"
  "v_lshlrev_b32 v212, 16, v180\n"
  "v_and_b32 v213, 0xffff0000, v180\n"
  "v_lshlrev_b32 v214, 16, v181\n"
  "v_and_b32 v215, 0xffff0000, v181\n"
  "v_lshlrev_b32 v216, 16, v196\n"
  "v_and_b32 v217, 0xffff0000, v196\n"
  "v_lshlrev_b32 v218, 16, v197\n"
  "v_and_b32 v219, 0xffff0000, v197\n"
  "v_pk_add_f32 v[132:133], v[132:133], v[208:209]\n"
  "v_pk_add_f32 v[134:135], v[134:135], v[210:211]\n"
  "v_pk_add_f32 v[212:213], v[212:213], v[216:217]\n"
  "v_pk_add_f32 v[214:215], v[214:215], v[218:219]\n"
  "v_pk_add_f32 v[212:213], v[212:213], v[100:101]\n"
  "v_pk_add_f32 v[214:215], v[214:215], v[102:103]\n"
  "v_pk_mul_f32 v[208:209], v[212:213], s[82:83]\n"
  "v_pk_mul_f32 v[210:211], v[214:215], s[82:83]\n"
  "v_exp_f32 v208, v208\n"
  "v_exp_f32 v209, v209\n"
  "v_exp_f32 v210, v210\n"
  "v_exp_f32 v211, v211\n"
  "v_pk_add_f32 v[208:209], v[208:209], s[86:87]\n"
  "v_pk_add_f32 v[210:211], v[210:211], s[86:87]\n"
  "v_rcp_f32 v208, v208\n"
  "v_rcp_f32 v209, v209\n"
  "v_rcp_f32 v210, v210\n"
  "v_rcp_f32 v211, v211\n"
  "v_pk_mul_f32 v[208:209], v[208:209], v[212:213]\n"
  "v_pk_mul_f32 v[210:211], v[210:211], v[214:215]\n"
  "v_pk_fma_f32 v[208:209], v[132:133], v[208:209], v[232:233]\n"
  "v_pk_fma_f32 v[210:211], v[134:135], v[210:211], v[234:235]\n"
  "v_cvt_pk_bf16_f32 v116, v208, v209\n"
  "v_cvt_pk_bf16_f32 v117, v210, v211\n"
  "v_lshlrev_b32 v208, 16, v166\n"
  "v_and_b32 v209, 0xffff0000, v166\n"
  "v_lshlrev_b32 v210, 16, v167\n"
  "v_and_b32 v211, 0xffff0000, v167\n"
  "v_lshlrev_b32 v212, 16, v182\n"
  "v_and_b32 v213, 0xffff0000, v182\n"
  "v_lshlrev_b32 v214, 16, v183\n"
  "v_and_b32 v215, 0xffff0000, v183\n"
  "v_lshlrev_b32 v216, 16, v198\n"
  "v_and_b32 v217, 0xffff0000, v198\n"
  "v_lshlrev_b32 v218, 16, v199\n"
  "v_and_b32 v219, 0xffff0000, v199\n"
  "v_pk_add_f32 v[148:149], v[148:149], v[208:209]\n"
  "v_pk_add_f32 v[150:151], v[150:151], v[210:211]\n"
  "v_pk_add_f32 v[212:213], v[212:213], v[216:217]\n"
  "v_pk_add_f32 v[214:215], v[214:215], v[218:219]\n"
  "v_pk_add_f32 v[212:213], v[212:213], v[104:105]\n"
  "v_pk_add_f32 v[214:215], v[214:215], v[106:107]\n"
  "v_pk_mul_f32 v[208:209], v[212:213], s[82:83]\n"
  "v_pk_mul_f32 v[210:211], v[214:215], s[82:83]\n"
  "v_exp_f32 v208, v208\n"
  "v_exp_f32 v209, v209\n"
  "v_exp_f32 v210, v210\n"
  "v_exp_f32 v211, v211\n"
  "v_pk_add_f32 v[208:209], v[208:209], s[86:87]\n"
  "v_pk_add_f32 v[210:211], v[210:211], s[86:87]\n"
  "v_rcp_f32 v208, v208\n"
  "v_rcp_f32 v209, v209\n"
  "v_rcp_f32 v210, v210\n"
  "v_rcp_f32 v211, v211\n"
  "v_pk_mul_f32 v[208:209], v[208:209], v[212:213]\n"
  "v_pk_mul_f32 v[210:211], v[210:211], v[214:215]\n"
  "v_pk_fma_f32 v[208:209], v[148:149], v[208:209], v[236:237]\n"
  "v_pk_fma_f32 v[210:211], v[150:151], v[210:211], v[238:239]\n"
  "v_cvt_pk_bf16_f32 v118, v208, v209\n"
  "v_cvt_pk_bf16_f32 v119, v210, v211\n"
  "s_nop 0\n"
  "v_permlane16_swap_b32 v116, v118\n"
  "v_permlane16_swap_b32 v117, v119\n"
  "s_mov_b32 s30, 0x8000\n"
  "buffer_store_dwordx4 v[116:119], v48, s[16:19], s30 offen\n"
  "v_lshlrev_b32 v208, 16, v168\n"
  "v_and_b32 v209, 0xffff0000, v168\n"
  "v_lshlrev_b32 v210, 16, v169\n"
  "v_and_b32 v211, 0xffff0000, v169\n"
  "v_lshlrev_b32 v212, 16, v184\n"
  "v_and_b32 v213, 0xffff0000, v184\n"
  "v_lshlrev_b32 v214, 16, v185\n"
  "v_and_b32 v215, 0xffff0000, v185\n"
  "v_lshlrev_b32 v216, 16, v200\n"
  "v_and_b32 v217, 0xffff0000, v200\n"
  "v_lshlrev_b32 v218, 16, v201\n"
  "v_and_b32 v219, 0xffff0000, v201\n"
  "v_pk_add_f32 v[136:137], v[136:137], v[208:209]\n"
  "v_pk_add_f32 v[138:139], v[138:139], v[210:211]\n"
  "v_pk_add_f32 v[212:213], v[212:213], v[216:217]\n"
  "v_pk_add_f32 v[214:215], v[214:215], v[218:219]\n"
  "v_pk_add_f32 v[212:213], v[212:213], v[100:101]\n"
  "v_pk_add_f32 v[214:215], v[214:215], v[102:103]\n"
  "v_pk_mul_f32 v[208:209], v[212:213], s[82:83]\n"
  "v_pk_mul_f32 v[210:211], v[214:215], s[82:83]\n"
  "v_exp_f32 v208, v208\n"
  "v_exp_f32 v209, v209\n"
  "v_exp_f32 v210, v210\n"
  "v_exp_f32 v211, v211\n"
  "v_pk_add_f32 v[208:209], v[208:209], s[86:87]\n"
  "v_pk_add_f32 v[210:211], v[210:211], s[86:87]\n"
  "v_rcp_f32 v208, v208\n"
  "v_rcp_f32 v209, v209\n"
  "v_rcp_f32 v210, v210\n"
  "v_rcp_f32 v211, v211\n"
  "v_pk_mul_f32 v[208:209], v[208:209], v[212:213]\n"
  "v_pk_mul_f32 v[210:211], v[210:211], v[214:215]\n"
  "v_pk_fma_f32 v[208:209], v[136:137], v[208:209], v[240:241]\n"
  "v_pk_fma_f32 v[210:211], v[138:139], v[210:211], v[242:243]\n"
  "v_cvt_pk_bf16_f32 v120, v208, v209\n"
  "v_cvt_pk_bf16_f32 v121, v210, v211\n"
  "v_lshlrev_b32 v208, 16, v170\n"
  "v_and_b32 v209, 0xffff0000, v170\n"
  "v_lshlrev_b32 v210, 16, v171\n"
  "v_and_b32 v211, 0xffff0000, v171\n"
  "v_lshlrev_b32 v212, 16, v186\n"
  "v_and_b32 v213, 0xffff0000, v186\n"
  "v_lshlrev_b32 v214, 16, v187\n"
  "v_and_b32 v215, 0xffff0000, v187\n"
  "v_lshlrev_b32 v216, 16, v202\n"
  "v_and_b32 v217, 0xffff0000, v202\n"
  "v_lshlrev_b32 v218, 16, v203\n"
  "v_and_b32 v219, 0xffff0000, v203\n"
  "v_pk_add_f32 v[152:153], v[152:153], v[208:209]\n"
  "v_pk_add_f32 v[154:155], v[154:155], v[210:211]\n"
  "v_pk_add_f32 v[212:213], v[212:213], v[216:217]\n"
  "v_pk_add_f32 v[214:215], v[214:215], v[218:219]\n"
  "v_pk_add_f32 v[212:213], v[212:213], v[104:105]\n"
  "v_pk_add_f32 v[214:215], v[214:215], v[106:107]\n"
  "v_pk_mul_f32 v[208:209], v[212:213], s[82:83]\n"
  "v_pk_mul_f32 v[210:211], v[214:215], s[82:83]\n"
  "v_exp_f32 v208, v208\n"
  "v_exp_f32 v209, v209\n"
  "v_exp_f32 v210, v210\n"
  "v_exp_f32 v211, v211\n"
  "v_pk_add_f32 v[208:209], v[208:209], s[86:87]\n"
  "v_pk_add_f32 v[210:211], v[210:211], s[86:87]\n"
  "v_rcp_f32 v208, v208\n"
  "v_rcp_f32 v209, v209\n"
  "v_rcp_f32 v210, v210\n"
  "v_rcp_f32 v211, v211\n"
  "v_pk_mul_f32 v[208:209], v[208:209], v[212:213]\n"
  "v_pk_mul_f32 v[210:211], v[210:211], v[214:215]\n"
  "v_pk_fma_f32 v[208:209], v[152:153], v[208:209], v[244:245]\n"
  "v_pk_fma_f32 v[210:211], v[154:155], v[210:211], v[246:247]\n"
  "v_cvt_pk_bf16_f32 v122, v208, v209\n"
  "v_cvt_pk_bf16_f32 v123, v210, v211\n"
  "s_nop 0\n"
  "v_permlane16_swap_b32 v120, v122\n"
  "v_permlane16_swap_b32 v121, v123\n"
  "s_mov_b32 s30, 0x10000\n"
  "buffer_store_dwordx4 v[120:123], v48, s[16:19], s30 offen\n"
  "v_lshlrev_b32 v208, 16, v172\n"
  "v_and_b32 v209, 0xffff0000, v172\n"
  "v_lshlrev_b32 v210, 16, v173\n"
  "v_and_b32 v211, 0xffff0000, v173\n"
  "v_lshlrev_b32 v212, 16, v188\n"
  "v_and_b32 v213, 0xffff0000, v188\n"
  "v_lshlrev_b32 v214, 16, v189\n"
  "v_and_b32 v215, 0xffff0000, v189\n"
  "v_lshlrev_b32 v216, 16, v204\n"
  "v_and_b32 v217, 0xffff0000, v204\n"
  "v_lshlrev_b32 v218, 16, v205\n"
  "v_and_b32 v219, 0xffff0000, v205\n"
  "v_pk_add_f32 v[140:141], v[140:141], v[208:209]\n"
  "v_pk_add_f32 v[142:143], v[142:143], v[210:211]\n"
  "v_pk_add_f32 v[212:213], v[212:213], v[216:217]\n"
  "v_pk_add_f32 v[214:215], v[214:215], v[218:219]\n"
  "v_pk_add_f32 v[212:213], v[212:213], v[100:101]\n"
  "v_pk_add_f32 v[214:215], v[214:215], v[102:103]\n"
  "v_pk_mul_f32 v[208:209], v[212:213], s[82:83]\n"
  "v_pk_mul_f32 v[210:211], v[214:215], s[82:83]\n"
  "v_exp_f32 v208, v208\n"
  "v_exp_f32 v209, v209\n"
  "v_exp_f32 v210, v210\n"
  "v_exp_f32 v211, v211\n"
  "v_pk_add_f32 v[208:209], v[208:209], s[86:87]\n"
  "v_pk_add_f32 v[210:211], v[210:211], s[86:87]\n"
  "v_rcp_f32 v208, v208\n"
  "v_rcp_f32 v209, v209\n"
  "v_rcp_f32 v210, v210\n"
  "v_rcp_f32 v211, v211\n"
  "v_pk_mul_f32 v[208:209], v[208:209], v[212:213]\n"
  "v_pk_mul_f32 v[210:211], v[210:211], v[214:215]\n"
  "v_pk_fma_f32 v[208:209], v[140:141], v[208:209], v[248:249]\n"
  "v_pk_fma_f32 v[210:211], v[142:143], v[210:211], v[250:251]\n"
  "v_cvt_pk_bf16_f32 v124, v208, v209\n"
  "v_cvt_pk_bf16_f32 v125, v210, v211\n"
  "v_lshlrev_b32 v208, 16, v174\n"
  "v_and_b32 v209, 0xffff0000, v174\n"
  "v_lshlrev_b32 v210, 16, v175\n"
  "v_and_b32 v211, 0xffff0000, v175\n"
  "v_lshlrev_b32 v212, 16, v190\n"
  "v_and_b32 v213, 0xffff0000, v190\n"
  "v_lshlrev_b32 v214, 16, v191\n"
  "v_and_b32 v215, 0xffff0000, v191\n"
  "v_lshlrev_b32 v216, 16, v206\n"
  "v_and_b32 v217, 0xffff0000, v206\n"
  "v_lshlrev_b32 v218, 16, v207\n"
  "v_and_b32 v219, 0xffff0000, v207\n"
  "v_pk_add_f32 v[156:157], v[156:157], v[208:209]\n"
  "v_pk_add_f32 v[158:159], v[158:159], v[210:211]\n"
  "v_pk_add_f32 v[212:213], v[212:213], v[216:217]\n"
  "v_pk_add_f32 v[214:215], v[214:215], v[218:219]\n"
  "v_pk_add_f32 v[212:213], v[212:213], v[104:105]\n"
  "v_pk_add_f32 v[214:215], v[214:215], v[106:107]\n"
  "v_pk_mul_f32 v[208:209], v[212:213], s[82:83]\n"
  "v_pk_mul_f32 v[210:211], v[214:215], s[82:83]\n"
  "v_exp_f32 v208, v208\n"
  "v_exp_f32 v209, v209\n"
  "v_exp_f32 v210, v210\n"
  "v_exp_f32 v211, v211\n"
  "v_pk_add_f32 v[208:209], v[208:209], s[86:87]\n"
  "v_pk_add_f32 v[210:211], v[210:211], s[86:87]\n"
  "v_rcp_f32 v208, v208\n"
  "v_rcp_f32 v209, v209\n"
  "v_rcp_f32 v210, v210\n"
  "v_rcp_f32 v211, v211\n"
  "v_pk_mul_f32 v[208:209], v[208:209], v[212:213]\n"
  "v_pk_mul_f32 v[210:211], v[210:211], v[214:215]\n"
  "v_pk_fma_f32 v[208:209], v[156:157], v[208:209], v[252:253]\n"
  "v_pk_fma_f32 v[210:211], v[158:159], v[210:211], v[254:255]\n"
  "v_cvt_pk_bf16_f32 v126, v208, v209\n"
  "v_cvt_pk_bf16_f32 v127, v210, v211\n"
  "s_nop 0\n"
  "v_permlane16_swap_b32 v124, v126\n"
  "v_permlane16_swap_b32 v125, v127\n"
  "s_mov_b32 s30, 0x18000\n"
  "buffer_store_dwordx4 v[124:127], v48, s[16:19], s30 offen\n"
  "s_branch FIN_END_%=\n"
  "FIN_G_%=:\n"
  "s_waitcnt vmcnt(0)\n"
  "v_lshlrev_b32 v208, 16, v160\n"
  "v_and_b32 v209, 0xffff0000, v160\n"
  "v_lshlrev_b32 v210, 16, v161\n"
  "v_and_b32 v211, 0xffff0000, v161\n"
  "v_lshlrev_b32 v212, 16, v176\n"
  "v_and_b32 v213, 0xffff0000, v176\n"
  "v_lshlrev_b32 v214, 16, v177\n"
  "v_and_b32 v215, 0xffff0000, v177\n"
  "v_lshlrev_b32 v216, 16, v192\n"
  "v_and_b32 v217, 0xffff0000, v192\n"
  "v_lshlrev_b32 v218, 16, v193\n"
  "v_and_b32 v219, 0xffff0000, v193\n"
  "v_pk_add_f32 v[128:129], v[128:129], v[208:209]\n"
  "v_pk_add_f32 v[130:131], v[130:131], v[210:211]\n"
  "v_pk_add_f32 v[212:213], v[212:213], v[216:217]\n"
  "v_pk_add_f32 v[214:215], v[214:215], v[218:219]\n"
  "v_pk_add_f32 v[128:129], v[128:129], v[100:101]\n"
  "v_pk_add_f32 v[130:131], v[130:131], v[102:103]\n"
  "v_pk_mul_f32 v[208:209], v[128:129], s[82:83]\n"
  "v_pk_mul_f32 v[210:211], v[130:131], s[82:83]\n"
  "v_exp_f32 v208, v208\n"
  "v_exp_f32 v209, v209\n"
  "v_exp_f32 v210, v210\n"
  "v_exp_f32 v211, v211\n"
  "v_pk_add_f32 v[208:209], v[208:209], s[86:87]\n"
  "v_pk_add_f32 v[210:211], v[210:211], s[86:87]\n"
  "v_rcp_f32 v208, v208\n"
  "v_rcp_f32 v209, v209\n"
  "v_rcp_f32 v210, v210\n"
  "v_rcp_f32 v211, v211\n"
  "v_pk_mul_f32 v[208:209], v[208:209], v[128:129]\n"
  "v_pk_mul_f32 v[210:211], v[210:211], v[130:131]\n"
  "v_pk_fma_f32 v[208:209], v[212:213], v[208:209], v[224:225]\n"
  "v_pk_fma_f32 v[210:211], v[214:215], v[210:211], v[226:227]\n"
  "v_cvt_pk_bf16_f32 v112, v208, v209\n"
  "v_cvt_pk_bf16_f32 v113, v210, v211\n"
  "v_lshlrev_b32 v208, 16, v162\n"
  "v_and_b32 v209, 0xffff0000, v162\n"
  "v_lshlrev_b32 v210, 16, v163\n"
  "v_and_b32 v211, 0xffff0000, v163\n"
  "v_lshlrev_b32 v212, 16, v178\n"
  "v_and_b32 v213, 0xffff0000, v178\n"
  "v_lshlrev_b32 v214, 16, v179\n"
  "v_and_b32 v215, 0xffff0000, v179\n"
  "v_lshlrev_b32 v216, 16, v194\n"
  "v_and_b32 v217, 0xffff0000, v194\n"
  "v_lshlrev_b32 v218, 16, v195\n"
  "v_and_b32 v219, 0xffff0000, v195\n"
  "v_pk_add_f32 v[144:145], v[144:145], v[208:209]\n"
  "v_pk_add_f32 v[146:147], v[146:147], v[210:211]\n"
  "v_pk_add_f32 v[212:213], v[212:213], v[216:217]\n"
  "v_pk_add_f32 v[214:215], v[214:215], v[218:219]\n"
  "v_pk_add_f32 v[144:145], v[144:145], v[104:105]\n"
  "v_pk_add_f32 v[146:147], v[146:147], v[106:107]\n"
  "v_pk_mul_f32 v[208:209], v[144:145], s[82:83]\n"
  "v_pk_mul_f32 v[210:211], v[146:147], s[82:83]\n"
  "v_exp_f32 v208, v208\n"
  "v_exp_f32 v209, v209\n"
  "v_exp_f32 v210, v210\n"
  "v_exp_f32 v211, v211\n"
  "v_pk_add_f32 v[208:209], v[208:209], s[86:87]\n"
  "v_pk_add_f32 v[210:211], v[210:211], s[86:87]\n"
  "v_rcp_f32 v208, v208\n"
  "v_rcp_f32 v209, v209\n"
  "v_rcp_f32 v210, v210\n"
  "v_rcp_f32 v211, v211\n"
  "v_pk_mul_f32 v[208:209], v[208:209], v[144:145]\n"
  "v_pk_mul_f32 v[210:211], v[210:211], v[146:147]\n"
  "v_pk_fma_f32 v[208:209], v[212:213], v[208:209], v[228:229]\n"
  "v_pk_fma_f32 v[210:211], v[214:215], v[210:211], v[230:231]\n"
  "v_cvt_pk_bf16_f32 v114, v208, v209\n"
  "v_cvt_pk_bf16_f32 v115, v210, v211\n"
  "s_nop 0\n"
  "v_permlane16_swap_b32 v112, v114\n"
  "v_permlane16_swap_b32 v113, v115\n"
  "s_mov_b32 s30, 0\n"
  "buffer_store_dwordx4 v[112:115], v48, s[16:19], s30 offen\n"
  "v_lshlrev_b32 v208, 16, v164\n"
  "v_and_b32 v209, 0xffff0000, v164\n"
  "v_lshlrev_b32 v210, 16, v165\n"
  "v_and_b32 v211, 0xffff0000, v165\n"
  "v_lshlrev_b32 v212, 16, v180\n"
  "v_and_b32 v213, 0xffff0000, v180\n"
  "v_lshlrev_b32 v214, 16, v181\n"
  "v_and_b32 v215, 0xffff0000, v181\n"
  "v_lshlrev_b32 v216, 16, v196\n"
  "v_and_b32 v217, 0xffff0000, v196\n"
  "v_lshlrev_b32 v218, 16, v197\n"
  "v_and_b32 v219, 0xffff0000, v197\n"
  "v_pk_add_f32 v[132:133], v[132:133], v[208:209]\n"
  "v_pk_add_f32 v[134:135], v[134:135], v[210:211]\n"
  "v_pk_add_f32 v[212:213], v[212:213], v[216:217]\n"
  "v_pk_add_f32 v[214:215], v[214:215], v[218:219]\n"
  "v_pk_add_f32 v[132:133], v[132:133], v[100:101]\n"
  "v_pk_add_f32 v[134:135], v[134:135], v[102:103]\n"
  "v_pk_mul_f32 v[208:209], v[132:133], s[82:83]\n"
  "v_pk_mul_f32 v[210:211], v[134:135], s[82:83]\n"
  "v_exp_f32 v208, v208\n"
  "v_exp_f32 v209, v209\n"
  "v_exp_f32 v210, v210\n"
  "v_exp_f32 v211, v211\n"
  "v_pk_add_f32 v[208:209], v[208:209], s[86:87]\n"
  "v_pk_add_f32 v[210:211], v[210:211], s[86:87]\n"
  "v_rcp_f32 v208, v208\n"
  "v_rcp_f32 v209, v209\n"
  "v_rcp_f32 v210, v210\n"
  "v_rcp_f32 v211, v211\n"
  "v_pk_mul_f32 v[208:209], v[208:209], v[132:133]\n"
  "v_pk_mul_f32 v[210:211], v[210:211], v[134:135]\n"
  "v_pk_fma_f32 v[208:209], v[212:213], v[208:209], v[232:233]\n"
  "v_pk_fma_f32 v[210:211], v[214:215], v[210:211], v[234:235]\n"
  "v_cvt_pk_bf16_f32 v116, v208, v209\n"
  "v_cvt_pk_bf16_f32 v117, v210, v211\n"
  "v_lshlrev_b32 v208, 16, v166\n"
  "v_and_b32 v209, 0xffff0000, v166\n"
  "v_lshlrev_b32 v210, 16, v167\n"
  "v_and_b32 v211, 0xffff0000, v167\n"
  "v_lshlrev_b32 v212, 16, v182\n"
  "v_and_b32 v213, 0xffff0000, v182\n"
  "v_lshlrev_b32 v214, 16, v183\n"
  "v_and_b32 v215, 0xffff0000, v183\n"
  "v_lshlrev_b32 v216, 16, v198\n"
  "v_and_b32 v217, 0xffff0000, v198\n"
  "v_lshlrev_b32 v218, 16, v199\n"
  "v_and_b32 v219, 0xffff0000, v199\n"
  "v_pk_add_f32 v[148:149], v[148:149], v[208:209]\n"
  "v_pk_add_f32 v[150:151], v[150:151], v[210:211]\n"
  "v_pk_add_f32 v[212:213], v[212:213], v[216:217]\n"
  "v_pk_add_f32 v[214:215], v[214:215], v[218:219]\n"
  "v_pk_add_f32 v[148:149], v[148:149], v[104:105]\n"
  "v_pk_add_f32 v[150:151], v[150:151], v[106:107]\n"
  "v_pk_mul_f32 v[208:209], v[148:149], s[82:83]\n"
  "v_pk_mul_f32 v[210:211], v[150:151], s[82:83]\n"
  "v_exp_f32 v208, v208\n"
  "v_exp_f32 v209, v209\n"
  "v_exp_f32 v210, v210\n"
  "v_exp_f32 v211, v211\n"
  "v_pk_add_f32 v[208:209], v[208:209], s[86:87]\n"
  "v_pk_add_f32 v[210:211], v[210:211], s[86:87]\n"
  "v_rcp_f32 v208, v208\n"
  "v_rcp_f32 v209, v209\n"
  "v_rcp_f32 v210, v210\n"
  "v_rcp_f32 v211, v211\n"
  "v_pk_mul_f32 v[208:209], v[208:209], v[148:149]\n"
  "v_pk_mul_f32 v[210:211], v[210:211], v[150:151]\n"
  "v_pk_fma_f32 v[208:209], v[212:213], v[208:209], v[236:237]\n"
  "v_pk_fma_f32 v[210:211], v[214:215], v[210:211], v[238:239]\n"
  "v_cvt_pk_bf16_f32 v118, v208, v209\n"
  "v_cvt_pk_bf16_f32 v119, v210, v211\n"
  "s_nop 0\n"
  "v_permlane16_swap_b32 v116, v118\n"
  "v_permlane16_swap_b32 v117, v119\n"
  "s_mov_b32 s30, 0x8000\n"
  "buffer_store_dwordx4 v[116:119], v48, s[16:19], s30 offen\n"
  "v_lshlrev_b32 v208, 16, v168\n"
  "v_and_b32 v209, 0xffff0000, v168\n"
  "v_lshlrev_b32 v210, 16, v169\n"
  "v_and_b32 v211, 0xffff0000, v169\n"
  "v_lshlrev_b32 v212, 16, v184\n"
  "v_and_b32 v213, 0xffff0000, v184\n"
  "v_lshlrev_b32 v214, 16, v185\n"
  "v_and_b32 v215, 0xffff0000, v185\n"
  "v_lshlrev_b32 v216, 16, v200\n"
  "v_and_b32 v217, 0xffff0000, v200\n"
  "v_lshlrev_b32 v218, 16, v201\n"
  "v_and_b32 v219, 0xffff0000, v201\n"
  "v_pk_add_f32 v[136:137], v[136:137], v[208:209]\n"
  "v_pk_add_f32 v[138:139], v[138:139], v[210:211]\n"
  "v_pk_add_f32 v[212:213], v[212:213], v[216:217]\n"
  "v_pk_add_f32 v[214:215], v[214:215], v[218:219]\n"
  "v_pk_add_f32 v[136:137], v[136:137], v[100:101]\n"
  "v_pk_add_f32 v[138:139], v[138:139], v[102:103]\n"
  "v_pk_mul_f32 v[208:209], v[136:137], s[82:83]\n"
  "v_pk_mul_f32 v[210:211], v[138:139], s[82:83]\n"
  "v_exp_f32 v208, v208\n"
  "v_exp_f32 v209, v209\n"
  "v_exp_f32 v210, v210\n"
  "v_exp_f32 v211, v211\n"
  "v_pk_add_f32 v[208:209], v[208:209], s[86:87]\n"
  "v_pk_add_f32 v[210:211], v[210:211], s[86:87]\n"
  "v_rcp_f32 v208, v208\n"
  "v_rcp_f32 v209, v209\n"
  "v_rcp_f32 v210, v210\n"
  "v_rcp_f32 v211, v211\n"
  "v_pk_mul_f32 v[208:209], v[208:209], v[136:137]\n"
  "v_pk_mul_f32 v[210:211], v[210:211], v[138:139]\n"
  "v_pk_fma_f32 v[208:209], v[212:213], v[208:209], v[240:241]\n"
  "v_pk_fma_f32 v[210:211], v[214:215], v[210:211], v[242:243]\n"
  "v_cvt_pk_bf16_f32 v120, v208, v209\n"
  "v_cvt_pk_bf16_f32 v121, v210, v211\n"
  "v_lshlrev_b32 v208, 16, v170\n"
  "v_and_b32 v209, 0xffff0000, v170\n"
  "v_lshlrev_b32 v210, 16, v171\n"
  "v_and_b32 v211, 0xffff0000, v171\n"
  "v_lshlrev_b32 v212, 16, v186\n"
  "v_and_b32 v213, 0xffff0000, v186\n"
  "v_lshlrev_b32 v214, 16, v187\n"
  "v_and_b32 v215, 0xffff0000, v187\n"
  "v_lshlrev_b32 v216, 16, v202\n"
  "v_and_b32 v217, 0xffff0000, v202\n"
  "v_lshlrev_b32 v218, 16, v203\n"
  "v_and_b32 v219, 0xffff0000, v203\n"
  "v_pk_add_f32 v[152:153], v[152:153], v[208:209]\n"
  "v_pk_add_f32 v[154:155], v[154:155], v[210:211]\n"
  "v_pk_add_f32 v[212:213], v[212:213], v[216:217]\n"
  "v_pk_add_f32 v[214:215], v[214:215], v[218:219]\n"
  "v_pk_add_f32 v[152:153], v[152:153], v[104:105]\n"
  "v_pk_add_f32 v[154:155], v[154:155], v[106:107]\n"
  "v_pk_mul_f32 v[208:209], v[152:153], s[82:83]\n"
  "v_pk_mul_f32 v[210:211], v[154:155], s[82:83]\n"
  "v_exp_f32 v208, v208\n"
  "v_exp_f32 v209, v209\n"
  "v_exp_f32 v210, v210\n"
  "v_exp_f32 v211, v211\n"
  "v_pk_add_f32 v[208:209], v[208:209], s[86:87]\n"
  "v_pk_add_f32 v[210:211], v[210:211], s[86:87]\n"
  "v_rcp_f32 v208, v208\n"
  "v_rcp_f32 v209, v209\n"
  "v_rcp_f32 v210, v210\n"
  "v_rcp_f32 v211, v211\n"
  "v_pk_mul_f32 v[208:209], v[208:209], v[152:153]\n"
  "v_pk_mul_f32 v[210:211], v[210:211], v[154:155]\n"
  "v_pk_fma_f32 v[208:209], v[212:213], v[208:209], v[244:245]\n"
  "v_pk_fma_f32 v[210:211], v[214:215], v[210:211], v[246:247]\n"
  "v_cvt_pk_bf16_f32 v122, v208, v209\n"
  "v_cvt_pk_bf16_f32 v123, v210, v211\n"
  "s_nop 0\n"
  "v_permlane16_swap_b32 v120, v122\n"
  "v_permlane16_swap_b32 v121, v123\n"
  "s_mov_b32 s30, 0x10000\n"
  "buffer_store_dwordx4 v[120:123], v48, s[16:19], s30 offen\n"
  "v_lshlrev_b32 v208, 16, v172\n"
  "v_and_b32 v209, 0xffff0000, v172\n"
  "v_lshlrev_b32 v210, 16, v173\n"
  "v_and_b32 v211, 0xffff0000, v173\n"
  "v_lshlrev_b32 v212, 16, v188\n"
  "v_and_b32 v213, 0xffff0000, v188\n"
  "v_lshlrev_b32 v214, 16, v189\n"
  "v_and_b32 v215, 0xffff0000, v189\n"
  "v_lshlrev_b32 v216, 16, v204\n"
  "v_and_b32 v217, 0xffff0000, v204\n"
  "v_lshlrev_b32 v218, 16, v205\n"
  "v_and_b32 v219, 0xffff0000, v205\n"
  "v_pk_add_f32 v[140:141], v[140:141], v[208:209]\n"
  "v_pk_add_f32 v[142:143], v[142:143], v[210:211]\n"
  "v_pk_add_f32 v[212:213], v[212:213], v[216:217]\n"
  "v_pk_add_f32 v[214:215], v[214:215], v[218:219]\n"
  "v_pk_add_f32 v[140:141], v[140:141], v[100:101]\n"
  "v_pk_add_f32 v[142:143], v[142:143], v[102:103]\n"
  "v_pk_mul_f32 v[208:209], v[140:141], s[82:83]\n"
  "v_pk_mul_f32 v[210:211], v[142:143], s[82:83]\n"
  "v_exp_f32 v208, v208\n"
  "v_exp_f32 v209, v209\n"
  "v_exp_f32 v210, v210\n"
  "v_exp_f32 v211, v211\n"
  "v_pk_add_f32 v[208:209], v[208:209], s[86:87]\n"
  "v_pk_add_f32 v[210:211], v[210:211], s[86:87]\n"
  "v_rcp_f32 v208, v208\n"
  "v_rcp_f32 v209, v209\n"
  "v_rcp_f32 v210, v210\n"
  "v_rcp_f32 v211, v211\n"
  "v_pk_mul_f32 v[208:209], v[208:209], v[140:141]\n"
  "v_pk_mul_f32 v[210:211], v[210:211], v[142:143]\n"
  "v_pk_fma_f32 v[208:209], v[212:213], v[208:209], v[248:249]\n"
  "v_pk_fma_f32 v[210:211], v[214:215], v[210:211], v[250:251]\n"
  "v_cvt_pk_bf16_f32 v124, v208, v209\n"
  "v_cvt_pk_bf16_f32 v125, v210, v211\n"
  "v_lshlrev_b32 v208, 16, v174\n"
  "v_and_b32 v209, 0xffff0000, v174\n"
  "v_lshlrev_b32 v210, 16, v175\n"
  "v_and_b32 v211, 0xffff0000, v175\n"
  "v_lshlrev_b32 v212, 16, v190\n"
  "v_and_b32 v213, 0xffff0000, v190\n"
  "v_lshlrev_b32 v214, 16, v191\n"
  "v_and_b32 v215, 0xffff0000, v191\n"
  "v_lshlrev_b32 v216, 16, v206\n"
  "v_and_b32 v217, 0xffff0000, v206\n"
  "v_lshlrev_b32 v218, 16, v207\n"
  "v_and_b32 v219, 0xffff0000, v207\n"
  "v_pk_add_f32 v[156:157], v[156:157], v[208:209]\n"
  "v_pk_add_f32 v[158:159], v[158:159], v[210:211]\n"
  "v_pk_add_f32 v[212:213], v[212:213], v[216:217]\n"
  "v_pk_add_f32 v[214:215], v[214:215], v[218:219]\n"
  "v_pk_add_f32 v[156:157], v[156:157], v[104:105]\n"
  "v_pk_add_f32 v[158:159], v[158:159], v[106:107]\n"
  "v_pk_mul_f32 v[208:209], v[156:157], s[82:83]\n"
  "v_pk_mul_f32 v[210:211], v[158:159], s[82:83]\n"
  "v_exp_f32 v208, v208\n"
  "v_exp_f32 v209, v209\n"
  "v_exp_f32 v210, v210\n"
  "v_exp_f32 v211, v211\n"
  "v_pk_add_f32 v[208:209], v[208:209], s[86:87]\n"
  "v_pk_add_f32 v[210:211], v[210:211], s[86:87]\n"
  "v_rcp_f32 v208, v208\n"
  "v_rcp_f32 v209, v209\n"
  "v_rcp_f32 v210, v210\n"
  "v_rcp_f32 v211, v211\n"
  "v_pk_mul_f32 v[208:209], v[208:209], v[156:157]\n"
  "v_pk_mul_f32 v[210:211], v[210:211], v[158:159]\n"
  "v_pk_fma_f32 v[208:209], v[212:213], v[208:209], v[252:253]\n"
  "v_pk_fma_f32 v[210:211], v[214:215], v[210:211], v[254:255]\n"
  "v_cvt_pk_bf16_f32 v126, v208, v209\n"
  "v_cvt_pk_bf16_f32 v127, v210, v211\n"
  "s_nop 0\n"
  "v_permlane16_swap_b32 v124, v126\n"
  "v_permlane16_swap_b32 v125, v127\n"
  "s_mov_b32 s30, 0x18000\n"
  "buffer_store_dwordx4 v[124:127], v48, s[16:19], s30 offen\n"
  "FIN_END_%=:\n"
  "s_lshl_b32 s29, s26, 0\n"
  "s_or_b32 s29, s29, s27\n"
  "s_or_b32 s29, s29, s25\n"
  "s_cmp_lg_u32 s29, 0\n"
  "s_cbranch_scc1 EP_SKIP_%=\n"
  "v_lshlrev_b32 v54, 4, v1\n"
  "v_add_u32 v54, 0x400, v54\n"
  "s_mov_b32 s63, 0x30d40\n"
  "EP_POLL_%=:\n"
  "global_load_dwordx4 v[64:67], v54, s[80:81] sc1\n"
  "s_waitcnt vmcnt(0)\n"
  "v_xor_b32 v64, s90, v64\n"
  "v_xor_b32 v65, s90, v65\n"
  "v_xor_b32 v66, s90, v66\n"
  "v_xor_b32 v67, s90, v67\n"
  "v_or3_b32 v64, v64, v65, v66\n"
  "v_or_b32 v64, v64, v67\n"
  "v_cmp_ne_u32 vcc, 0, v64\n"
  "s_nop 1\n"
  "s_cmp_eq_u64 vcc, 0\n"
  "s_cbranch_scc1 EP_OK_%=\n"
  "s_sleep 1\n"
  "s_sub_u32 s63, s63, 1\n"
  "s_cmp_lg_u32 s63, 0\n"
  "s_cbranch_scc1 EP_POLL_%=\n"
  "EP_OK_%=:\n"
  "v_mov_b32 v56, 0\n"
  "v_mov_b32 v57, s90\n"
  "s_mov_b64 exec, 1\n"
  "global_store_dword v56, v57, s[80:81] sc1\n"
  "s_mov_b64 exec, -1\n"
  "EP_SKIP_%=:\n"
  "s_waitcnt vmcnt(0)\n"
  "s_endpgm\n"
  "GEMM_FIRST_%=:\n"
  "s_nop 4\n"
  "v_mfma_f32_16x16x32_bf16 v[128:131], a[0:3], a[64:67], 0\n"
  "ds_read_b64_tr_b16 a[32:33], v7 offset:8192\n"
  "v_mfma_f32_16x16x32_bf16 v[132:135], a[0:3], a[68:71], 0\n"
  "ds_read_b64_tr_b16 a[34:35], v7 offset:12288\n"
  "v_mfma_f32_16x16x32_bf16 v[136:139], a[0:3], a[72:75], 0\n"
  "ds_read_b64_tr_b16 a[36:37], v8 offset:8192\n"
  "v_mfma_f32_16x16x32_bf16 v[140:143], a[0:3], a[76:79], 0\n"
  "ds_read_b64_tr_b16 a[38:39], v8 offset:12288\n"
  "v_mfma_f32_16x16x32_bf16 v[144:147], a[4:7], a[64:67], 0\n"
  "ds_read_b64_tr_b16 a[40:41], v9 offset:8192\n"
  "buffer_load_dwordx4 a[176:179], v2, s[4:7], s21 offen sc1\n"
  "v_mfma_f32_16x16x32_bf16 v[148:151], a[4:7], a[68:71], 0\n"
  "ds_read_b64_tr_b16 a[42:43], v9 offset:12288\n"
  "v_mfma_f32_16x16x32_bf16 v[152:155], a[4:7], a[72:75], 0\n"
  "ds_read_b64_tr_b16 a[44:45], v10 offset:8192\n"
  "v_mfma_f32_16x16x32_bf16 v[156:159], a[4:7], a[76:79], 0\n"
  "ds_read_b64_tr_b16 a[46:47], v10 offset:12288\n"
  "v_mfma_f32_16x16x32_bf16 v[160:163], a[8:11], a[64:67], 0\n"
  "ds_read_b64_tr_b16 a[48:49], v11 offset:8192\n"
  "v_mfma_f32_16x16x32_bf16 v[164:167], a[8:11], a[68:71], 0\n"
  "ds_read_b64_tr_b16 a[50:51], v11 offset:12288\n"
  "v_mfma_f32_16x16x32_bf16 v[168:171], a[8:11], a[72:75], 0\n"
  "ds_read_b64_tr_b16 a[52:53], v12 offset:8192\n"
  "v_mfma_f32_16x16x32_bf16 v[172:175], a[8:11], a[76:79], 0\n"
  "ds_read_b64_tr_b16 a[54:55], v12 offset:12288\n"
  "v_mfma_f32_16x16x32_bf16 v[176:179], a[12:15], a[64:67], 0\n"
  "ds_read_b64_tr_b16 a[56:57], v13 offset:8192\n"
  "buffer_load_dwordx4 a[180:183], v2, s[4:7], s21 offen offset:256 sc1\n"
  "v_mfma_f32_16x16x32_bf16 v[180:183], a[12:15], a[68:71], 0\n"
  "ds_read_b64_tr_b16 a[58:59], v13 offset:12288\n"
  "v_mfma_f32_16x16x32_bf16 v[184:187], a[12:15], a[72:75], 0\n"
  "ds_read_b64_tr_b16 a[60:61], v14 offset:8192\n"
  "v_mfma_f32_16x16x32_bf16 v[188:191], a[12:15], a[76:79], 0\n"
  "ds_read_b64_tr_b16 a[62:63], v14 offset:12288\n"
  "v_mfma_f32_16x16x32_bf16 v[192:195], a[16:19], a[64:67], 0\n"
  "buffer_store_dwordx4 v[64:67], v2, s[12:15], s91 offen sc1\n"
  "v_mfma_f32_16x16x32_bf16 v[196:199], a[16:19], a[68:71], 0\n"
  "v_mfma_f32_16x16x32_bf16 v[200:203], a[16:19], a[72:75], 0\n"
  "v_mfma_f32_16x16x32_bf16 v[204:207], a[16:19], a[76:79], 0\n"
  "v_mfma_f32_16x16x32_bf16 v[208:211], a[20:23], a[64:67], 0\n"
  "buffer_load_dwordx4 a[184:187], v2, s[4:7], s21 offen offset:512 sc1\n"
  "v_mfma_f32_16x16x32_bf16 v[212:215], a[20:23], a[68:71], 0\n"
  "v_mfma_f32_16x16x32_bf16 v[216:219], a[20:23], a[72:75], 0\n"
  "v_mfma_f32_16x16x32_bf16 v[220:223], a[20:23], a[76:79], 0\n"
  "v_mfma_f32_16x16x32_bf16 v[224:227], a[24:27], a[64:67], 0\n"
  "v_mfma_f32_16x16x32_bf16 v[228:231], a[24:27], a[68:71], 0\n"
  "v_mfma_f32_16x16x32_bf16 v[232:235], a[24:27], a[72:75], 0\n"
  "v_mfma_f32_16x16x32_bf16 v[236:239], a[24:27], a[76:79], 0\n"
  "v_mfma_f32_16x16x32_bf16 v[240:243], a[28:31], a[64:67], 0\n"
  "buffer_load_dwordx4 a[188:191], v2, s[4:7], s21 offen offset:768 sc1\n"
  "v_mfma_f32_16x16x32_bf16 v[244:247], a[28:31], a[68:71], 0\n"
  "s_add_u32 s21, s21, 0x4000\n"
  "v_mfma_f32_16x16x32_bf16 v[248:251], a[28:31], a[72:75], 0\n"
  "v_mfma_f32_16x16x32_bf16 v[252:255], a[28:31], a[76:79], 0\n"
  "s_waitcnt vmcnt(21) lgkmcnt(0)\n"
  "s_barrier\n"
  "v_mfma_f32_16x16x32_bf16 v[128:131], a[32:35], a[80:83], v[128:131]\n"
  "ds_read_b64_tr_b16 a[0:1], v7 offset:16384\n"
  "v_mfma_f32_16x16x32_bf16 v[132:135], a[32:35], a[84:87], v[132:135]\n"
  "ds_read_b64_tr_b16 a[2:3], v7 offset:20480\n"
  "v_mfma_f32_16x16x32_bf16 v[136:139], a[32:35], a[88:91], v[136:139]\n"
  "ds_read_b64_tr_b16 a[4:5], v8 offset:16384\n"
  "v_mfma_f32_16x16x32_bf16 v[140:143], a[32:35], a[92:95], v[140:143]\n"
  "ds_read_b64_tr_b16 a[6:7], v8 offset:20480\n"
  "buffer_load_dwordx4 a[64:67], v2, s[4:7], s21 offen sc1\n"
  "v_mfma_f32_16x16x32_bf16 v[144:147], a[36:39], a[80:83], v[144:147]\n"
  "ds_read_b64_tr_b16 a[8:9], v9 offset:16384\n"
  "v_mfma_f32_16x16x32_bf16 v[148:151], a[36:39], a[84:87], v[148:151]\n"
  "ds_read_b64_tr_b16 a[10:11], v9 offset:20480\n"
  "v_mfma_f32_16x16x32_bf16 v[152:155], a[36:39], a[88:91], v[152:155]\n"
  "ds_read_b64_tr_b16 a[12:13], v10 offset:16384\n"
  "v_mfma_f32_16x16x32_bf16 v[156:159], a[36:39], a[92:95], v[156:159]\n"
  "ds_read_b64_tr_b16 a[14:15], v10 offset:20480\n"
  "buffer_load_dwordx4 a[68:71], v2, s[4:7], s21 offen offset:256 sc1\n"
  "v_mfma_f32_16x16x32_bf16 v[160:163], a[40:43], a[80:83], v[160:163]\n"
  "ds_read_b64_tr_b16 a[16:17], v11 offset:16384\n"
  "v_mfma_f32_16x16x32_bf16 v[164:167], a[40:43], a[84:87], v[164:167]\n"
  "ds_read_b64_tr_b16 a[18:19], v11 offset:20480\n"
  "v_mfma_f32_16x16x32_bf16 v[168:171], a[40:43], a[88:91], v[168:171]\n"
  "ds_read_b64_tr_b16 a[20:21], v12 offset:16384\n"
  "v_mfma_f32_16x16x32_bf16 v[172:175], a[40:43], a[92:95], v[172:175]\n"
  "ds_read_b64_tr_b16 a[22:23], v12 offset:20480\n"
  "buffer_load_dwordx4 a[72:75], v2, s[4:7], s21 offen offset:512 sc1\n"
  "v_mfma_f32_16x16x32_bf16 v[176:179], a[44:47], a[80:83], v[176:179]\n"
  "ds_read_b64_tr_b16 a[24:25], v13 offset:16384\n"
  "v_mfma_f32_16x16x32_bf16 v[180:183], a[44:47], a[84:87], v[180:183]\n"
  "ds_read_b64_tr_b16 a[26:27], v13 offset:20480\n"
  "v_mfma_f32_16x16x32_bf16 v[184:187], a[44:47], a[88:91], v[184:187]\n"
  "ds_read_b64_tr_b16 a[28:29], v14 offset:16384\n"
  "v_mfma_f32_16x16x32_bf16 v[188:191], a[44:47], a[92:95], v[188:191]\n"
  "ds_read_b64_tr_b16 a[30:31], v14 offset:20480\n"
  "buffer_load_dwordx4 a[76:79], v2, s[4:7], s21 offen offset:768 sc1\n"
  "v_mfma_f32_16x16x32_bf16 v[192:195], a[48:51], a[80:83], v[192:195]\n"
  "s_add_u32 s21, s21, 0x4000\n"
  "v_mfma_f32_16x16x32_bf16 v[196:199], a[48:51], a[84:87], v[196:199]\n"
  "v_mfma_f32_16x16x32_bf16 v[200:203], a[48:51], a[88:91], v[200:203]\n"
  "s_add_u32 m0, s24, 0\n"
  "v_mfma_f32_16x16x32_bf16 v[204:207], a[48:51], a[92:95], v[204:207]\n"
  "buffer_load_dwordx4 v3, s[8:11], s22 offen lds\n"
  "v_mfma_f32_16x16x32_bf16 v[208:211], a[52:55], a[80:83], v[208:211]\n"
  "v_mfma_f32_16x16x32_bf16 v[212:215], a[52:55], a[84:87], v[212:215]\n"
  "v_mfma_f32_16x16x32_bf16 v[216:219], a[52:55], a[88:91], v[216:219]\n"
  "buffer_load_dwordx4 v4, s[8:11], s22 offen offset:1024 lds\n"
  "v_mfma_f32_16x16x32_bf16 v[220:223], a[52:55], a[92:95], v[220:223]\n"
  "v_mfma_f32_16x16x32_bf16 v[224:227], a[56:59], a[80:83], v[224:227]\n"
  "v_mfma_f32_16x16x32_bf16 v[228:231], a[56:59], a[84:87], v[228:231]\n"
  "buffer_load_dwordx4 v5, s[8:11], s22 offen offset:2048 lds\n"
  "v_mfma_f32_16x16x32_bf16 v[232:235], a[56:59], a[88:91], v[232:235]\n"
  "v_mfma_f32_16x16x32_bf16 v[236:239], a[56:59], a[92:95], v[236:239]\n"
  "v_mfma_f32_16x16x32_bf16 v[240:243], a[60:63], a[80:83], v[240:243]\n"
  "buffer_load_dwordx4 v6, s[8:11], s22 offen offset:3072 lds\n"
  "v_mfma_f32_16x16x32_bf16 v[244:247], a[60:63], a[84:87], v[244:247]\n"
  "s_add_u32 s22, s22, s23\n"
  "v_mfma_f32_16x16x32_bf16 v[248:251], a[60:63], a[88:91], v[248:251]\n"
  "v_mfma_f32_16x16x32_bf16 v[252:255], a[60:63], a[92:95], v[252:255]\n"
  "s_waitcnt lgkmcnt(14)\n"
  "v_mfma_f32_16x16x32_bf16 v[128:131], a[0:3], a[96:99], v[128:131]\n"
  "ds_read_b64_tr_b16 a[32:33], v7 offset:24576\n"
  "v_mfma_f32_16x16x32_bf16 v[132:135], a[0:3], a[100:103], v[132:135]\n"
  "ds_read_b64_tr_b16 a[34:35], v7 offset:28672\n"
  "v_mfma_f32_16x16x32_bf16 v[136:139], a[0:3], a[104:107], v[136:139]\n"
  "ds_read_b64_tr_b16 a[36:37], v8 offset:24576\n"
  "v_mfma_f32_16x16x32_bf16 v[140:143], a[0:3], a[108:111], v[140:143]\n"
  "ds_read_b64_tr_b16 a[38:39], v8 offset:28672\n"
  "v_mfma_f32_16x16x32_bf16 v[144:147], a[4:7], a[96:99], v[144:147]\n"
  "ds_read_b64_tr_b16 a[40:41], v9 offset:24576\n"
  "buffer_load_dwordx4 a[80:83], v2, s[4:7], s21 offen sc1\n"
  "v_mfma_f32_16x16x32_bf16 v[148:151], a[4:7], a[100:103], v[148:151]\n"
  "ds_read_b64_tr_b16 a[42:43], v9 offset:28672\n"
  "v_mfma_f32_16x16x32_bf16 v[152:155], a[4:7], a[104:107], v[152:155]\n"
  "ds_read_b64_tr_b16 a[44:45], v10 offset:24576\n"
  "v_mfma_f32_16x16x32_bf16 v[156:159], a[4:7], a[108:111], v[156:159]\n"
  "ds_read_b64_tr_b16 a[46:47], v10 offset:28672\n"
  "v_mfma_f32_16x16x32_bf16 v[160:163], a[8:11], a[96:99], v[160:163]\n"
  "ds_read_b64_tr_b16 a[48:49], v11 offset:24576\n"
  "v_mfma_f32_16x16x32_bf16 v[164:167], a[8:11], a[100:103], v[164:167]\n"
  "ds_read_b64_tr_b16 a[50:51], v11 offset:28672\n"
  "v_mfma_f32_16x16x32_bf16 v[168:171], a[8:11], a[104:107], v[168:171]\n"
  "ds_read_b64_tr_b16 a[52:53], v12 offset:24576\n"
  "v_mfma_f32_16x16x32_bf16 v[172:175], a[8:11], a[108:111], v[172:175]\n"
  "ds_read_b64_tr_b16 a[54:55], v12 offset:28672\n"
  "v_mfma_f32_16x16x32_bf16 v[176:179], a[12:15], a[96:99], v[176:179]\n"
  "ds_read_b64_tr_b16 a[56:57], v13 offset:24576\n"
  "buffer_load_dwordx4 a[84:87], v2, s[4:7], s21 offen offset:256 sc1\n"
  "v_mfma_f32_16x16x32_bf16 v[180:183], a[12:15], a[100:103], v[180:183]\n"
  "ds_read_b64_tr_b16 a[58:59], v13 offset:28672\n"
  "v_mfma_f32_16x16x32_bf16 v[184:187], a[12:15], a[104:107], v[184:187]\n"
  "ds_read_b64_tr_b16 a[60:61], v14 offset:24576\n"
  "v_mfma_f32_16x16x32_bf16 v[188:191], a[12:15], a[108:111], v[188:191]\n"
  "ds_read_b64_tr_b16 a[62:63], v14 offset:28672\n"
  "v_mfma_f32_16x16x32_bf16 v[192:195], a[16:19], a[96:99], v[192:195]\n"
  "buffer_store_dwordx4 v[68:71], v2, s[12:15], s91 offen offset:256 sc1\n"
  "v_mfma_f32_16x16x32_bf16 v[196:199], a[16:19], a[100:103], v[196:199]\n"
  "v_mov_b32 v64, v80\n"
  "v_mfma_f32_16x16x32_bf16 v[200:203], a[16:19], a[104:107], v[200:203]\n"
  "v_mov_b32 v65, v81\n"
  "v_mfma_f32_16x16x32_bf16 v[204:207], a[16:19], a[108:111], v[204:207]\n"
  "v_mov_b32 v66, v82\n"
  "v_mfma_f32_16x16x32_bf16 v[208:211], a[20:23], a[96:99], v[208:211]\n"
  "buffer_load_dwordx4 a[88:91], v2, s[4:7], s21 offen offset:512 sc1\n"
  "v_mfma_f32_16x16x32_bf16 v[212:215], a[20:23], a[100:103], v[212:215]\n"
  "v_mov_b32 v67, v83\n"
  "v_mfma_f32_16x16x32_bf16 v[216:219], a[20:23], a[104:107], v[216:219]\n"
  "v_mov_b32 v80, v96\n"
  "v_mfma_f32_16x16x32_bf16 v[220:223], a[20:23], a[108:111], v[220:223]\n"
  "v_mov_b32 v81, v97\n"
  "v_mfma_f32_16x16x32_bf16 v[224:227], a[24:27], a[96:99], v[224:227]\n"
  "v_mov_b32 v82, v98\n"
  "v_mfma_f32_16x16x32_bf16 v[228:231], a[24:27], a[100:103], v[228:231]\n"
  "v_mov_b32 v83, v99\n"
  "v_mfma_f32_16x16x32_bf16 v[232:235], a[24:27], a[104:107], v[232:235]\n"
  "v_mov_b32 v96, v112\n"
  "v_mfma_f32_16x16x32_bf16 v[236:239], a[24:27], a[108:111], v[236:239]\n"
  "v_mov_b32 v97, v113\n"
  "v_mfma_f32_16x16x32_bf16 v[240:243], a[28:31], a[96:99], v[240:243]\n"
  "buffer_load_dwordx4 a[92:95], v2, s[4:7], s21 offen offset:768 sc1\n"
  "v_mfma_f32_16x16x32_bf16 v[244:247], a[28:31], a[100:103], v[244:247]\n"
  "s_add_u32 s21, s21, 0x4000\n"
  "v_mfma_f32_16x16x32_bf16 v[248:251], a[28:31], a[104:107], v[248:251]\n"
  "v_mov_b32 v98, v114\n"
  "v_mfma_f32_16x16x32_bf16 v[252:255], a[28:31], a[108:111], v[252:255]\n"
  "v_mov_b32 v99, v115\n"
  "s_waitcnt vmcnt(18) lgkmcnt(0)\n"
  "s_barrier\n"
  "s_cmp_eq_u32 s88, -1\n"
  "s_cbranch_scc1 GF_NOPUB_%=\n"
  "v_mov_b32 v56, s88\n"
  "v_mov_b32 v57, s90\n"
  "s_mov_b64 exec, 1\n"
  "global_store_dword v56, v57, s[80:81] sc1\n"
  "s_mov_b64 exec, -1\n"
  "GF_NOPUB_%=:\n"
  "v_mfma_f32_16x16x32_bf16 v[128:131], a[32:35], a[112:115], v[128:131]\n"
  "ds_read_b64_tr_b16 a[0:1], v7 offset:32768\n"
  "v_mfma_f32_16x16x32_bf16 v[132:135], a[32:35], a[116:119], v[132:135]\n"
  "ds_read_b64_tr_b16 a[2:3], v7 offset:36864\n"
  "v_mfma_f32_16x16x32_bf16 v[136:139], a[32:35], a[120:123], v[136:139]\n"
  "ds_read_b64_tr_b16 a[4:5], v8 offset:32768\n"
  "v_mfma_f32_16x16x32_bf16 v[140:143], a[32:35], a[124:127], v[140:143]\n"
  "ds_read_b64_tr_b16 a[6:7], v8 offset:36864\n"
  "buffer_load_dwordx4 a[96:99], v2, s[4:7], s21 offen sc1\n"
  "v_mfma_f32_16x16x32_bf16 v[144:147], a[36:39], a[112:115], v[144:147]\n"
  "ds_read_b64_tr_b16 a[8:9], v9 offset:32768\n"
  "v_mfma_f32_16x16x32_bf16 v[148:151], a[36:39], a[116:119], v[148:151]\n"
  "ds_read_b64_tr_b16 a[10:11], v9 offset:36864\n"
  "v_mfma_f32_16x16x32_bf16 v[152:155], a[36:39], a[120:123], v[152:155]\n"
  "ds_read_b64_tr_b16 a[12:13], v10 offset:32768\n"
  "v_mfma_f32_16x16x32_bf16 v[156:159], a[36:39], a[124:127], v[156:159]\n"
  "ds_read_b64_tr_b16 a[14:15], v10 offset:36864\n"
  "buffer_load_dwordx4 a[100:103], v2, s[4:7], s21 offen offset:256 sc1\n"
  "v_mfma_f32_16x16x32_bf16 v[160:163], a[40:43], a[112:115], v[160:163]\n"
  "ds_read_b64_tr_b16 a[16:17], v11 offset:32768\n"
  "v_mfma_f32_16x16x32_bf16 v[164:167], a[40:43], a[116:119], v[164:167]\n"
  "ds_read_b64_tr_b16 a[18:19], v11 offset:36864\n"
  "v_mfma_f32_16x16x32_bf16 v[168:171], a[40:43], a[120:123], v[168:171]\n"
  "ds_read_b64_tr_b16 a[20:21], v12 offset:32768\n"
  "v_mfma_f32_16x16x32_bf16 v[172:175], a[40:43], a[124:127], v[172:175]\n"
  "ds_read_b64_tr_b16 a[22:23], v12 offset:36864\n"
  "buffer_load_dwordx4 a[104:107], v2, s[4:7], s21 offen offset:512 sc1\n"
  "v_mfma_f32_16x16x32_bf16 v[176:179], a[44:47], a[112:115], v[176:179]\n"
  "ds_read_b64_tr_b16 a[24:25], v13 offset:32768\n"
  "v_mfma_f32_16x16x32_bf16 v[180:183], a[44:47], a[116:119], v[180:183]\n"
  "ds_read_b64_tr_b16 a[26:27], v13 offset:36864\n"
  "v_mfma_f32_16x16x32_bf16 v[184:187], a[44:47], a[120:123], v[184:187]\n"
  "ds_read_b64_tr_b16 a[28:29], v14 offset:32768\n"
  "v_mfma_f32_16x16x32_bf16 v[188:191], a[44:47], a[124:127], v[188:191]\n"
  "ds_read_b64_tr_b16 a[30:31], v14 offset:36864\n"
  "buffer_load_dwordx4 a[108:111], v2, s[4:7], s21 offen offset:768 sc1\n"
  "v_mfma_f32_16x16x32_bf16 v[192:195], a[48:51], a[112:115], v[192:195]\n"
  "s_add_u32 s21, s21, 0x4000\n"
  "v_mfma_f32_16x16x32_bf16 v[196:199], a[48:51], a[116:119], v[196:199]\n"
  "v_mfma_f32_16x16x32_bf16 v[200:203], a[48:51], a[120:123], v[200:203]\n"
  "s_add_u32 m0, s24, 0x4000\n"
  "v_mfma_f32_16x16x32_bf16 v[204:207], a[48:51], a[124:127], v[204:207]\n"
  "buffer_load_dwordx4 v3, s[8:11], s22 offen lds\n"
  "v_mfma_f32_16x16x32_bf16 v[208:211], a[52:55], a[112:115], v[208:211]\n"
  "v_mfma_f32_16x16x32_bf16 v[212:215], a[52:55], a[116:119], v[212:215]\n"
  "v_mfma_f32_16x16x32_bf16 v[216:219], a[52:55], a[120:123], v[216:219]\n"
  "buffer_load_dwordx4 v4, s[8:11], s22 offen offset:1024 lds\n"
  "v_mfma_f32_16x16x32_bf16 v[220:223], a[52:55], a[124:127], v[220:223]\n"
  "v_mfma_f32_16x16x32_bf16 v[224:227], a[56:59], a[112:115], v[224:227]\n"
  "v_mfma_f32_16x16x32_bf16 v[228:231], a[56:59], a[116:119], v[228:231]\n"
  "buffer_load_dwordx4 v5, s[8:11], s22 offen offset:2048 lds\n"
  "v_mfma_f32_16x16x32_bf16 v[232:235], a[56:59], a[120:123], v[232:235]\n"
  "v_mfma_f32_16x16x32_bf16 v[236:239], a[56:59], a[124:127], v[236:239]\n"
  "v_mfma_f32_16x16x32_bf16 v[240:243], a[60:63], a[112:115], v[240:243]\n"
  "buffer_load_dwordx4 v6, s[8:11], s22 offen offset:3072 lds\n"
  "v_mfma_f32_16x16x32_bf16 v[244:247], a[60:63], a[116:119], v[244:247]\n"
  "s_add_u32 s22, s22, s23\n"
  "v_mfma_f32_16x16x32_bf16 v[248:251], a[60:63], a[120:123], v[248:251]\n"
  "v_mfma_f32_16x16x32_bf16 v[252:255], a[60:63], a[124:127], v[252:255]\n"
  "s_waitcnt lgkmcnt(14)\n"
  "v_mfma_f32_16x16x32_bf16 v[128:131], a[0:3], a[128:131], v[128:131]\n"
  "ds_read_b64_tr_b16 a[32:33], v7 offset:40960\n"
  "v_mfma_f32_16x16x32_bf16 v[132:135], a[0:3], a[132:135], v[132:135]\n"
  "ds_read_b64_tr_b16 a[34:35], v7 offset:45056\n"
  "v_mfma_f32_16x16x32_bf16 v[136:139], a[0:3], a[136:139], v[136:139]\n"
  "ds_read_b64_tr_b16 a[36:37], v8 offset:40960\n"
  "v_mfma_f32_16x16x32_bf16 v[140:143], a[0:3], a[140:143], v[140:143]\n"
  "ds_read_b64_tr_b16 a[38:39], v8 offset:45056\n"
  "v_mfma_f32_16x16x32_bf16 v[144:147], a[4:7], a[128:131], v[144:147]\n"
  "ds_read_b64_tr_b16 a[40:41], v9 offset:40960\n"
  "buffer_load_dwordx4 a[112:115], v2, s[4:7], s21 offen sc1\n"
  "v_mfma_f32_16x16x32_bf16 v[148:151], a[4:7], a[132:135], v[148:151]\n"
  "ds_read_b64_tr_b16 a[42:43], v9 offset:45056\n"
  "v_mfma_f32_16x16x32_bf16 v[152:155], a[4:7], a[136:139], v[152:155]\n"
  "ds_read_b64_tr_b16 a[44:45], v10 offset:40960\n"
  "v_mfma_f32_16x16x32_bf16 v[156:159], a[4:7], a[140:143], v[156:159]\n"
  "ds_read_b64_tr_b16 a[46:47], v10 offset:45056\n"
  "v_mfma_f32_16x16x32_bf16 v[160:163], a[8:11], a[128:131], v[160:163]\n"
  "ds_read_b64_tr_b16 a[48:49], v11 offset:40960\n"
  "v_mfma_f32_16x16x32_bf16 v[164:167], a[8:11], a[132:135], v[164:167]\n"
  "ds_read_b64_tr_b16 a[50:51], v11 offset:45056\n"
  "v_mfma_f32_16x16x32_bf16 v[168:171], a[8:11], a[136:139], v[168:171]\n"
  "ds_read_b64_tr_b16 a[52:53], v12 offset:40960\n"
  "v_mfma_f32_16x16x32_bf16 v[172:175], a[8:11], a[140:143], v[172:175]\n"
  "ds_read_b64_tr_b16 a[54:55], v12 offset:45056\n"
  "v_mfma_f32_16x16x32_bf16 v[176:179], a[12:15], a[128:131], v[176:179]\n"
  "ds_read_b64_tr_b16 a[56:57], v13 offset:40960\n"
  "buffer_load_dwordx4 a[116:119], v2, s[4:7], s21 offen offset:256 sc1\n"
  "v_mfma_f32_16x16x32_bf16 v[180:183], a[12:15], a[132:135], v[180:183]\n"
  "ds_read_b64_tr_b16 a[58:59], v13 offset:45056\n"
  "v_mfma_f32_16x16x32_bf16 v[184:187], a[12:15], a[136:139], v[184:187]\n"
  "ds_read_b64_tr_b16 a[60:61], v14 offset:40960\n"
  "v_mfma_f32_16x16x32_bf16 v[188:191], a[12:15], a[140:143], v[188:191]\n"
  "ds_read_b64_tr_b16 a[62:63], v14 offset:45056\n"
  "v_mfma_f32_16x16x32_bf16 v[192:195], a[16:19], a[128:131], v[192:195]\n"
  "buffer_store_dwordx4 v[72:75], v2, s[12:15], s91 offen offset:512 sc1\n"
  "v_mfma_f32_16x16x32_bf16 v[196:199], a[16:19], a[132:135], v[196:199]\n"
  "v_mov_b32 v68, v84\n"
  "v_mfma_f32_16x16x32_bf16 v[200:203], a[16:19], a[136:139], v[200:203]\n"
  "v_mov_b32 v69, v85\n"
  "v_mfma_f32_16x16x32_bf16 v[204:207], a[16:19], a[140:143], v[204:207]\n"
  "v_mov_b32 v70, v86\n"
  "v_mfma_f32_16x16x32_bf16 v[208:211], a[20:23], a[128:131], v[208:211]\n"
  "buffer_load_dwordx4 a[120:123], v2, s[4:7], s21 offen offset:512 sc1\n"
  "v_mfma_f32_16x16x32_bf16 v[212:215], a[20:23], a[132:135], v[212:215]\n"
  "v_mov_b32 v71, v87\n"
  "v_mfma_f32_16x16x32_bf16 v[216:219], a[20:23], a[136:139], v[216:219]\n"
  "v_mov_b32 v84, v100\n"
  "v_mfma_f32_16x16x32_bf16 v[220:223], a[20:23], a[140:143], v[220:223]\n"
  "v_mov_b32 v85, v101\n"
  "v_mfma_f32_16x16x32_bf16 v[224:227], a[24:27], a[128:131], v[224:227]\n"
  "v_mov_b32 v86, v102\n"
  "v_mfma_f32_16x16x32_bf16 v[228:231], a[24:27], a[132:135], v[228:231]\n"
  "v_mov_b32 v87, v103\n"
  "v_mfma_f32_16x16x32_bf16 v[232:235], a[24:27], a[136:139], v[232:235]\n"
  "v_mov_b32 v100, v116\n"
  "v_mfma_f32_16x16x32_bf16 v[236:239], a[24:27], a[140:143], v[236:239]\n"
  "v_mov_b32 v101, v117\n"
  "v_mfma_f32_16x16x32_bf16 v[240:243], a[28:31], a[128:131], v[240:243]\n"
  "buffer_load_dwordx4 a[124:127], v2, s[4:7], s21 offen offset:768 sc1\n"
  "v_mfma_f32_16x16x32_bf16 v[244:247], a[28:31], a[132:135], v[244:247]\n"
  "s_add_u32 s21, s21, 0x4000\n"
  "v_mfma_f32_16x16x32_bf16 v[248:251], a[28:31], a[136:139], v[248:251]\n"
  "v_mov_b32 v102, v118\n"
  "v_mfma_f32_16x16x32_bf16 v[252:255], a[28:31], a[140:143], v[252:255]\n"
  "v_mov_b32 v103, v119\n"
  "s_waitcnt vmcnt(31) lgkmcnt(0)\n"
  "s_barrier\n"
  "v_mfma_f32_16x16x32_bf16 v[128:131], a[32:35], a[144:147], v[128:131]\n"
  "ds_read_b64_tr_b16 a[0:1], v7 offset:49152\n"
  "v_mfma_f32_16x16x32_bf16 v[132:135], a[32:35], a[148:151], v[132:135]\n"
  "ds_read_b64_tr_b16 a[2:3], v7 offset:53248\n"
  "v_mfma_f32_16x16x32_bf16 v[136:139], a[32:35], a[152:155], v[136:139]\n"
  "ds_read_b64_tr_b16 a[4:5], v8 offset:49152\n"
  "v_mfma_f32_16x16x32_bf16 v[140:143], a[32:35], a[156:159], v[140:143]\n"
  "ds_read_b64_tr_b16 a[6:7], v8 offset:53248\n"
  "buffer_load_dwordx4 a[128:131], v2, s[4:7], s21 offen sc1\n"
  "v_mfma_f32_16x16x32_bf16 v[144:147], a[36:39], a[144:147], v[144:147]\n"
  "ds_read_b64_tr_b16 a[8:9], v9 offset:49152\n"
  "v_mfma_f32_16x16x32_bf16 v[148:151], a[36:39], a[148:151], v[148:151]\n"
  "ds_read_b64_tr_b16 a[10:11], v9 offset:53248\n"
  "v_mfma_f32_16x16x32_bf16 v[152:155], a[36:39], a[152:155], v[152:155]\n"
  "ds_read_b64_tr_b16 a[12:13], v10 offset:49152\n"
  "v_mfma_f32_16x16x32_bf16 v[156:159], a[36:39], a[156:159], v[156:159]\n"
  "ds_read_b64_tr_b16 a[14:15], v10 offset:53248\n"
  "buffer_load_dwordx4 a[132:135], v2, s[4:7], s21 offen offset:256 sc1\n"
  "v_mfma_f32_16x16x32_bf16 v[160:163], a[40:43], a[144:147], v[160:163]\n"
  "ds_read_b64_tr_b16 a[16:17], v11 offset:49152\n"
  "v_mfma_f32_16x16x32_bf16 v[164:167], a[40:43], a[148:151], v[164:167]\n"
  "ds_read_b64_tr_b16 a[18:19], v11 offset:53248\n"
  "v_mfma_f32_16x16x32_bf16 v[168:171], a[40:43], a[152:155], v[168:171]\n"
  "ds_read_b64_tr_b16 a[20:21], v12 offset:49152\n"
  "v_mfma_f32_16x16x32_bf16 v[172:175], a[40:43], a[156:159], v[172:175]\n"
  "ds_read_b64_tr_b16 a[22:23], v12 offset:53248\n"
  "buffer_load_dwordx4 a[136:139], v2, s[4:7], s21 offen offset:512 sc1\n"
  "v_mfma_f32_16x16x32_bf16 v[176:179], a[44:47], a[144:147], v[176:179]\n"
  "ds_read_b64_tr_b16 a[24:25], v13 offset:49152\n"
  "v_mfma_f32_16x16x32_bf16 v[180:183], a[44:47], a[148:151], v[180:183]\n"
  "ds_read_b64_tr_b16 a[26:27], v13 offset:53248\n"
  "v_mfma_f32_16x16x32_bf16 v[184:187], a[44:47], a[152:155], v[184:187]\n"
  "ds_read_b64_tr_b16 a[28:29], v14 offset:49152\n"
  "v_mfma_f32_16x16x32_bf16 v[188:191], a[44:47], a[156:159], v[188:191]\n"
  "ds_read_b64_tr_b16 a[30:31], v14 offset:53248\n"
  "buffer_load_dwordx4 a[140:143], v2, s[4:7], s21 offen offset:768 sc1\n"
  "v_mfma_f32_16x16x32_bf16 v[192:195], a[48:51], a[144:147], v[192:195]\n"
  "s_add_u32 s21, s21, 0x4000\n"
  "v_mfma_f32_16x16x32_bf16 v[196:199], a[48:51], a[148:151], v[196:199]\n"
  "v_mfma_f32_16x16x32_bf16 v[200:203], a[48:51], a[152:155], v[200:203]\n"
  "s_add_u32 m0, s24, 0x8000\n"
  "v_mfma_f32_16x16x32_bf16 v[204:207], a[48:51], a[156:159], v[204:207]\n"
  "buffer_load_dwordx4 v3, s[8:11], s22 offen lds\n"
  "v_mfma_f32_16x16x32_bf16 v[208:211], a[52:55], a[144:147], v[208:211]\n"
  "v_mfma_f32_16x16x32_bf16 v[212:215], a[52:55], a[148:151], v[212:215]\n"
  "v_mfma_f32_16x16x32_bf16 v[216:219], a[52:55], a[152:155], v[216:219]\n"
  "buffer_load_dwordx4 v4, s[8:11], s22 offen offset:1024 lds\n"
  "v_mfma_f32_16x16x32_bf16 v[220:223], a[52:55], a[156:159], v[220:223]\n"
  "v_mfma_f32_16x16x32_bf16 v[224:227], a[56:59], a[144:147], v[224:227]\n"
  "v_mfma_f32_16x16x32_bf16 v[228:231], a[56:59], a[148:151], v[228:231]\n"
  "buffer_load_dwordx4 v5, s[8:11], s22 offen offset:2048 lds\n"
  "v_mfma_f32_16x16x32_bf16 v[232:235], a[56:59], a[152:155], v[232:235]\n"
  "v_mfma_f32_16x16x32_bf16 v[236:239], a[56:59], a[156:159], v[236:239]\n"
  "v_mfma_f32_16x16x32_bf16 v[240:243], a[60:63], a[144:147], v[240:243]\n"
  "buffer_load_dwordx4 v6, s[8:11], s22 offen offset:3072 lds\n"
  "v_mfma_f32_16x16x32_bf16 v[244:247], a[60:63], a[148:151], v[244:247]\n"
  "s_add_u32 s22, s22, s23\n"
  "v_mfma_f32_16x16x32_bf16 v[248:251], a[60:63], a[152:155], v[248:251]\n"
  "v_mfma_f32_16x16x32_bf16 v[252:255], a[60:63], a[156:159], v[252:255]\n"
  "s_waitcnt lgkmcnt(14)\n"
  "v_mfma_f32_16x16x32_bf16 v[128:131], a[0:3], a[160:163], v[128:131]\n"
  "ds_read_b64_tr_b16 a[32:33], v7 offset:57344\n"
  "v_mfma_f32_16x16x32_bf16 v[132:135], a[0:3], a[164:167], v[132:135]\n"
  "ds_read_b64_tr_b16 a[34:35], v7 offset:61440\n"
  "v_mfma_f32_16x16x32_bf16 v[136:139], a[0:3], a[168:171], v[136:139]\n"
  "ds_read_b64_tr_b16 a[36:37], v8 offset:57344\n"
  "v_mfma_f32_16x16x32_bf16 v[140:143], a[0:3], a[172:175], v[140:143]\n"
  "ds_read_b64_tr_b16 a[38:39], v8 offset:61440\n"
  "v_mfma_f32_16x16x32_bf16 v[144:147], a[4:7], a[160:163], v[144:147]\n"
  "ds_read_b64_tr_b16 a[40:41], v9 offset:57344\n"
  "buffer_load_dwordx4 a[144:147], v2, s[4:7], s21 offen sc1\n"
  "v_mfma_f32_16x16x32_bf16 v[148:151], a[4:7], a[164:167], v[148:151]\n"
  "ds_read_b64_tr_b16 a[42:43], v9 offset:61440\n"
  "v_mfma_f32_16x16x32_bf16 v[152:155], a[4:7], a[168:171], v[152:155]\n"
  "ds_read_b64_tr_b16 a[44:45], v10 offset:57344\n"
  "v_mfma_f32_16x16x32_bf16 v[156:159], a[4:7], a[172:175], v[156:159]\n"
  "ds_read_b64_tr_b16 a[46:47], v10 offset:61440\n"
  "v_mfma_f32_16x16x32_bf16 v[160:163], a[8:11], a[160:163], v[160:163]\n"
  "ds_read_b64_tr_b16 a[48:49], v11 offset:57344\n"
  "v_mfma_f32_16x16x32_bf16 v[164:167], a[8:11], a[164:167], v[164:167]\n"
  "ds_read_b64_tr_b16 a[50:51], v11 offset:61440\n"
  "v_mfma_f32_16x16x32_bf16 v[168:171], a[8:11], a[168:171], v[168:171]\n"
  "ds_read_b64_tr_b16 a[52:53], v12 offset:57344\n"
  "v_mfma_f32_16x16x32_bf16 v[172:175], a[8:11], a[172:175], v[172:175]\n"
  "ds_read_b64_tr_b16 a[54:55], v12 offset:61440\n"
  "v_mfma_f32_16x16x32_bf16 v[176:179], a[12:15], a[160:163], v[176:179]\n"
  "ds_read_b64_tr_b16 a[56:57], v13 offset:57344\n"
  "buffer_load_dwordx4 a[148:151], v2, s[4:7], s21 offen offset:256 sc1\n"
  "v_mfma_f32_16x16x32_bf16 v[180:183], a[12:15], a[164:167], v[180:183]\n"
  "ds_read_b64_tr_b16 a[58:59], v13 offset:61440\n"
  "v_mfma_f32_16x16x32_bf16 v[184:187], a[12:15], a[168:171], v[184:187]\n"
  "ds_read_b64_tr_b16 a[60:61], v14 offset:57344\n"
  "v_mfma_f32_16x16x32_bf16 v[188:191], a[12:15], a[172:175], v[188:191]\n"
  "ds_read_b64_tr_b16 a[62:63], v14 offset:61440\n"
  "v_mfma_f32_16x16x32_bf16 v[192:195], a[16:19], a[160:163], v[192:195]\n"
  "buffer_store_dwordx4 v[76:79], v2, s[12:15], s91 offen offset:768 sc1\n"
  "s_add_u32 s91, s91, 0x4000\n"
  "v_mfma_f32_16x16x32_bf16 v[196:199], a[16:19], a[164:167], v[196:199]\n"
  "v_mov_b32 v72, v88\n"
  "v_mfma_f32_16x16x32_bf16 v[200:203], a[16:19], a[168:171], v[200:203]\n"
  "v_mov_b32 v73, v89\n"
  "v_mfma_f32_16x16x32_bf16 v[204:207], a[16:19], a[172:175], v[204:207]\n"
  "v_mov_b32 v74, v90\n"
  "v_mfma_f32_16x16x32_bf16 v[208:211], a[20:23], a[160:163], v[208:211]\n"
  "buffer_load_dwordx4 a[152:155], v2, s[4:7], s21 offen offset:512 sc1\n"
  "v_mfma_f32_16x16x32_bf16 v[212:215], a[20:23], a[164:167], v[212:215]\n"
  "v_mov_b32 v75, v91\n"
  "v_mfma_f32_16x16x32_bf16 v[216:219], a[20:23], a[168:171], v[216:219]\n"
  "v_mov_b32 v88, v104\n"
  "v_mfma_f32_16x16x32_bf16 v[220:223], a[20:23], a[172:175], v[220:223]\n"
  "v_mov_b32 v89, v105\n"
  "v_mfma_f32_16x16x32_bf16 v[224:227], a[24:27], a[160:163], v[224:227]\n"
  "v_mov_b32 v90, v106\n"
  "v_mfma_f32_16x16x32_bf16 v[228:231], a[24:27], a[164:167], v[228:231]\n"
  "v_mov_b32 v91, v107\n"
  "v_mfma_f32_16x16x32_bf16 v[232:235], a[24:27], a[168:171], v[232:235]\n"
  "v_mov_b32 v104, v120\n"
  "v_mfma_f32_16x16x32_bf16 v[236:239], a[24:27], a[172:175], v[236:239]\n"
  "v_mov_b32 v105, v121\n"
  "v_mfma_f32_16x16x32_bf16 v[240:243], a[28:31], a[160:163], v[240:243]\n"
  "buffer_load_dwordx4 a[156:159], v2, s[4:7], s21 offen offset:768 sc1\n"
  "v_mfma_f32_16x16x32_bf16 v[244:247], a[28:31], a[164:167], v[244:247]\n"
  "s_add_u32 s21, s21, 0x4000\n"
  "v_mfma_f32_16x16x32_bf16 v[248:251], a[28:31], a[168:171], v[248:251]\n"
  "v_mov_b32 v106, v122\n"
  "v_mfma_f32_16x16x32_bf16 v[252:255], a[28:31], a[172:175], v[252:255]\n"
  "v_mov_b32 v107, v123\n"
  "s_waitcnt vmcnt(31) lgkmcnt(0)\n"
  "s_barrier\n"
  "v_mfma_f32_16x16x32_bf16 v[128:131], a[32:35], a[176:179], v[128:131]\n"
  "ds_read_b64_tr_b16 a[0:1], v7\n"
  "v_mfma_f32_16x16x32_bf16 v[132:135], a[32:35], a[180:183], v[132:135]\n"
  "ds_read_b64_tr_b16 a[2:3], v7 offset:4096\n"
  "v_mfma_f32_16x16x32_bf16 v[136:139], a[32:35], a[184:187], v[136:139]\n"
  "ds_read_b64_tr_b16 a[4:5], v8\n"
  "v_mfma_f32_16x16x32_bf16 v[140:143], a[32:35], a[188:191], v[140:143]\n"
  "ds_read_b64_tr_b16 a[6:7], v8 offset:4096\n"
  "buffer_load_dwordx4 a[160:163], v2, s[4:7], s21 offen sc1\n"
  "v_mfma_f32_16x16x32_bf16 v[144:147], a[36:39], a[176:179], v[144:147]\n"
  "ds_read_b64_tr_b16 a[8:9], v9\n"
  "v_mfma_f32_16x16x32_bf16 v[148:151], a[36:39], a[180:183], v[148:151]\n"
  "ds_read_b64_tr_b16 a[10:11], v9 offset:4096\n"
  "v_mfma_f32_16x16x32_bf16 v[152:155], a[36:39], a[184:187], v[152:155]\n"
  "ds_read_b64_tr_b16 a[12:13], v10\n"
  "v_mfma_f32_16x16x32_bf16 v[156:159], a[36:39], a[188:191], v[156:159]\n"
  "ds_read_b64_tr_b16 a[14:15], v10 offset:4096\n"
  "buffer_load_dwordx4 a[164:167], v2, s[4:7], s21 offen offset:256 sc1\n"
  "v_mfma_f32_16x16x32_bf16 v[160:163], a[40:43], a[176:179], v[160:163]\n"
  "ds_read_b64_tr_b16 a[16:17], v11\n"
  "v_mfma_f32_16x16x32_bf16 v[164:167], a[40:43], a[180:183], v[164:167]\n"
  "ds_read_b64_tr_b16 a[18:19], v11 offset:4096\n"
  "v_mfma_f32_16x16x32_bf16 v[168:171], a[40:43], a[184:187], v[168:171]\n"
  "ds_read_b64_tr_b16 a[20:21], v12\n"
  "v_mfma_f32_16x16x32_bf16 v[172:175], a[40:43], a[188:191], v[172:175]\n"
  "ds_read_b64_tr_b16 a[22:23], v12 offset:4096\n"
  "buffer_load_dwordx4 a[168:171], v2, s[4:7], s21 offen offset:512 sc1\n"
  "v_mfma_f32_16x16x32_bf16 v[176:179], a[44:47], a[176:179], v[176:179]\n"
  "ds_read_b64_tr_b16 a[24:25], v13\n"
  "v_mfma_f32_16x16x32_bf16 v[180:183], a[44:47], a[180:183], v[180:183]\n"
  "ds_read_b64_tr_b16 a[26:27], v13 offset:4096\n"
  "v_mfma_f32_16x16x32_bf16 v[184:187], a[44:47], a[184:187], v[184:187]\n"
  "ds_read_b64_tr_b16 a[28:29], v14\n"
  "v_mfma_f32_16x16x32_bf16 v[188:191], a[44:47], a[188:191], v[188:191]\n"
  "ds_read_b64_tr_b16 a[30:31], v14 offset:4096\n"
  "buffer_load_dwordx4 a[172:175], v2, s[4:7], s21 offen offset:768 sc1\n"
  "v_mfma_f32_16x16x32_bf16 v[192:195], a[48:51], a[176:179], v[192:195]\n"
  "s_add_u32 s21, s21, 0x4000\n"
  "v_mfma_f32_16x16x32_bf16 v[196:199], a[48:51], a[180:183], v[196:199]\n"
  "v_mfma_f32_16x16x32_bf16 v[200:203], a[48:51], a[184:187], v[200:203]\n"
  "s_add_u32 m0, s24, 0xc000\n"
  "v_mfma_f32_16x16x32_bf16 v[204:207], a[48:51], a[188:191], v[204:207]\n"
  "buffer_load_dwordx4 v3, s[8:11], s22 offen lds\n"
  "v_mfma_f32_16x16x32_bf16 v[208:211], a[52:55], a[176:179], v[208:211]\n"
  "v_mfma_f32_16x16x32_bf16 v[212:215], a[52:55], a[180:183], v[212:215]\n"
  "v_mfma_f32_16x16x32_bf16 v[216:219], a[52:55], a[184:187], v[216:219]\n"
  "buffer_load_dwordx4 v4, s[8:11], s22 offen offset:1024 lds\n"
  "v_mfma_f32_16x16x32_bf16 v[220:223], a[52:55], a[188:191], v[220:223]\n"
  "v_mfma_f32_16x16x32_bf16 v[224:227], a[56:59], a[176:179], v[224:227]\n"
  "v_mfma_f32_16x16x32_bf16 v[228:231], a[56:59], a[180:183], v[228:231]\n"
  "buffer_load_dwordx4 v5, s[8:11], s22 offen offset:2048 lds\n"
  "v_mfma_f32_16x16x32_bf16 v[232:235], a[56:59], a[184:187], v[232:235]\n"
  "v_mfma_f32_16x16x32_bf16 v[236:239], a[56:59], a[188:191], v[236:239]\n"
  "v_mfma_f32_16x16x32_bf16 v[240:243], a[60:63], a[176:179], v[240:243]\n"
  "buffer_load_dwordx4 v6, s[8:11], s22 offen offset:3072 lds\n"
  "v_mfma_f32_16x16x32_bf16 v[244:247], a[60:63], a[180:183], v[244:247]\n"
  "s_add_u32 s22, s22, s23\n"
  "v_mfma_f32_16x16x32_bf16 v[248:251], a[60:63], a[184:187], v[248:251]\n"
  "v_mfma_f32_16x16x32_bf16 v[252:255], a[60:63], a[188:191], v[252:255]\n"
  "s_cmp_eq_u32 s20, 0\n"
  "s_cbranch_scc1 GEMM_RET_%=\n"
  "GEMM_LOOP_%=:\n"
  "s_nop 1\n"
  "s_waitcnt lgkmcnt(0)\n"
  "GL_%=:\n"
  "s_waitcnt lgkmcnt(14)\n"
  "v_mfma_f32_16x16x32_bf16 v[128:131], a[0:3], a[64:67], v[128:131]\n"
  "ds_read_b64_tr_b16 a[32:33], v7 offset:8192\n"
  "v_mfma_f32_16x16x32_bf16 v[132:135], a[0:3], a[68:71], v[132:135]\n"
  "ds_read_b64_tr_b16 a[34:35], v7 offset:12288\n"
  "v_mfma_f32_16x16x32_bf16 v[136:139], a[0:3], a[72:75], v[136:139]\n"
  "ds_read_b64_tr_b16 a[36:37], v8 offset:8192\n"
  "v_mfma_f32_16x16x32_bf16 v[140:143], a[0:3], a[76:79], v[140:143]\n"
  "ds_read_b64_tr_b16 a[38:39], v8 offset:12288\n"
  "v_mfma_f32_16x16x32_bf16 v[144:147], a[4:7], a[64:67], v[144:147]\n"
  "ds_read_b64_tr_b16 a[40:41], v9 offset:8192\n"
  "buffer_load_dwordx4 a[176:179], v2, s[4:7], s21 offen sc1\n"
  "v_mfma_f32_16x16x32_bf16 v[148:151], a[4:7], a[68:71], v[148:151]\n"
  "ds_read_b64_tr_b16 a[42:43], v9 offset:12288\n"
  "v_mfma_f32_16x16x32_bf16 v[152:155], a[4:7], a[72:75], v[152:155]\n"
  "ds_read_b64_tr_b16 a[44:45], v10 offset:8192\n"
  "v_mfma_f32_16x16x32_bf16 v[156:159], a[4:7], a[76:79], v[156:159]\n"
  "ds_read_b64_tr_b16 a[46:47], v10 offset:12288\n"
  "v_mfma_f32_16x16x32_bf16 v[160:163], a[8:11], a[64:67], v[160:163]\n"
  "ds_read_b64_tr_b16 a[48:49], v11 offset:8192\n"
  "v_mfma_f32_16x16x32_bf16 v[164:167], a[8:11], a[68:71], v[164:167]\n"
  "ds_read_b64_tr_b16 a[50:51], v11 offset:12288\n"
  "v_mfma_f32_16x16x32_bf16 v[168:171], a[8:11], a[72:75], v[168:171]\n"
  "ds_read_b64_tr_b16 a[52:53], v12 offset:8192\n"
  "v_mfma_f32_16x16x32_bf16 v[172:175], a[8:11], a[76:79], v[172:175]\n"
  "ds_read_b64_tr_b16 a[54:55], v12 offset:12288\n"
  "v_mfma_f32_16x16x32_bf16 v[176:179], a[12:15], a[64:67], v[176:179]\n"
  "ds_read_b64_tr_b16 a[56:57], v13 offset:8192\n"
  "buffer_load_dwordx4 a[180:183], v2, s[4:7], s21 offen offset:256 sc1\n"
  "v_mfma_f32_16x16x32_bf16 v[180:183], a[12:15], a[68:71], v[180:183]\n"
  "ds_read_b64_tr_b16 a[58:59], v13 offset:12288\n"
  "v_mfma_f32_16x16x32_bf16 v[184:187], a[12:15], a[72:75], v[184:187]\n"
  "ds_read_b64_tr_b16 a[60:61], v14 offset:8192\n"
  "v_mfma_f32_16x16x32_bf16 v[188:191], a[12:15], a[76:79], v[188:191]\n"
  "ds_read_b64_tr_b16 a[62:63], v14 offset:12288\n"
  "v_mfma_f32_16x16x32_bf16 v[192:195], a[16:19], a[64:67], v[192:195]\n"
  "buffer_store_dwordx4 v[64:67], v2, s[12:15], s91 offen sc1\n"
  "v_mfma_f32_16x16x32_bf16 v[196:199], a[16:19], a[68:71], v[196:199]\n"
  "v_mov_b32 v76, v92\n"
  "v_mfma_f32_16x16x32_bf16 v[200:203], a[16:19], a[72:75], v[200:203]\n"
  "v_mov_b32 v77, v93\n"
  "v_mfma_f32_16x16x32_bf16 v[204:207], a[16:19], a[76:79], v[204:207]\n"
  "v_mov_b32 v78, v94\n"
  "v_mfma_f32_16x16x32_bf16 v[208:211], a[20:23], a[64:67], v[208:211]\n"
  "buffer_load_dwordx4 a[184:187], v2, s[4:7], s21 offen offset:512 sc1\n"
  "v_mfma_f32_16x16x32_bf16 v[212:215], a[20:23], a[68:71], v[212:215]\n"
  "v_mov_b32 v79, v95\n"
  "v_mfma_f32_16x16x32_bf16 v[216:219], a[20:23], a[72:75], v[216:219]\n"
  "v_mov_b32 v92, v108\n"
  "v_mfma_f32_16x16x32_bf16 v[220:223], a[20:23], a[76:79], v[220:223]\n"
  "v_mov_b32 v93, v109\n"
  "v_mfma_f32_16x16x32_bf16 v[224:227], a[24:27], a[64:67], v[224:227]\n"
  "v_mov_b32 v94, v110\n"
  "v_mfma_f32_16x16x32_bf16 v[228:231], a[24:27], a[68:71], v[228:231]\n"
  "v_mov_b32 v95, v111\n"
  "v_mfma_f32_16x16x32_bf16 v[232:235], a[24:27], a[72:75], v[232:235]\n"
  "v_mov_b32 v108, v124\n"
  "v_mfma_f32_16x16x32_bf16 v[236:239], a[24:27], a[76:79], v[236:239]\n"
  "v_mov_b32 v109, v125\n"
  "v_mfma_f32_16x16x32_bf16 v[240:243], a[28:31], a[64:67], v[240:243]\n"
  "buffer_load_dwordx4 a[188:191], v2, s[4:7], s21 offen offset:768 sc1\n"
  "v_mfma_f32_16x16x32_bf16 v[244:247], a[28:31], a[68:71], v[244:247]\n"
  "s_add_u32 s21, s21, 0x4000\n"
  "v_mfma_f32_16x16x32_bf16 v[248:251], a[28:31], a[72:75], v[248:251]\n"
  "v_mov_b32 v110, v126\n"
  "v_mfma_f32_16x16x32_bf16 v[252:255], a[28:31], a[76:79], v[252:255]\n"
  "v_mov_b32 v111, v127\n"
  "s_waitcnt vmcnt(31) lgkmcnt(0)\n"
  "s_barrier\n"
  "v_mfma_f32_16x16x32_bf16 v[128:131], a[32:35], a[80:83], v[128:131]\n"
  "ds_read_b64_tr_b16 a[0:1], v7 offset:16384\n"
  "v_mfma_f32_16x16x32_bf16 v[132:135], a[32:35], a[84:87], v[132:135]\n"
  "ds_read_b64_tr_b16 a[2:3], v7 offset:20480\n"
  "v_mfma_f32_16x16x32_bf16 v[136:139], a[32:35], a[88:91], v[136:139]\n"
  "ds_read_b64_tr_b16 a[4:5], v8 offset:16384\n"
  "v_mfma_f32_16x16x32_bf16 v[140:143], a[32:35], a[92:95], v[140:143]\n"
  "ds_read_b64_tr_b16 a[6:7], v8 offset:20480\n"
  "buffer_load_dwordx4 a[64:67], v2, s[4:7], s21 offen sc1\n"
  "v_mfma_f32_16x16x32_bf16 v[144:147], a[36:39], a[80:83], v[144:147]\n"
  "ds_read_b64_tr_b16 a[8:9], v9 offset:16384\n"
  "v_mfma_f32_16x16x32_bf16 v[148:151], a[36:39], a[84:87], v[148:151]\n"
  "ds_read_b64_tr_b16 a[10:11], v9 offset:20480\n"
  "v_mfma_f32_16x16x32_bf16 v[152:155], a[36:39], a[88:91], v[152:155]\n"
  "ds_read_b64_tr_b16 a[12:13], v10 offset:16384\n"
  "v_mfma_f32_16x16x32_bf16 v[156:159], a[36:39], a[92:95], v[156:159]\n"
  "ds_read_b64_tr_b16 a[14:15], v10 offset:20480\n"
  "buffer_load_dwordx4 a[68:71], v2, s[4:7], s21 offen offset:256 sc1\n"
  "v_mfma_f32_16x16x32_bf16 v[160:163], a[40:43], a[80:83], v[160:163]\n"
  "ds_read_b64_tr_b16 a[16:17], v11 offset:16384\n"
  "v_mfma_f32_16x16x32_bf16 v[164:167], a[40:43], a[84:87], v[164:167]\n"
  "ds_read_b64_tr_b16 a[18:19], v11 offset:20480\n"
  "v_mfma_f32_16x16x32_bf16 v[168:171], a[40:43], a[88:91], v[168:171]\n"
  "ds_read_b64_tr_b16 a[20:21], v12 offset:16384\n"
  "v_mfma_f32_16x16x32_bf16 v[172:175], a[40:43], a[92:95], v[172:175]\n"
  "ds_read_b64_tr_b16 a[22:23], v12 offset:20480\n"
  "buffer_load_dwordx4 a[72:75], v2, s[4:7], s21 offen offset:512 sc1\n"
  "v_mfma_f32_16x16x32_bf16 v[176:179], a[44:47], a[80:83], v[176:179]\n"
  "ds_read_b64_tr_b16 a[24:25], v13 offset:16384\n"
  "v_mfma_f32_16x16x32_bf16 v[180:183], a[44:47], a[84:87], v[180:183]\n"
  "ds_read_b64_tr_b16 a[26:27], v13 offset:20480\n"
  "v_mfma_f32_16x16x32_bf16 v[184:187], a[44:47], a[88:91], v[184:187]\n"
  "ds_read_b64_tr_b16 a[28:29], v14 offset:16384\n"
  "v_mfma_f32_16x16x32_bf16 v[188:191], a[44:47], a[92:95], v[188:191]\n"
  "ds_read_b64_tr_b16 a[30:31], v14 offset:20480\n"
  "buffer_load_dwordx4 a[76:79], v2, s[4:7], s21 offen offset:768 sc1\n"
  "v_mfma_f32_16x16x32_bf16 v[192:195], a[48:51], a[80:83], v[192:195]\n"
  "s_add_u32 s21, s21, 0x4000\n"
  "v_mfma_f32_16x16x32_bf16 v[196:199], a[48:51], a[84:87], v[196:199]\n"
  "v_mfma_f32_16x16x32_bf16 v[200:203], a[48:51], a[88:91], v[200:203]\n"
  "s_add_u32 m0, s24, 0\n"
  "v_mfma_f32_16x16x32_bf16 v[204:207], a[48:51], a[92:95], v[204:207]\n"
  "buffer_load_dwordx4 v3, s[8:11], s22 offen lds\n"
  "v_mfma_f32_16x16x32_bf16 v[208:211], a[52:55], a[80:83], v[208:211]\n"
  "v_mfma_f32_16x16x32_bf16 v[212:215], a[52:55], a[84:87], v[212:215]\n"
  "v_mfma_f32_16x16x32_bf16 v[216:219], a[52:55], a[88:91], v[216:219]\n"
  "buffer_load_dwordx4 v4, s[8:11], s22 offen offset:1024 lds\n"
  "v_mfma_f32_16x16x32_bf16 v[220:223], a[52:55], a[92:95], v[220:223]\n"
  "v_mfma_f32_16x16x32_bf16 v[224:227], a[56:59], a[80:83], v[224:227]\n"
  "v_mfma_f32_16x16x32_bf16 v[228:231], a[56:59], a[84:87], v[228:231]\n"
  "buffer_load_dwordx4 v5, s[8:11], s22 offen offset:2048 lds\n"
  "v_mfma_f32_16x16x32_bf16 v[232:235], a[56:59], a[88:91], v[232:235]\n"
  "v_mfma_f32_16x16x32_bf16 v[236:239], a[56:59], a[92:95], v[236:239]\n"
  "v_mfma_f32_16x16x32_bf16 v[240:243], a[60:63], a[80:83], v[240:243]\n"
  "buffer_load_dwordx4 v6, s[8:11], s22 offen offset:3072 lds\n"
  "v_mfma_f32_16x16x32_bf16 v[244:247], a[60:63], a[84:87], v[244:247]\n"
  "s_add_u32 s22, s22, s23\n"
  "v_mfma_f32_16x16x32_bf16 v[248:251], a[60:63], a[88:91], v[248:251]\n"
  "v_mfma_f32_16x16x32_bf16 v[252:255], a[60:63], a[92:95], v[252:255]\n"
  "s_waitcnt lgkmcnt(14)\n"
  "v_mfma_f32_16x16x32_bf16 v[128:131], a[0:3], a[96:99], v[128:131]\n"
  "ds_read_b64_tr_b16 a[32:33], v7 offset:24576\n"
  "v_mfma_f32_16x16x32_bf16 v[132:135], a[0:3], a[100:103], v[132:135]\n"
  "ds_read_b64_tr_b16 a[34:35], v7 offset:28672\n"
  "v_mfma_f32_16x16x32_bf16 v[136:139], a[0:3], a[104:107], v[136:139]\n"
  "ds_read_b64_tr_b16 a[36:37], v8 offset:24576\n"
  "v_mfma_f32_16x16x32_bf16 v[140:143], a[0:3], a[108:111], v[140:143]\n"
  "ds_read_b64_tr_b16 a[38:39], v8 offset:28672\n"
  "v_mfma_f32_16x16x32_bf16 v[144:147], a[4:7], a[96:99], v[144:147]\n"
  "ds_read_b64_tr_b16 a[40:41], v9 offset:24576\n"
  "buffer_load_dwordx4 a[80:83], v2, s[4:7], s21 offen sc1\n"
  "v_mfma_f32_16x16x32_bf16 v[148:151], a[4:7], a[100:103], v[148:151]\n"
  "ds_read_b64_tr_b16 a[42:43], v9 offset:28672\n"
  "v_mfma_f32_16x16x32_bf16 v[152:155], a[4:7], a[104:107], v[152:155]\n"
  "ds_read_b64_tr_b16 a[44:45], v10 offset:24576\n"
  "v_mfma_f32_16x16x32_bf16 v[156:159], a[4:7], a[108:111], v[156:159]\n"
  "ds_read_b64_tr_b16 a[46:47], v10 offset:28672\n"
  "v_mfma_f32_16x16x32_bf16 v[160:163], a[8:11], a[96:99], v[160:163]\n"
  "ds_read_b64_tr_b16 a[48:49], v11 offset:24576\n"
  "v_mfma_f32_16x16x32_bf16 v[164:167], a[8:11], a[100:103], v[164:167]\n"
  "ds_read_b64_tr_b16 a[50:51], v11 offset:28672\n"
  "v_mfma_f32_16x16x32_bf16 v[168:171], a[8:11], a[104:107], v[168:171]\n"
  "ds_read_b64_tr_b16 a[52:53], v12 offset:24576\n"
  "v_mfma_f32_16x16x32_bf16 v[172:175], a[8:11], a[108:111], v[172:175]\n"
  "ds_read_b64_tr_b16 a[54:55], v12 offset:28672\n"
  "v_mfma_f32_16x16x32_bf16 v[176:179], a[12:15], a[96:99], v[176:179]\n"
  "ds_read_b64_tr_b16 a[56:57], v13 offset:24576\n"
  "buffer_load_dwordx4 a[84:87], v2, s[4:7], s21 offen offset:256 sc1\n"
  "v_mfma_f32_16x16x32_bf16 v[180:183], a[12:15], a[100:103], v[180:183]\n"
  "ds_read_b64_tr_b16 a[58:59], v13 offset:28672\n"
  "v_mfma_f32_16x16x32_bf16 v[184:187], a[12:15], a[104:107], v[184:187]\n"
  "ds_read_b64_tr_b16 a[60:61], v14 offset:24576\n"
  "v_mfma_f32_16x16x32_bf16 v[188:191], a[12:15], a[108:111], v[188:191]\n"
  "ds_read_b64_tr_b16 a[62:63], v14 offset:28672\n"
  "v_mfma_f32_16x16x32_bf16 v[192:195], a[16:19], a[96:99], v[192:195]\n"
  "buffer_store_dwordx4 v[68:71], v2, s[12:15], s91 offen offset:256 sc1\n"
  "v_mfma_f32_16x16x32_bf16 v[196:199], a[16:19], a[100:103], v[196:199]\n"
  "v_mov_b32 v64, v80\n"
  "v_mfma_f32_16x16x32_bf16 v[200:203], a[16:19], a[104:107], v[200:203]\n"
  "v_mov_b32 v65, v81\n"
  "v_mfma_f32_16x16x32_bf16 v[204:207], a[16:19], a[108:111], v[204:207]\n"
  "v_mov_b32 v66, v82\n"
  "v_mfma_f32_16x16x32_bf16 v[208:211], a[20:23], a[96:99], v[208:211]\n"
  "buffer_load_dwordx4 a[88:91], v2, s[4:7], s21 offen offset:512 sc1\n"
  "v_mfma_f32_16x16x32_bf16 v[212:215], a[20:23], a[100:103], v[212:215]\n"
  "v_mov_b32 v67, v83\n"
  "v_mfma_f32_16x16x32_bf16 v[216:219], a[20:23], a[104:107], v[216:219]\n"
  "v_mov_b32 v80, v96\n"
  "v_mfma_f32_16x16x32_bf16 v[220:223], a[20:23], a[108:111], v[220:223]\n"
  "v_mov_b32 v81, v97\n"
  "v_mfma_f32_16x16x32_bf16 v[224:227], a[24:27], a[96:99], v[224:227]\n"
  "v_mov_b32 v82, v98\n"
  "v_mfma_f32_16x16x32_bf16 v[228:231], a[24:27], a[100:103], v[228:231]\n"
  "v_mov_b32 v83, v99\n"
  "v_mfma_f32_16x16x32_bf16 v[232:235], a[24:27], a[104:107], v[232:235]\n"
  "v_mov_b32 v96, v112\n"
  "v_mfma_f32_16x16x32_bf16 v[236:239], a[24:27], a[108:111], v[236:239]\n"
  "v_mov_b32 v97, v113\n"
  "v_mfma_f32_16x16x32_bf16 v[240:243], a[28:31], a[96:99], v[240:243]\n"
  "buffer_load_dwordx4 a[92:95], v2, s[4:7], s21 offen offset:768 sc1\n"
  "v_mfma_f32_16x16x32_bf16 v[244:247], a[28:31], a[100:103], v[244:247]\n"
  "s_add_u32 s21, s21, 0x4000\n"
  "v_mfma_f32_16x16x32_bf16 v[248:251], a[28:31], a[104:107], v[248:251]\n"
  "v_mov_b32 v98, v114\n"
  "v_mfma_f32_16x16x32_bf16 v[252:255], a[28:31], a[108:111], v[252:255]\n"
  "v_mov_b32 v99, v115\n"
  "s_waitcnt vmcnt(31) lgkmcnt(0)\n"
  "s_barrier\n"
  "v_mfma_f32_16x16x32_bf16 v[128:131], a[32:35], a[112:115], v[128:131]\n"
  "ds_read_b64_tr_b16 a[0:1], v7 offset:32768\n"
  "v_mfma_f32_16x16x32_bf16 v[132:135], a[32:35], a[116:119], v[132:135]\n"
  "ds_read_b64_tr_b16 a[2:3], v7 offset:36864\n"
  "v_mfma_f32_16x16x32_bf16 v[136:139], a[32:35], a[120:123], v[136:139]\n"
  "ds_read_b64_tr_b16 a[4:5], v8 offset:32768\n"
  "v_mfma_f32_16x16x32_bf16 v[140:143], a[32:35], a[124:127], v[140:143]\n"
  "ds_read_b64_tr_b16 a[6:7], v8 offset:36864\n"
  "buffer_load_dwordx4 a[96:99], v2, s[4:7], s21 offen sc1\n"
  "v_mfma_f32_16x16x32_bf16 v[144:147], a[36:39], a[112:115], v[144:147]\n"
  "ds_read_b64_tr_b16 a[8:9], v9 offset:32768\n"
  "v_mfma_f32_16x16x32_bf16 v[148:151], a[36:39], a[116:119], v[148:151]\n"
  "ds_read_b64_tr_b16 a[10:11], v9 offset:36864\n"
  "v_mfma_f32_16x16x32_bf16 v[152:155], a[36:39], a[120:123], v[152:155]\n"
  "ds_read_b64_tr_b16 a[12:13], v10 offset:32768\n"
  "v_mfma_f32_16x16x32_bf16 v[156:159], a[36:39], a[124:127], v[156:159]\n"
  "ds_read_b64_tr_b16 a[14:15], v10 offset:36864\n"
  "buffer_load_dwordx4 a[100:103], v2, s[4:7], s21 offen offset:256 sc1\n"
  "v_mfma_f32_16x16x32_bf16 v[160:163], a[40:43], a[112:115], v[160:163]\n"
  "ds_read_b64_tr_b16 a[16:17], v11 offset:32768\n"
  "v_mfma_f32_16x16x32_bf16 v[164:167], a[40:43], a[116:119], v[164:167]\n"
  "ds_read_b64_tr_b16 a[18:19], v11 offset:36864\n"
  "v_mfma_f32_16x16x32_bf16 v[168:171], a[40:43], a[120:123], v[168:171]\n"
  "ds_read_b64_tr_b16 a[20:21], v12 offset:32768\n"
  "v_mfma_f32_16x16x32_bf16 v[172:175], a[40:43], a[124:127], v[172:175]\n"
  "ds_read_b64_tr_b16 a[22:23], v12 offset:36864\n"
  "buffer_load_dwordx4 a[104:107], v2, s[4:7], s21 offen offset:512 sc1\n"
  "v_mfma_f32_16x16x32_bf16 v[176:179], a[44:47], a[112:115], v[176:179]\n"
  "ds_read_b64_tr_b16 a[24:25], v13 offset:32768\n"
  "v_mfma_f32_16x16x32_bf16 v[180:183], a[44:47], a[116:119], v[180:183]\n"
  "ds_read_b64_tr_b16 a[26:27], v13 offset:36864\n"
  "v_mfma_f32_16x16x32_bf16 v[184:187], a[44:47], a[120:123], v[184:187]\n"
  "ds_read_b64_tr_b16 a[28:29], v14 offset:32768\n"
  "v_mfma_f32_16x16x32_bf16 v[188:191], a[44:47], a[124:127], v[188:191]\n"
  "ds_read_b64_tr_b16 a[30:31], v14 offset:36864\n"
  "buffer_load_dwordx4 a[108:111], v2, s[4:7], s21 offen offset:768 sc1\n"
  "v_mfma_f32_16x16x32_bf16 v[192:195], a[48:51], a[112:115], v[192:195]\n"
  "s_add_u32 s21, s21, 0x4000\n"
  "v_mfma_f32_16x16x32_bf16 v[196:199], a[48:51], a[116:119], v[196:199]\n"
  "v_mfma_f32_16x16x32_bf16 v[200:203], a[48:51], a[120:123], v[200:203]\n"
  "s_add_u32 m0, s24, 0x4000\n"
  "v_mfma_f32_16x16x32_bf16 v[204:207], a[48:51], a[124:127], v[204:207]\n"
  "buffer_load_dwordx4 v3, s[8:11], s22 offen lds\n"
  "v_mfma_f32_16x16x32_bf16 v[208:211], a[52:55], a[112:115], v[208:211]\n"
  "v_mfma_f32_16x16x32_bf16 v[212:215], a[52:55], a[116:119], v[212:215]\n"
  "v_mfma_f32_16x16x32_bf16 v[216:219], a[52:55], a[120:123], v[216:219]\n"
  "buffer_load_dwordx4 v4, s[8:11], s22 offen offset:1024 lds\n"
  "v_mfma_f32_16x16x32_bf16 v[220:223], a[52:55], a[124:127], v[220:223]\n"
  "v_mfma_f32_16x16x32_bf16 v[224:227], a[56:59], a[112:115], v[224:227]\n"
  "v_mfma_f32_16x16x32_bf16 v[228:231], a[56:59], a[116:119], v[228:231]\n"
  "buffer_load_dwordx4 v5, s[8:11], s22 offen offset:2048 lds\n"
  "v_mfma_f32_16x16x32_bf16 v[232:235], a[56:59], a[120:123], v[232:235]\n"
  "v_mfma_f32_16x16x32_bf16 v[236:239], a[56:59], a[124:127], v[236:239]\n"
  "v_mfma_f32_16x16x32_bf16 v[240:243], a[60:63], a[112:115], v[240:243]\n"
  "buffer_load_dwordx4 v6, s[8:11], s22 offen offset:3072 lds\n"
  "v_mfma_f32_16x16x32_bf16 v[244:247], a[60:63], a[116:119], v[244:247]\n"
  "s_add_u32 s22, s22, s23\n"
  "v_mfma_f32_16x16x32_bf16 v[248:251], a[60:63], a[120:123], v[248:251]\n"
  "v_mfma_f32_16x16x32_bf16 v[252:255], a[60:63], a[124:127], v[252:255]\n"
  "s_waitcnt lgkmcnt(14)\n"
  "v_mfma_f32_16x16x32_bf16 v[128:131], a[0:3], a[128:131], v[128:131]\n"
  "ds_read_b64_tr_b16 a[32:33], v7 offset:40960\n"
  "v_mfma_f32_16x16x32_bf16 v[132:135], a[0:3], a[132:135], v[132:135]\n"
  "ds_read_b64_tr_b16 a[34:35], v7 offset:45056\n"
  "v_mfma_f32_16x16x32_bf16 v[136:139], a[0:3], a[136:139], v[136:139]\n"
  "ds_read_b64_tr_b16 a[36:37], v8 offset:40960\n"
  "v_mfma_f32_16x16x32_bf16 v[140:143], a[0:3], a[140:143], v[140:143]\n"
  "ds_read_b64_tr_b16 a[38:39], v8 offset:45056\n"
  "v_mfma_f32_16x16x32_bf16 v[144:147], a[4:7], a[128:131], v[144:147]\n"
  "ds_read_b64_tr_b16 a[40:41], v9 offset:40960\n"
  "buffer_load_dwordx4 a[112:115], v2, s[4:7], s21 offen sc1\n"
  "v_mfma_f32_16x16x32_bf16 v[148:151], a[4:7], a[132:135], v[148:151]\n"
  "ds_read_b64_tr_b16 a[42:43], v9 offset:45056\n"
  "v_mfma_f32_16x16x32_bf16 v[152:155], a[4:7], a[136:139], v[152:155]\n"
  "ds_read_b64_tr_b16 a[44:45], v10 offset:40960\n"
  "v_mfma_f32_16x16x32_bf16 v[156:159], a[4:7], a[140:143], v[156:159]\n"
  "ds_read_b64_tr_b16 a[46:47], v10 offset:45056\n"
  "v_mfma_f32_16x16x32_bf16 v[160:163], a[8:11], a[128:131], v[160:163]\n"
  "ds_read_b64_tr_b16 a[48:49], v11 offset:40960\n"
  "v_mfma_f32_16x16x32_bf16 v[164:167], a[8:11], a[132:135], v[164:167]\n"
  "ds_read_b64_tr_b16 a[50:51], v11 offset:45056\n"
  "v_mfma_f32_16x16x32_bf16 v[168:171], a[8:11], a[136:139], v[168:171]\n"
  "ds_read_b64_tr_b16 a[52:53], v12 offset:40960\n"
  "v_mfma_f32_16x16x32_bf16 v[172:175], a[8:11], a[140:143], v[172:175]\n"
  "ds_read_b64_tr_b16 a[54:55], v12 offset:45056\n"
  "v_mfma_f32_16x16x32_bf16 v[176:179], a[12:15], a[128:131], v[176:179]\n"
  "ds_read_b64_tr_b16 a[56:57], v13 offset:40960\n"
  "buffer_load_dwordx4 a[116:119], v2, s[4:7], s21 offen offset:256 sc1\n"
  "v_mfma_f32_16x16x32_bf16 v[180:183], a[12:15], a[132:135], v[180:183]\n"
  "ds_read_b64_tr_b16 a[58:59], v13 offset:45056\n"
  "v_mfma_f32_16x16x32_bf16 v[184:187], a[12:15], a[136:139], v[184:187]\n"
  "ds_read_b64_tr_b16 a[60:61], v14 offset:40960\n"
  "v_mfma_f32_16x16x32_bf16 v[188:191], a[12:15], a[140:143], v[188:191]\n"
  "ds_read_b64_tr_b16 a[62:63], v14 offset:45056\n"
  "v_mfma_f32_16x16x32_bf16 v[192:195], a[16:19], a[128:131], v[192:195]\n"
  "buffer_store_dwordx4 v[72:75], v2, s[12:15], s91 offen offset:512 sc1\n"
  "v_mfma_f32_16x16x32_bf16 v[196:199], a[16:19], a[132:135], v[196:199]\n"
  "v_mov_b32 v68, v84\n"
  "v_mfma_f32_16x16x32_bf16 v[200:203], a[16:19], a[136:139], v[200:203]\n"
  "v_mov_b32 v69, v85\n"
  "v_mfma_f32_16x16x32_bf16 v[204:207], a[16:19], a[140:143], v[204:207]\n"
  "v_mov_b32 v70, v86\n"
  "v_mfma_f32_16x16x32_bf16 v[208:211], a[20:23], a[128:131], v[208:211]\n"
  "buffer_load_dwordx4 a[120:123], v2, s[4:7], s21 offen offset:512 sc1\n"
  "v_mfma_f32_16x16x32_bf16 v[212:215], a[20:23], a[132:135], v[212:215]\n"
  "v_mov_b32 v71, v87\n"
  "v_mfma_f32_16x16x32_bf16 v[216:219], a[20:23], a[136:139], v[216:219]\n"
  "v_mov_b32 v84, v100\n"
  "v_mfma_f32_16x16x32_bf16 v[220:223], a[20:23], a[140:143], v[220:223]\n"
  "v_mov_b32 v85, v101\n"
  "v_mfma_f32_16x16x32_bf16 v[224:227], a[24:27], a[128:131], v[224:227]\n"
  "v_mov_b32 v86, v102\n"
  "v_mfma_f32_16x16x32_bf16 v[228:231], a[24:27], a[132:135], v[228:231]\n"
  "v_mov_b32 v87, v103\n"
  "v_mfma_f32_16x16x32_bf16 v[232:235], a[24:27], a[136:139], v[232:235]\n"
  "v_mov_b32 v100, v116\n"
  "v_mfma_f32_16x16x32_bf16 v[236:239], a[24:27], a[140:143], v[236:239]\n"
  "v_mov_b32 v101, v117\n"
  "v_mfma_f32_16x16x32_bf16 v[240:243], a[28:31], a[128:131], v[240:243]\n"
  "buffer_load_dwordx4 a[124:127], v2, s[4:7], s21 offen offset:768 sc1\n"
  "v_mfma_f32_16x16x32_bf16 v[244:247], a[28:31], a[132:135], v[244:247]\n"
  "s_add_u32 s21, s21, 0x4000\n"
  "v_mfma_f32_16x16x32_bf16 v[248:251], a[28:31], a[136:139], v[248:251]\n"
  "v_mov_b32 v102, v118\n"
  "v_mfma_f32_16x16x32_bf16 v[252:255], a[28:31], a[140:143], v[252:255]\n"
  "v_mov_b32 v103, v119\n"
  "s_waitcnt vmcnt(31) lgkmcnt(0)\n"
  "s_barrier\n"
  "v_mfma_f32_16x16x32_bf16 v[128:131], a[32:35], a[144:147], v[128:131]\n"
  "ds_read_b64_tr_b16 a[0:1], v7 offset:49152\n"
  "v_mfma_f32_16x16x32_bf16 v[132:135], a[32:35], a[148:151], v[132:135]\n"
  "ds_read_b64_tr_b16 a[2:3], v7 offset:53248\n"
  "v_mfma_f32_16x16x32_bf16 v[136:139], a[32:35], a[152:155], v[136:139]\n"
  "ds_read_b64_tr_b16 a[4:5], v8 offset:49152\n"
  "v_mfma_f32_16x16x32_bf16 v[140:143], a[32:35], a[156:159], v[140:143]\n"
  "ds_read_b64_tr_b16 a[6:7], v8 offset:53248\n"
  "buffer_load_dwordx4 a[128:131], v2, s[4:7], s21 offen sc1\n"
  "v_mfma_f32_16x16x32_bf16 v[144:147], a[36:39], a[144:147], v[144:147]\n"
  "ds_read_b64_tr_b16 a[8:9], v9 offset:49152\n"
  "v_mfma_f32_16x16x32_bf16 v[148:151], a[36:39], a[148:151], v[148:151]\n"
  "ds_read_b64_tr_b16 a[10:11], v9 offset:53248\n"
  "v_mfma_f32_16x16x32_bf16 v[152:155], a[36:39], a[152:155], v[152:155]\n"
  "ds_read_b64_tr_b16 a[12:13], v10 offset:49152\n"
  "v_mfma_f32_16x16x32_bf16 v[156:159], a[36:39], a[156:159], v[156:159]\n"
  "ds_read_b64_tr_b16 a[14:15], v10 offset:53248\n"
  "buffer_load_dwordx4 a[132:135], v2, s[4:7], s21 offen offset:256 sc1\n"
  "v_mfma_f32_16x16x32_bf16 v[160:163], a[40:43], a[144:147], v[160:163]\n"
  "ds_read_b64_tr_b16 a[16:17], v11 offset:49152\n"
  "v_mfma_f32_16x16x32_bf16 v[164:167], a[40:43], a[148:151], v[164:167]\n"
  "ds_read_b64_tr_b16 a[18:19], v11 offset:53248\n"
  "v_mfma_f32_16x16x32_bf16 v[168:171], a[40:43], a[152:155], v[168:171]\n"
  "ds_read_b64_tr_b16 a[20:21], v12 offset:49152\n"
  "v_mfma_f32_16x16x32_bf16 v[172:175], a[40:43], a[156:159], v[172:175]\n"
  "ds_read_b64_tr_b16 a[22:23], v12 offset:53248\n"
  "buffer_load_dwordx4 a[136:139], v2, s[4:7], s21 offen offset:512 sc1\n"
  "v_mfma_f32_16x16x32_bf16 v[176:179], a[44:47], a[144:147], v[176:179]\n"
  "ds_read_b64_tr_b16 a[24:25], v13 offset:49152\n"
  "v_mfma_f32_16x16x32_bf16 v[180:183], a[44:47], a[148:151], v[180:183]\n"
  "ds_read_b64_tr_b16 a[26:27], v13 offset:53248\n"
  "v_mfma_f32_16x16x32_bf16 v[184:187], a[44:47], a[152:155], v[184:187]\n"
  "ds_read_b64_tr_b16 a[28:29], v14 offset:49152\n"
  "v_mfma_f32_16x16x32_bf16 v[188:191], a[44:47], a[156:159], v[188:191]\n"
  "ds_read_b64_tr_b16 a[30:31], v14 offset:53248\n"
  "buffer_load_dwordx4 a[140:143], v2, s[4:7], s21 offen offset:768 sc1\n"
  "v_mfma_f32_16x16x32_bf16 v[192:195], a[48:51], a[144:147], v[192:195]\n"
  "s_add_u32 s21, s21, 0x4000\n"
  "v_mfma_f32_16x16x32_bf16 v[196:199], a[48:51], a[148:151], v[196:199]\n"
  "v_mfma_f32_16x16x32_bf16 v[200:203], a[48:51], a[152:155], v[200:203]\n"
  "s_add_u32 m0, s24, 0x8000\n"
  "v_mfma_f32_16x16x32_bf16 v[204:207], a[48:51], a[156:159], v[204:207]\n"
  "buffer_load_dwordx4 v3, s[8:11], s22 offen lds\n"
  "v_mfma_f32_16x16x32_bf16 v[208:211], a[52:55], a[144:147], v[208:211]\n"
  "v_mfma_f32_16x16x32_bf16 v[212:215], a[52:55], a[148:151], v[212:215]\n"
  "v_mfma_f32_16x16x32_bf16 v[216:219], a[52:55], a[152:155], v[216:219]\n"
  "buffer_load_dwordx4 v4, s[8:11], s22 offen offset:1024 lds\n"
  "v_mfma_f32_16x16x32_bf16 v[220:223], a[52:55], a[156:159], v[220:223]\n"
  "v_mfma_f32_16x16x32_bf16 v[224:227], a[56:59], a[144:147], v[224:227]\n"
  "v_mfma_f32_16x16x32_bf16 v[228:231], a[56:59], a[148:151], v[228:231]\n"
  "buffer_load_dwordx4 v5, s[8:11], s22 offen offset:2048 lds\n"
  "v_mfma_f32_16x16x32_bf16 v[232:235], a[56:59], a[152:155], v[232:235]\n"
  "v_mfma_f32_16x16x32_bf16 v[236:239], a[56:59], a[156:159], v[236:239]\n"
  "v_mfma_f32_16x16x32_bf16 v[240:243], a[60:63], a[144:147], v[240:243]\n"
  "buffer_load_dwordx4 v6, s[8:11], s22 offen offset:3072 lds\n"
  "v_mfma_f32_16x16x32_bf16 v[244:247], a[60:63], a[148:151], v[244:247]\n"
  "s_add_u32 s22, s22, s23\n"
  "v_mfma_f32_16x16x32_bf16 v[248:251], a[60:63], a[152:155], v[248:251]\n"
  "v_mfma_f32_16x16x32_bf16 v[252:255], a[60:63], a[156:159], v[252:255]\n"
  "s_waitcnt lgkmcnt(14)\n"
  "v_mfma_f32_16x16x32_bf16 v[128:131], a[0:3], a[160:163], v[128:131]\n"
  "ds_read_b64_tr_b16 a[32:33], v7 offset:57344\n"
  "v_mfma_f32_16x16x32_bf16 v[132:135], a[0:3], a[164:167], v[132:135]\n"
  "ds_read_b64_tr_b16 a[34:35], v7 offset:61440\n"
  "v_mfma_f32_16x16x32_bf16 v[136:139], a[0:3], a[168:171], v[136:139]\n"
  "ds_read_b64_tr_b16 a[36:37], v8 offset:57344\n"
  "v_mfma_f32_16x16x32_bf16 v[140:143], a[0:3], a[172:175], v[140:143]\n"
  "ds_read_b64_tr_b16 a[38:39], v8 offset:61440\n"
  "v_mfma_f32_16x16x32_bf16 v[144:147], a[4:7], a[160:163], v[144:147]\n"
  "ds_read_b64_tr_b16 a[40:41], v9 offset:57344\n"
  "buffer_load_dwordx4 a[144:147], v2, s[4:7], s21 offen sc1\n"
  "v_mfma_f32_16x16x32_bf16 v[148:151], a[4:7], a[164:167], v[148:151]\n"
  "ds_read_b64_tr_b16 a[42:43], v9 offset:61440\n"
  "v_mfma_f32_16x16x32_bf16 v[152:155], a[4:7], a[168:171], v[152:155]\n"
  "ds_read_b64_tr_b16 a[44:45], v10 offset:57344\n"
  "v_mfma_f32_16x16x32_bf16 v[156:159], a[4:7], a[172:175], v[156:159]\n"
  "ds_read_b64_tr_b16 a[46:47], v10 offset:61440\n"
  "v_mfma_f32_16x16x32_bf16 v[160:163], a[8:11], a[160:163], v[160:163]\n"
  "ds_read_b64_tr_b16 a[48:49], v11 offset:57344\n"
  "v_mfma_f32_16x16x32_bf16 v[164:167], a[8:11], a[164:167], v[164:167]\n"
  "ds_read_b64_tr_b16 a[50:51], v11 offset:61440\n"
  "v_mfma_f32_16x16x32_bf16 v[168:171], a[8:11], a[168:171], v[168:171]\n"
  "ds_read_b64_tr_b16 a[52:53], v12 offset:57344\n"
  "v_mfma_f32_16x16x32_bf16 v[172:175], a[8:11], a[172:175], v[172:175]\n"
  "ds_read_b64_tr_b16 a[54:55], v12 offset:61440\n"
  "v_mfma_f32_16x16x32_bf16 v[176:179], a[12:15], a[160:163], v[176:179]\n"
  "ds_read_b64_tr_b16 a[56:57], v13 offset:57344\n"
  "buffer_load_dwordx4 a[148:151], v2, s[4:7], s21 offen offset:256 sc1\n"
  "v_mfma_f32_16x16x32_bf16 v[180:183], a[12:15], a[164:167], v[180:183]\n"
  "ds_read_b64_tr_b16 a[58:59], v13 offset:61440\n"
  "v_mfma_f32_16x16x32_bf16 v[184:187], a[12:15], a[168:171], v[184:187]\n"
  "ds_read_b64_tr_b16 a[60:61], v14 offset:57344\n"
  "v_mfma_f32_16x16x32_bf16 v[188:191], a[12:15], a[172:175], v[188:191]\n"
  "ds_read_b64_tr_b16 a[62:63], v14 offset:61440\n"
  "v_mfma_f32_16x16x32_bf16 v[192:195], a[16:19], a[160:163], v[192:195]\n"
  "buffer_store_dwordx4 v[76:79], v2, s[12:15], s91 offen offset:768 sc1\n"
  "s_add_u32 s91, s91, 0x4000\n"
  "v_mfma_f32_16x16x32_bf16 v[196:199], a[16:19], a[164:167], v[196:199]\n"
  "v_mov_b32 v72, v88\n"
  "v_mfma_f32_16x16x32_bf16 v[200:203], a[16:19], a[168:171], v[200:203]\n"
  "v_mov_b32 v73, v89\n"
  "v_mfma_f32_16x16x32_bf16 v[204:207], a[16:19], a[172:175], v[204:207]\n"
  "v_mov_b32 v74, v90\n"
  "v_mfma_f32_16x16x32_bf16 v[208:211], a[20:23], a[160:163], v[208:211]\n"
  "buffer_load_dwordx4 a[152:155], v2, s[4:7], s21 offen offset:512 sc1\n"
  "v_mfma_f32_16x16x32_bf16 v[212:215], a[20:23], a[164:167], v[212:215]\n"
  "v_mov_b32 v75, v91\n"
  "v_mfma_f32_16x16x32_bf16 v[216:219], a[20:23], a[168:171], v[216:219]\n"
  "v_mov_b32 v88, v104\n"
  "v_mfma_f32_16x16x32_bf16 v[220:223], a[20:23], a[172:175], v[220:223]\n"
  "v_mov_b32 v89, v105\n"
  "v_mfma_f32_16x16x32_bf16 v[224:227], a[24:27], a[160:163], v[224:227]\n"
  "v_mov_b32 v90, v106\n"
  "v_mfma_f32_16x16x32_bf16 v[228:231], a[24:27], a[164:167], v[228:231]\n"
  "v_mov_b32 v91, v107\n"
  "v_mfma_f32_16x16x32_bf16 v[232:235], a[24:27], a[168:171], v[232:235]\n"
  "v_mov_b32 v104, v120\n"
  "v_mfma_f32_16x16x32_bf16 v[236:239], a[24:27], a[172:175], v[236:239]\n"
  "v_mov_b32 v105, v121\n"
  "v_mfma_f32_16x16x32_bf16 v[240:243], a[28:31], a[160:163], v[240:243]\n"
  "buffer_load_dwordx4 a[156:159], v2, s[4:7], s21 offen offset:768 sc1\n"
  "v_mfma_f32_16x16x32_bf16 v[244:247], a[28:31], a[164:167], v[244:247]\n"
  "s_add_u32 s21, s21, 0x4000\n"
  "v_mfma_f32_16x16x32_bf16 v[248:251], a[28:31], a[168:171], v[248:251]\n"
  "v_mov_b32 v106, v122\n"
  "v_mfma_f32_16x16x32_bf16 v[252:255], a[28:31], a[172:175], v[252:255]\n"
  "v_mov_b32 v107, v123\n"
  "s_waitcnt vmcnt(31) lgkmcnt(0)\n"
  "s_barrier\n"
  "v_mfma_f32_16x16x32_bf16 v[128:131], a[32:35], a[176:179], v[128:131]\n"
  "ds_read_b64_tr_b16 a[0:1], v7\n"
  "v_mfma_f32_16x16x32_bf16 v[132:135], a[32:35], a[180:183], v[132:135]\n"
  "ds_read_b64_tr_b16 a[2:3], v7 offset:4096\n"
  "v_mfma_f32_16x16x32_bf16 v[136:139], a[32:35], a[184:187], v[136:139]\n"
  "ds_read_b64_tr_b16 a[4:5], v8\n"
  "v_mfma_f32_16x16x32_bf16 v[140:143], a[32:35], a[188:191], v[140:143]\n"
  "ds_read_b64_tr_b16 a[6:7], v8 offset:4096\n"
  "buffer_load_dwordx4 a[160:163], v2, s[4:7], s21 offen sc1\n"
  "v_mfma_f32_16x16x32_bf16 v[144:147], a[36:39], a[176:179], v[144:147]\n"
  "ds_read_b64_tr_b16 a[8:9], v9\n"
  "v_mfma_f32_16x16x32_bf16 v[148:151], a[36:39], a[180:183], v[148:151]\n"
  "ds_read_b64_tr_b16 a[10:11], v9 offset:4096\n"
  "v_mfma_f32_16x16x32_bf16 v[152:155], a[36:39], a[184:187], v[152:155]\n"
  "ds_read_b64_tr_b16 a[12:13], v10\n"
  "v_mfma_f32_16x16x32_bf16 v[156:159], a[36:39], a[188:191], v[156:159]\n"
  "ds_read_b64_tr_b16 a[14:15], v10 offset:4096\n"
  "buffer_load_dwordx4 a[164:167], v2, s[4:7], s21 offen offset:256 sc1\n"
  "v_mfma_f32_16x16x32_bf16 v[160:163], a[40:43], a[176:179], v[160:163]\n"
  "ds_read_b64_tr_b16 a[16:17], v11\n"
  "v_mfma_f32_16x16x32_bf16 v[164:167], a[40:43], a[180:183], v[164:167]\n"
  "ds_read_b64_tr_b16 a[18:19], v11 offset:4096\n"
  "v_mfma_f32_16x16x32_bf16 v[168:171], a[40:43], a[184:187], v[168:171]\n"
  "ds_read_b64_tr_b16 a[20:21], v12\n"
  "v_mfma_f32_16x16x32_bf16 v[172:175], a[40:43], a[188:191], v[172:175]\n"
  "ds_read_b64_tr_b16 a[22:23], v12 offset:4096\n"
  "buffer_load_dwordx4 a[168:171], v2, s[4:7], s21 offen offset:512 sc1\n"
  "v_mfma_f32_16x16x32_bf16 v[176:179], a[44:47], a[176:179], v[176:179]\n"
  "ds_read_b64_tr_b16 a[24:25], v13\n"
  "v_mfma_f32_16x16x32_bf16 v[180:183], a[44:47], a[180:183], v[180:183]\n"
  "ds_read_b64_tr_b16 a[26:27], v13 offset:4096\n"
  "v_mfma_f32_16x16x32_bf16 v[184:187], a[44:47], a[184:187], v[184:187]\n"
  "ds_read_b64_tr_b16 a[28:29], v14\n"
  "v_mfma_f32_16x16x32_bf16 v[188:191], a[44:47], a[188:191], v[188:191]\n"
  "ds_read_b64_tr_b16 a[30:31], v14 offset:4096\n"
  "buffer_load_dwordx4 a[172:175], v2, s[4:7], s21 offen offset:768 sc1\n"
  "v_mfma_f32_16x16x32_bf16 v[192:195], a[48:51], a[176:179], v[192:195]\n"
  "s_add_u32 s21, s21, 0x4000\n"
  "v_mfma_f32_16x16x32_bf16 v[196:199], a[48:51], a[180:183], v[196:199]\n"
  "v_mfma_f32_16x16x32_bf16 v[200:203], a[48:51], a[184:187], v[200:203]\n"
  "s_add_u32 m0, s24, 0xc000\n"
  "v_mfma_f32_16x16x32_bf16 v[204:207], a[48:51], a[188:191], v[204:207]\n"
  "buffer_load_dwordx4 v3, s[8:11], s22 offen lds\n"
  "v_mfma_f32_16x16x32_bf16 v[208:211], a[52:55], a[176:179], v[208:211]\n"
  "v_mfma_f32_16x16x32_bf16 v[212:215], a[52:55], a[180:183], v[212:215]\n"
  "v_mfma_f32_16x16x32_bf16 v[216:219], a[52:55], a[184:187], v[216:219]\n"
  "buffer_load_dwordx4 v4, s[8:11], s22 offen offset:1024 lds\n"
  "v_mfma_f32_16x16x32_bf16 v[220:223], a[52:55], a[188:191], v[220:223]\n"
  "v_mfma_f32_16x16x32_bf16 v[224:227], a[56:59], a[176:179], v[224:227]\n"
  "v_mfma_f32_16x16x32_bf16 v[228:231], a[56:59], a[180:183], v[228:231]\n"
  "buffer_load_dwordx4 v5, s[8:11], s22 offen offset:2048 lds\n"
  "v_mfma_f32_16x16x32_bf16 v[232:235], a[56:59], a[184:187], v[232:235]\n"
  "v_mfma_f32_16x16x32_bf16 v[236:239], a[56:59], a[188:191], v[236:239]\n"
  "v_mfma_f32_16x16x32_bf16 v[240:243], a[60:63], a[176:179], v[240:243]\n"
  "buffer_load_dwordx4 v6, s[8:11], s22 offen offset:3072 lds\n"
  "v_mfma_f32_16x16x32_bf16 v[244:247], a[60:63], a[180:183], v[244:247]\n"
  "s_add_u32 s22, s22, s23\n"
  "v_mfma_f32_16x16x32_bf16 v[248:251], a[60:63], a[184:187], v[248:251]\n"
  "v_mfma_f32_16x16x32_bf16 v[252:255], a[60:63], a[188:191], v[252:255]\n"
  "s_sub_u32 s20, s20, 1\n"
  "s_cmp_lg_u32 s20, 0\n"
  "s_cbranch_scc1 GL_%=\n"
  "GEMM_RET_%=:\n"
  "s_nop 7\n"
  "s_setpc_b64 s[48:49]\n"
  "KEND_%=:\n"
  :: "s"(__builtin_amdgcn_kernarg_segment_ptr()), "s"(__builtin_amdgcn_workgroup_id_x()), "v"(__builtin_amdgcn_workitem_id_x())
  : "a0", "a1", "a2", "a3", "a4", "a5", "a6", "a7", "a8", "a9", "a10", "a11", "a12", "a13", "a14", "a15", "a16", "a17", "a18", "a19", "a20", "a21", "a22", "a23", "a24", "a25", "a26", "a27", "a28", "a29", "a30", "a31", "a32", "a33", "a34", "a35", "a36", "a37", "a38", "a39", "a40", "a41", "a42", "a43", "a44", "a45", "a46", "a47", "a48", "a49", "a50", "a51", "a52", "a53", "a54", "a55", "a56", "a57", "a58", "a59", "a60", "a61", "a62", "a63", "a64", "a65", "a66", "a67", "a68", "a69", "a70", "a71", "a72", "a73", "a74", "a75", "a76", "a77", "a78", "a79", "a80", "a81", "a82", "a83", "a84", "a85", "a86", "a87", "a88", "a89", "a90", "a91", "a92", "a93", "a94", "a95", "a96", "a97", "a98", "a99", "a100", "a101", "a102", "a103", "a104", "a105", "a106", "a107", "a108", "a109", "a110", "a111", "a112", "a113", "a114", "a115", "a116", "a117", "a118", "a119", "a120", "a121", "a122", "a123", "a124", "a125", "a126", "a127", "a128", "a129", "a130", "a131", "a132", "a133", "a134", "a135", "a136", "a137", "a138", "a139", "a140", "a141", "a142", "a143", "a144", "a145", "a146", "a147", "a148", "a149", "a150", "a151", "a152", "a153", "a154", "a155", "a156", "a157", "a158", "a159", "a160", "a161", "a162", "a163", "a164", "a165", "a166", "a167", "a168", "a169", "a170", "a171", "a172", "a173", "a174", "a175", "a176", "a177", "a178", "a179", "a180", "a181", "a182", "a183", "a184", "a185", "a186", "a187", "a188", "a189", "a190", "a191", "a192", "a193", "a194", "a195", "a196", "a197", "a198", "a199", "a200", "a201", "a202", "a203", "a204", "a205", "a206", "a207", "a208", "a209", "a210", "a211", "a212", "a213", "a214", "a215", "a216", "a217", "a218", "a219", "a220", "a221", "a222", "a223", "a224", "a225", "a226", "a227", "a228", "a229", "a230", "a231", "a232", "a233", "a234", "a235", "a236", "a237", "a238", "a239", "a240", "a241", "a242", "a243", "a244", "a245", "a246", "a247", "a248", "a249", "a250", "a251", "a252", "a253", "a254", "a255", "s4", "s5", "s6", "s7", "s8", "s9", "s10", "s11", "s12", "s13", "s14", "s15", "s16", "s17", "s18", "s19", "s20", "s21", "s22", "s23", "s24", "s25", "s26", "s27", "s28", "s29", "s30", "s31", "s34", "s35", "s36", "s37", "s38", "s39", "s40", "s41", "s42", "s43", "s44", "s45", "s46", "s47", "s48", "s49", "s50", "s51", "s52", "s53", "s54", "s55", "s56", "s57", "s58", "s59", "s60", "s61", "s62", "s63", "s64", "s65", "s66", "s67", "s68", "s69", "s70", "s71", "s72", "s73", "s74", "s75", "s76", "s77", "s78", "s79", "s80", "s81", "s82", "s83", "s84", "s85", "s86", "s87", "s88", "s89", "s90", "s91", "s92", "s93", "s94", "s95", "s96", "s97", "s98", "s99", "vcc", "memory", "v1", "v2", "v3", "v4", "v5", "v6", "v7", "v8", "v9", "v10", "v11", "v12", "v13", "v14", "v15", "v16", "v17", "v18", "v19", "v20", "v21", "v22", "v23", "v24", "v25", "v26", "v27", "v28", "v29", "v30", "v31", "v32", "v33", "v34", "v35", "v36", "v37", "v38", "v39", "v40", "v41", "v42", "v43", "v44", "v45", "v46", "v47", "v48", "v49", "v50", "v51", "v52", "v53", "v54", "v55", "v56", "v57", "v58", "v59", "v60", "v61", "v62", "v63", "v64", "v65", "v66", "v67", "v68", "v69", "v70", "v71", "v72", "v73", "v74", "v75", "v76", "v77", "v78", "v79", "v80", "v81", "v82", "v83", "v84", "v85", "v86", "v87", "v88", "v89", "v90", "v91", "v92", "v93", "v94", "v95", "v96", "v97", "v98", "v99", "v100", "v101", "v102", "v103", "v104", "v105", "v106", "v107", "v108", "v109", "v110", "v111", "v112", "v113", "v114", "v115", "v116", "v117", "v118", "v119", "v120", "v121", "v122", "v123", "v124", "v125", "v126", "v127", "v128", "v129", "v130", "v131", "v132", "v133", "v134", "v135", "v136", "v137", "v138", "v139", "v140", "v141", "v142", "v143", "v144", "v145", "v146", "v147", "v148", "v149", "v150", "v151", "v152", "v153", "v154", "v155", "v156", "v157", "v158", "v159", "v160", "v161", "v162", "v163", "v164", "v165", "v166", "v167", "v168", "v169", "v170", "v171", "v172", "v173", "v174", "v175", "v176", "v177", "v178", "v179", "v180", "v181", "v182", "v183", "v184", "v185", "v186", "v187", "v188", "v189", "v190", "v191", "v192", "v193", "v194", "v195", "v196", "v197", "v198", "v199", "v200", "v201", "v202", "v203", "v204", "v205", "v206", "v207", "v208", "v209", "v210", "v211", "v212", "v213", "v214", "v215", "v216", "v217", "v218", "v219", "v220", "v221", "v222", "v223", "v224", "v225", "v226", "v227", "v228", "v229", "v230", "v231", "v232", "v233", "v234", "v235", "v236", "v237", "v238", "v239", "v240", "v241", "v242", "v243", "v244", "v245", "v246", "v247", "v248", "v249", "v250", "v251", "v252", "v253", "v254", "v255");
}

extern "C" void kernel_launch(void* const* d_in, const int* in_sizes, int n_in, void* d_out, int out_size, void* d_ws, size_t ws_size, hipStream_t stream) {
  (void)in_sizes; (void)n_in; (void)out_size; (void)ws_size;
  (void)hipFuncSetAttribute((const void*)main_kernel, hipFuncAttributeMaxDynamicSharedMemorySize, MAIN_LDS);
  MainArgs a; a.x = d_in[0]; a.wq = d_in[3]; a.wo = d_in[4]; a.w1 = d_in[6]; a.b1 = d_in[7]; a.w2 = d_in[8]; a.b2 = d_in[9]; a.out = d_out; a.ws = d_ws;
  a.kc = d_in[1]; a.vc = d_in[2];
  hipLaunchKernelGGL(main_kernel, dim3(256), dim3(256), MAIN_LDS, stream, a);
}
```
